```python
import torch

def _softmax(x, axis=-1):
    return torch.softmax(x, dim=axis)

def _gelu(x):
    return torch.nn.functional.gelu(x, approximate='tanh')

def _silu(x):
    return torch.nn.functional.silu(x)

def _sigmoid(x):
    return torch.sigmoid(x)

def _layernorm(x, axis=-1, eps=1e-05):
    m = x.mean(dim=axis, keepdim=True)
    v = x.var(dim=axis, keepdim=True, unbiased=False)
    return (x - m) / torch.sqrt(v + eps)

def _scatter_add(tgt, idx, src, axis=0):
    return tgt.clone().index_add_(axis, idx, src)

def _segment_sum(src, seg, n):
    out = torch.zeros((n,) + src.shape[1:], dtype=src.dtype)
    return out.index_add_(0, seg, src)

def _pad(x, pw):
    flat = tuple((v for pair in reversed(pw) for v in pair))
    return torch.nn.functional.pad(x, flat)
kernel_name = 'adaLN_cond__ssm_conv_s4d__glu_gated_geglu__prompt'

def setup_inputs():

    _w = lambda a: (lambda t: t.to(torch.bfloat16) if t.is_floating_point() else t)(torch.as_tensor(a))

    def _A_setup():
        Bsz, L, D, N = (1, 2048, 512, 128)
        return {'u': _w((lambda *s: torch.randn(*s, dtype=torch.bfloat16) * 0.05)(Bsz, L, D)), 'log_A_real': _w((lambda *s: torch.randn(*s, dtype=torch.bfloat16) * 3.2)(D, N) * 0.5), 'A_imag': _w((lambda *s: torch.randn(*s, dtype=torch.bfloat16) * 0.1)(D, N) * 3.14), 'C_real': _w((lambda *s: torch.randn(*s, dtype=torch.bfloat16) * 0.2)(D, N) / N ** 0.5), 'C_imag': _w((lambda *s: torch.randn(*s, dtype=torch.bfloat16) * 0.2)(D, N) / N ** 0.5), 'log_dt': _w((lambda *s: torch.randn(*s, dtype=torch.bfloat16) * 0.1)(D) * 0.5 - 4.0), 'D_skip': _w((lambda *s: torch.randn(*s, dtype=torch.bfloat16) * 0.1)(D))}
    _A_in = _A_setup()

    def _B_setup():
        B, S, Dm, Df = (1, 2048, 512, 1792)
        return {'x': _w((lambda *s: torch.randn(*s, dtype=torch.bfloat16) * 0.05)(B, S, Dm)), 'w_gate': _w((lambda *s: torch.randn(*s, dtype=torch.bfloat16) * 1.6)(Dm, Df) * Dm ** (-0.5)), 'w_up': _w((lambda *s: torch.randn(*s, dtype=torch.bfloat16) * 1.6)(Dm, Df) * Dm ** (-0.5)), 'w_down': _w((lambda *s: torch.randn(*s, dtype=torch.bfloat16) * 1.6)(Df, Dm) * Df ** (-0.5))}
    _B_in = _B_setup()
    return {'A_u': _A_in['u'], 'A_log_A_real': _A_in['log_A_real'], 'A_A_imag': _A_in['A_imag'], 'A_C_real': _A_in['C_real'], 'A_C_imag': _A_in['C_imag'], 'A_log_dt': _A_in['log_dt'], 'A_D_skip': _A_in['D_skip'], 'B_x': _B_in['x'], 'B_w_gate': _B_in['w_gate'], 'B_w_up': _B_in['w_up'], 'B_w_down': _B_in['w_down'], 'c': _w((lambda *s: torch.randn(*s, dtype=torch.bfloat16) * 0.2)(1, 512) * 8.0), 'w_mod': _w((lambda *s: torch.randn(*s, dtype=torch.bfloat16) * 0.2)(512, 6 * 512) * 512 ** (-0.5) * 8.0)}

def reference(A_u, A_log_A_real, A_A_imag, A_C_real, A_C_imag, A_log_dt, A_D_skip, B_x, B_w_gate, B_w_up, B_w_down, c, w_mod):

    def _A_ref(u, log_A_real, A_imag, C_real, C_imag, log_dt, D_skip):
        L = u.shape[1]
        dt = torch.exp(log_dt)
        A = -torch.exp(log_A_real) + 1j * A_imag
        C = (C_real + 1j * C_imag) * dt[:, None]
        pos = (lambda x, dt: x.to(dt))(torch.arange(L), log_dt.dtype)
        P = torch.exp((A * dt[:, None])[:, :, None] * pos[None, None, :])
        K = 2.0 * torch.einsum('dn,dnl->dl', C, P).real
        L2 = 2 * L
        u_t = (lambda x, *d: x.permute(*d))(u, 0, 2, 1)
        Kf = (lambda x, *a, **k: torch.fft.rfft(x.float(), *a, **k))(K, L2)
        Uf = (lambda x, *a, **k: torch.fft.rfft(x.float(), *a, **k))(u_t, L2)
        y = (lambda x, *a, **k: torch.fft.irfft(x, *a, **k).to(torch.bfloat16))(Uf * Kf[None, :, :], L2)[:, :, :L]
        y = (lambda x, *d: x.permute(*d))(y, 0, 2, 1)
        return y + u * D_skip[None, None, :]

    def _B_ref(x, w_gate, w_up, w_down):
        g = torch.einsum('bsd,df->bsf', x, w_gate)
        u = torch.einsum('bsd,df->bsf', x, w_up)
        h = _gelu(g) * u
        return torch.einsum('bsf,fd->bsd', h, w_down)
    x = A_u
    mod = (_silu(c) @ w_mod).reshape(c.shape[0], 6, 1, -1)
    sa, ca, ga, sb, cb, gb = (mod[:, 0].clone(), mod[:, 1].clone(), mod[:, 2].clone(), mod[:, 3].clone(), mod[:, 4].clone(), mod[:, 5].clone())
    h = _layernorm(x, axis=-1) * (1 + ca) + sa
    x = x + ga * _A_ref(h, A_log_A_real, A_A_imag, A_C_real, A_C_imag, A_log_dt, A_D_skip)
    h = _layernorm(x, axis=-1) * (1 + cb) + sb
    x = x + gb * _B_ref(h, B_w_gate, B_w_up, B_w_down)
    return x
```

```cpp
#include <hip/hip_runtime.h>
#include <hip/hip_bf16.h>
typedef unsigned short u16;
__device__ __forceinline__ float b2f(u16 v){ return __uint_as_float(((unsigned)v)<<16); }
__device__ __forceinline__ u16 f2b(float f){ unsigned u=__float_as_uint(f); return (u16)((u+0x7FFF+((u>>16)&1))>>16); }
__device__ __forceinline__ float rb(float f){ return b2f(f2b(f)); }
constexpr int L=2048, D=512, N=128, F=1792;
__global__ void k_mod(const u16* c, const u16* w, float* mod){
  int j=blockIdx.x*blockDim.x+threadIdx.x; float s=0;
  for(int k=0;k<D;k++){ float x=b2f(c[k]); float sl=rb(x/(1.f+__expf(-x))); s+=sl*b2f(w[k*3072+j]); }
  mod[j]=rb(s);
}
__global__ void k_stats(const u16* x, float* st){
  int l=blockIdx.x*blockDim.x+threadIdx.x; float s=0;
  for(int k=0;k<D;k++) s+=b2f(x[l*D+k]);
  float m=s/D, v=0;
  for(int k=0;k<D;k++){ float t=b2f(x[l*D+k])-m; v+=t*t; }
  v/=D; st[2*l]=rb(m); st[2*l+1]=rb(sqrtf(rb(rb(v)+1e-5f)));
}
__global__ void k_h(const u16* x, const float* st, const float* mod, float* h){
  int i=blockIdx.x*blockDim.x+threadIdx.x; int l=i/D,d=i%D;
  float n=rb(rb(b2f(x[i])-st[2*l])/st[2*l+1]);
  h[i]=rb(rb(n*rb(1.f+mod[512+d]))+mod[d]);
}
__global__ void k_K(const u16* la,const u16* ai,const u16* cr,const u16* ci,const u16* ldt,float* K){
  int i=blockIdx.x*blockDim.x+threadIdx.x; int d=i/L,l=i%L;
  float dt=rb(expf(b2f(ldt[d]))); float pos=rb((float)l); float s=0;
  for(int n=0;n<N;n++){ float re=-rb(expf(b2f(la[d*N+n])))*dt*pos, im=b2f(ai[d*N+n])*dt*pos;
    float e=expf(re); s+= e*(b2f(cr[d*N+n])*dt*cosf(im)-b2f(ci[d*N+n])*dt*sinf(im)); }
  K[l*D+d]=rb(2.f*s);
}
__global__ void k_conv(const u16* x,const float* h,const float* K,const float* mod,const u16* dsk,u16* x1){
  int i=blockIdx.x*blockDim.x+threadIdx.x; int l=i/D,d=i%D; float s=0;
  for(int j=0;j<=l;j++) s+=K[j*D+d]*h[(l-j)*D+d];
  float z=rb(rb(s)+rb(h[i]*b2f(dsk[d])));
  x1[i]=f2b(b2f(x[i])+rb(mod[1024+d]*z));
}
__global__ void k_wprep(const u16* wg,const u16* wu,const float* mod,float* wg2,float* wu2,float* aux){
  int f=blockIdx.x*blockDim.x+threadIdx.x; float cg=0,cu=0,bg=0,bu=0;
  for(int k=0;k<D;k++){ float sc=rb(1.f+mod[4*512+k]), sb=mod[3*512+k];
    float a=b2f(wg[k*F+f]), b=b2f(wu[k*F+f]); float a2=rb(a*sc), b2=rb(b*sc);
    wg2[k*F+f]=a2; wu2[k*F+f]=b2; cg+=a2; cu+=b2; bg+=sb*a; bu+=sb*b; }
  aux[f]=cg; aux[F+f]=cu; aux[2*F+f]=bg; aux[3*F+f]=bu;
}
__global__ void k_g1(const u16* x1,const float* st,const float* wg2,const float* wu2,const float* aux,float* act){
  int f=blockIdx.x*blockDim.x+threadIdx.x; int l=blockIdx.y; float g=0,u=0;
  for(int k=0;k<D;k++){ float a=b2f(x1[l*D+k]); g+=a*wg2[k*F+f]; u+=a*wu2[k*F+f]; }
  float m=st[2*l], r=1.f/st[2*l+1];
  g=(g-m*aux[f])*r+aux[2*F+f]; u=(u-m*aux[F+f])*r+aux[3*F+f];
  float t=0.7978845608f*(g+0.044715f*g*g*g);
  act[l*F+f]=rb(0.5f*g*(1.f+tanhf(t))*u);
}
__global__ void k_g2(const u16* x1,const float* act,const u16* wd,const float* mod,u16* out){
  int d=blockIdx.x*blockDim.x+threadIdx.x; int l=blockIdx.y; float s=0;
  for(int k=0;k<F;k++) s+=act[l*F+k]*b2f(wd[k*D+d]);
  out[l*D+d]=f2b(b2f(x1[l*D+d])+rb(mod[5*512+d]*rb(s)));
}
extern "C" void kernel_launch(void* const* d_in, const int* in_sizes, int n_in, void* d_out, int out_size, void* d_ws, size_t ws_size, hipStream_t stream) {
  const u16* x=(const u16*)d_in[0];
  float* ws=(float*)d_ws;
  float* mod=ws; float* st=ws+4096; float* st2=st+4096; float* aux=st2+4096; float* h=aux+8192; float* K=h+L*D; float* wg2=K+L*D; float* wu2=wg2+D*F; float* act=wu2+D*F; u16* x1=(u16*)(act+L*F);
  k_mod<<<48,64,0,stream>>>((const u16*)d_in[11],(const u16*)d_in[12],mod);
  k_stats<<<32,64,0,stream>>>(x,st);
  k_h<<<L*D/256,256,0,stream>>>(x,st,mod,h);
  k_K<<<L*D/256,256,0,stream>>>((const u16*)d_in[1],(const u16*)d_in[2],(const u16*)d_in[3],(const u16*)d_in[4],(const u16*)d_in[5],K);
  k_conv<<<L*D/256,256,0,stream>>>(x,h,K,mod,(const u16*)d_in[6],x1);
  k_stats<<<32,64,0,stream>>>(x1,st2);
  k_wprep<<<F/64,64,0,stream>>>((const u16*)d_in[8],(const u16*)d_in[9],mod,wg2,wu2,aux);
  k_g1<<<dim3(F/256,L),256,0,stream>>>(x1,st2,wg2,wu2,aux,act);
  k_g2<<<dim3(D/256,L),256,0,stream>>>(x1,act,(const u16*)d_in[10],mod,(u16*)d_out);
}
```

```cpp
#include <hip/hip_runtime.h>
#include <hip/hip_bf16.h>
#include <stdint.h>
#include <type_traits>

typedef unsigned short u16;
typedef unsigned int u32;
typedef __attribute__((ext_vector_type(8))) __bf16 bf16x8;
typedef __attribute__((ext_vector_type(4))) __bf16 bf16x4;
typedef __attribute__((ext_vector_type(2))) __bf16 bf16x2;
typedef __attribute__((ext_vector_type(2))) _Float16 f16x2;
typedef __attribute__((ext_vector_type(8))) _Float16 f16x8;
typedef __attribute__((ext_vector_type(2))) float f32x2;
typedef __attribute__((ext_vector_type(4))) float f32x4;
typedef __attribute__((ext_vector_type(16))) float f32x16;
typedef __attribute__((ext_vector_type(4))) u32 u32x4;
typedef __attribute__((ext_vector_type(2))) u32 u32x2;
typedef __attribute__((address_space(3))) bf16x4* lds_bf16x4_p;
typedef __attribute__((address_space(3))) char* lds_char_p;

#define DEVI __device__ __forceinline__

constexpr int SL = 2048, DM = 512, NS = 128, DF = 1792;
constexpr int NFT = DF / 16;

constexpr size_t OFF_MOD = 0;
constexpr size_t OFF_ST1 = 16384;
constexpr size_t OFF_XT = 65536;
constexpr size_t OFF_KR = OFF_XT + (2u << 20);
constexpr size_t OFF_WD = OFF_KR + (2u << 20);
constexpr size_t OFF_WP = OFF_WD + (2u << 20);
constexpr size_t OFF_X1T = OFF_WP + (4u << 20);
constexpr size_t OFF_ACT = OFF_X1T + (2u << 20);

template <int I, int N, class F>
DEVI void static_for(F&& f) {
  if constexpr (I < N) {
    f(std::integral_constant<int, I>{});
    static_for<I + 1, N>(f);
  }
}

DEVI float bflo(u32 v) { return __uint_as_float(v << 16); }
DEVI float bfhi(u32 v) { return __uint_as_float(v & 0xffff0000u); }
DEVI float b2f(u16 v) { return __uint_as_float(((u32)v) << 16); }
DEVI u32 pack_bf16(float a, float b) {
  f32x2 v = {a, b};
  bf16x2 r = __builtin_convertvector(v, bf16x2);
  return __builtin_bit_cast(u32, r);
}
DEVI float rb(float a) { return bflo(pack_bf16(a, a)); }
DEVI u32 pack_f16(float a, float b) {
  f32x2 v = {a, b};
  f16x2 r = __builtin_convertvector(v, f16x2);
  return __builtin_bit_cast(u32, r);
}
DEVI u32 perm_lo(u32 a, u32 b) { return __builtin_amdgcn_perm(b, a, 0x05040100u); }
DEVI u32 perm_hi(u32 a, u32 b) { return __builtin_amdgcn_perm(b, a, 0x07060302u); }

struct cf { float r, i; };
DEVI cf cmul(cf a, cf b) { return cf{a.r * b.r - a.i * b.i, a.r * b.i + a.i * b.r}; }
DEVI cf cexpk(float zr, float zi, float k) {
  float mag = __builtin_amdgcn_exp2f(zr * (k * 1.4426950408889634f));
  float rev = __builtin_amdgcn_fractf(zi * (k * 0.15915494309189535f));
  return cf{mag * __builtin_amdgcn_cosf(rev), mag * __builtin_amdgcn_sinf(rev)};
}

constexpr int K1_KGEN = 256, K1_LN = 64, K1_MOD = 96, K1_WD = 56;
constexpr int KG_ROWB = 272;
constexpr int KG_WAVEB = 57 * KG_ROWB;
constexpr int KG_KC = 656;
constexpr int K1_SMEM = 4 * KG_WAVEB + 2 * KG_KC * 4;

DEVI void k1_kgen(int bid, char* smem, const u16* __restrict__ la, const u16* __restrict__ ai, const u16* __restrict__ cr,
                  const u16* __restrict__ ci, const u16* __restrict__ ldt, u16* __restrict__ Kr) {
  const int tid = threadIdx.x, w = tid >> 6, lane = tid & 63;
  const int d = 2 * bid + (w >> 1), n = 64 * (w & 1) + lane;
  const float v_la = b2f(la[d * NS + n]), v_ai = b2f(ai[d * NS + n]);
  const float v_cr = b2f(cr[d * NS + n]), v_ci = b2f(ci[d * NS + n]);
  const float v_ldt = b2f(ldt[d]);
  float* Kc = (float*)(smem + 4 * KG_WAVEB);
  for (int i = tid; i < 2 * KG_KC; i += 256) Kc[i] = 0.f;
  const float dt = rb(expf(v_ldt));
  const float zr = -rb(expf(v_la)) * dt, zi = v_ai * dt;
  float Cr = v_cr * dt, Ci = v_ci * dt;
  float mx = fmaxf(fabsf(Cr), fabsf(Ci));
#pragma unroll
  for (int m = 32; m >= 1; m >>= 1) mx = fmaxf(mx, __shfl_xor(mx, m));
  int ex = (int)(__float_as_uint(mx) >> 23);
  ex = min(max(ex, 1), 252);
  const float sdn = __uint_as_float((u32)(254 - ex) << 23), sup = __uint_as_float((u32)(ex + 1) << 23);
  char* base = smem + w * KG_WAVEB + lane * 4;
  auto put = [&](int row, cf v) { *(u32*)(base + row * KG_ROWB) = pack_f16(v.r, v.i); };
  {
    const cf e8 = cexpk(zr, zi, 8.f);
    cf f[16];
    f[0] = cf{1.f, 0.f};
    f[1] = e8;
#pragma unroll
    for (int j = 2; j < 16; ++j) f[j] = (j & 1) ? cmul(f[j - 1], e8) : cmul(f[j / 2], f[j / 2]);
#pragma unroll
    for (int j = 0; j < 16; ++j) put(41 + j, f[j]);
  }
  {
    const cf e1 = cexpk(zr, -zi, 1.f), e128 = cexpk(zr, -zi, 128.f);
    cf u[8];
    u[1] = e1;
#pragma unroll
    for (int j = 2; j < 8; ++j) u[j] = (j & 1) ? cmul(u[j - 1], e1) : cmul(u[j / 2], u[j / 2]);
    cf p[17];
    p[1] = e128;
#pragma unroll
    for (int j = 2; j < 17; ++j) p[j] = (j & 1) ? cmul(p[j - 1], e128) : cmul(p[j / 2], p[j / 2]);
    const cf C = cf{Cr * sdn, -Ci * sdn};
#pragma unroll
    for (int i = 0; i < 17; ++i) {
      const cf g = i ? cmul(C, p[i]) : C;
      if (i < 2) {
        put(8 * i, g);
#pragma unroll
        for (int e = 1; e < 8; ++e) put(8 * i + e, cmul(g, u[e]));
      } else if (i < 4) {
        put(16 + 4 * (i - 2), g);
#pragma unroll
        for (int e = 1; e < 4; ++e) put(16 + 4 * (i - 2) + e, cmul(g, u[2 * e]));
      } else if (i < 8) {
        put(24 + 2 * (i - 4), g);
        put(24 + 2 * (i - 4) + 1, cmul(g, u[4]));
      } else {
        put(32 + (i - 8), g);
      }
    }
  }
  __syncthreads();
  const int r = lane & 15, q = lane >> 4;
  f32x4 acc[3] = {};
  const char* rbase = smem + w * KG_WAVEB + r * KG_ROWB + q * 16;
#pragma unroll
  for (int ks = 0; ks < 4; ++ks) {
    const f16x8 bfr = *(const f16x8*)(rbase + 41 * KG_ROWB + ks * 64);
#pragma unroll
    for (int t = 0; t < 3; ++t) {
      const f16x8 afr = *(const f16x8*)(rbase + 16 * t * KG_ROWB + ks * 64);
      acc[t] = __builtin_amdgcn_mfma_f32_16x16x32_f16(afr, bfr, acc[t], 0, 0, 0);
    }
  }
  float* kc = Kc + (w >> 1) * KG_KC;
#pragma unroll
  for (int e = 0; e < 4; ++e) {
    atomicAdd(kc + 128 * (q >> 1) + 4 * (q & 1) + e + 8 * r, acc[0][e] * sup);
    const int i1 = (q < 2) ? (256 + 64 * q + e + 4 * r) : (384 + 64 * (q - 2) + 32 * (e >> 1) + (e & 1) + 2 * r);
    atomicAdd(kc + i1, acc[1][e] * sup);
    if (4 * q + e <= 8) atomicAdd(kc + 512 + 16 * (4 * q + e) + r, acc[2][e] * sup);
  }
  __syncthreads();
  const int ch = tid >> 7, t = tid & 127;
  const float* kcr = Kc + ch * KG_KC;
#pragma unroll
  for (int i = 0; i < 2; ++i) {
    const int u0 = 8 * (t + 128 * i);
    u32x4 o;
#pragma unroll
    for (int e = 0; e < 4; ++e) {
      const int l0 = 2047 - u0 - 2 * e, l1 = l0 - 1;
      const u32 pb = pack_bf16((float)l0, (float)l1);
      const int i0 = (l0 < 128) ? l0 : (int)(pb & 0xffffu) - 133 * 128;
      const int i1 = (l1 < 128) ? l1 : (int)(pb >> 16) - 133 * 128;
      o[e] = pack_bf16(kcr[i0], kcr[i1]);
    }
    *(u32x4*)(Kr + (size_t)(2 * bid + ch) * SL + u0) = o;
  }
}

DEVI void k1_ln(int bid, const u16* __restrict__ x, u16* __restrict__ xT, f32x2* __restrict__ st1) {
  const int tid = threadIdx.x, w = tid >> 6, lane = tid & 63;
  const int g = 4 * bid + w;
  u32x4 r[8];
#pragma unroll
  for (int i = 0; i < 8; ++i) r[i] = *(const u32x4*)(x + (size_t)(8 * g + i) * DM + 8 * lane);
  const float piv_mine = b2f(x[(size_t)(8 * g + (lane >> 3)) * DM]);
#pragma unroll
  for (int k = 0; k < 8; ++k) {
    u32x4 o;
#pragma unroll
    for (int i = 0; i < 4; ++i)
      o[i] = (k & 1) ? perm_hi(r[2 * i][k >> 1], r[2 * i + 1][k >> 1]) : perm_lo(r[2 * i][k >> 1], r[2 * i + 1][k >> 1]);
    *(u32x4*)(xT + (size_t)g * 4096 + k * 512 + lane * 8) = o;
  }
  float s[8], qq[8];
#pragma unroll
  for (int i = 0; i < 8; ++i) {
    const float piv = bflo((u32)__builtin_amdgcn_readfirstlane((int)r[i][0]));
    float a = 0.f, b = 0.f;
#pragma unroll
    for (int k = 0; k < 4; ++k) {
      const float v0 = bflo(r[i][k]) - piv, v1 = bfhi(r[i][k]) - piv;
      a += v0 + v1;
      b += v0 * v0;
      b += v1 * v1;
    }
    s[i] = a;
    qq[i] = b;
  }
#pragma unroll
  for (int st = 0; st < 3; ++st) {
    const int m = 32 >> st, h = 4 >> st;
    const bool hi = lane & m;
#pragma unroll
    for (int i = 0; i < h; ++i) {
      const float ks = hi ? s[i + h] : s[i], ss = hi ? s[i] : s[i + h];
      const float kq = hi ? qq[i + h] : qq[i], sq = hi ? qq[i] : qq[i + h];
      s[i] = ks + __shfl_xor(ss, m);
      qq[i] = kq + __shfl_xor(sq, m);
    }
  }
#pragma unroll
  for (int m = 4; m >= 1; m >>= 1) {
    s[0] += __shfl_xor(s[0], m);
    qq[0] += __shfl_xor(qq[0], m);
  }
  if ((lane & 7) == 0) {
    const float dm = s[0] * (1.f / DM);
    const float var = fmaxf(qq[0] * (1.f / DM) - dm * dm, 0.f);
    const float sd = rb(sqrtf(rb(rb(var) + 1e-5f)));
    st1[8 * g + (lane >> 3)] = f32x2{rb(piv_mine + dm), 1.f / sd};
  }
}

DEVI void k1_mod(int bid, char* smem, const u16* __restrict__ c, const u16* __restrict__ wm, float* __restrict__ mod) {
  const int tid = threadIdx.x;
  const int cgp = 2 * (bid & 7) + ((bid >> 3) & 1) + 16 * (bid >> 4);
  const int kq = tid >> 2, cg = tid & 3;
  u32x4 wv[8];
  float cv[8];
#pragma unroll
  for (int i = 0; i < 8; ++i) wv[i] = *(const u32x4*)(wm + (size_t)(kq + 64 * i) * 3072 + 32 * cgp + 8 * cg);
#pragma unroll
  for (int i = 0; i < 8; ++i) cv[i] = b2f(c[kq + 64 * i]);
  float acc[8] = {};
#pragma unroll
  for (int i = 0; i < 8; ++i) {
    const float sl = rb(cv[i] / (1.f + __expf(-cv[i])));
#pragma unroll
    for (int k = 0; k < 4; ++k) {
      acc[2 * k] += sl * bflo(wv[i][k]);
      acc[2 * k + 1] += sl * bfhi(wv[i][k]);
    }
  }
  float* red = (float*)smem;
  *(f32x4*)(red + kq * 36 + 8 * cg) = f32x4{acc[0], acc[1], acc[2], acc[3]};
  *(f32x4*)(red + kq * 36 + 8 * cg + 4) = f32x4{acc[4], acc[5], acc[6], acc[7]};
  __syncthreads();
  const int col = tid >> 3, part = tid & 7;
  float s = 0.f;
#pragma unroll
  for (int i = 0; i < 8; ++i) s += red[(part + 8 * i) * 36 + col];
#pragma unroll
  for (int m = 4; m >= 1; m >>= 1) s += __shfl_xor(s, m);
  if (part == 0) mod[32 * cgp + col] = rb(s);
}

DEVI void k1_wd(int bid, const u16* __restrict__ wd, u16* __restrict__ wdt) {
  const int tid = threadIdx.x, w = tid >> 6, lane = tid & 63;
  const int fg = 4 * bid + w;
  u32x4 r[8];
#pragma unroll
  for (int i = 0; i < 8; ++i) r[i] = *(const u32x4*)(wd + (size_t)(8 * fg + i) * DM + 8 * lane);
  u16* dst = wdt + (size_t)(lane >> 2) * (NFT * 512) + fg * 256 + (lane & 3) * 64;
#pragma unroll
  for (int k = 0; k < 8; ++k) {
    u32x4 o;
#pragma unroll
    for (int i = 0; i < 4; ++i)
      o[i] = (k & 1) ? perm_hi(r[2 * i][k >> 1], r[2 * i + 1][k >> 1]) : perm_lo(r[2 * i][k >> 1], r[2 * i + 1][k >> 1]);
    *(u32x4*)(dst + k * 8) = o;
  }
}

__global__ __launch_bounds__(256) void k1_prep(const u16* __restrict__ x, const u16* __restrict__ la, const u16* __restrict__ ai,
                                               const u16* __restrict__ cr, const u16* __restrict__ ci,
                                               const u16* __restrict__ ldt, const u16* __restrict__ wd,
                                               const u16* __restrict__ c, const u16* __restrict__ wm, char* __restrict__ ws) {
  __shared__ __attribute__((aligned(16))) char smem[K1_SMEM];
  int b = blockIdx.x;
  if (b < K1_KGEN) {
    k1_kgen(b, smem, la, ai, cr, ci, ldt, (u16*)(ws + OFF_KR));
  } else if ((b -= K1_KGEN) < K1_MOD) {
    k1_mod(b, smem, c, wm, (float*)(ws + OFF_MOD));
  } else if ((b -= K1_MOD) < K1_LN) {
    k1_ln(b, x, (u16*)(ws + OFF_XT), (f32x2*)(ws + OFF_ST1));
  } else {
    k1_wd(b - K1_LN, wd, (u16*)(ws + OFF_WD));
  }
}

constexpr int CV_KCOPY = 4160;
constexpr int CV_K = 0;
constexpr int CV_H = 4 * CV_KCOPY;
constexpr int CV_HB = 2288 * 2;
constexpr int CV_X = CV_H + 2 * CV_HB;
constexpr int CV_RED = CV_X + 2 * 4096;
constexpr int K2_SMEM = CV_RED + 16384;

DEVI void k2_conv_role(int b, char* smem, const u16* __restrict__ dsk, char* __restrict__ ws) {
  const int tid = threadIdx.x, lane = tid & 63;
  const int w = __builtin_amdgcn_readfirstlane(tid >> 6);
  const u16* xT = (const u16*)(ws + OFF_XT);
  const u16* Kr = (const u16*)(ws + OFF_KR);
  const float* mod = (const float*)(ws + OFF_MOD);
  const f32x4* st1 = (const f32x4*)(ws + OFF_ST1);
  u16* x1T = (u16*)(ws + OFF_X1T);
  const int pi = 32 * (b >> 5) + 4 * (b & 7) + ((b >> 3) & 3);
  const int slot0 = 2 * pi, c0 = 8 * (slot0 & 63) + (slot0 >> 6);
  u32x4 xv[2], kv[2];
  u32 kn[2];
  f32x4 st[4];
#pragma unroll
  for (int c = 0; c < 2; ++c) {
    kv[c] = *(const u32x4*)(Kr + (size_t)(c0 + 8 * c) * SL + 8 * tid);
    kn[c] = *(const u32*)(Kr + (size_t)(c0 + 8 * c) * SL + min(8 * tid + 8, SL - 2));
  }
#pragma unroll
  for (int c = 0; c < 2; ++c) xv[c] = *(const u32x4*)(xT + (size_t)tid * 4096 + (slot0 + c) * 8);
#pragma unroll
  for (int i = 0; i < 4; ++i) st[i] = st1[4 * tid + i];
  if (tid < 60) {
    *(u32x4*)(smem + CV_H + (tid / 30) * CV_HB + (tid % 30) * 16) = u32x4{0, 0, 0, 0};
  } else if (tid >= 64 && tid < 72) {
    *(u32x4*)(smem + CV_K + ((tid - 64) >> 1) * CV_KCOPY + 4096 + (tid & 1) * 16) = u32x4{0, 0, 0, 0};
  }
#pragma unroll
  for (int c = 0; c < 2; ++c) {
    const u32 nx = (tid == 255) ? 0u : kn[c];
    u32x4 sh;
    sh[0] = __builtin_amdgcn_alignbit(kv[c][1], kv[c][0], 16);
    sh[1] = __builtin_amdgcn_alignbit(kv[c][2], kv[c][1], 16);
    sh[2] = __builtin_amdgcn_alignbit(kv[c][3], kv[c][2], 16);
    sh[3] = __builtin_amdgcn_alignbit(nx, kv[c][3], 16);
    *(u32x4*)(smem + CV_K + (2 * c) * CV_KCOPY + 16 * tid) = kv[c];
    *(u32x4*)(smem + CV_K + (2 * c + 1) * CV_KCOPY + 16 * tid) = sh;
  }
#pragma unroll
  for (int c = 0; c < 2; ++c) {
    const float sa = mod[c0 + 8 * c], ca1 = rb(1.f + mod[DM + c0 + 8 * c]);
    u32x4 hv;
#pragma unroll
    for (int i = 0; i < 4; ++i) {
      const u32 dd = pack_bf16(bflo(xv[c][i]) - st[i][0], bfhi(xv[c][i]) - st[i][2]);
      const u32 nn = pack_bf16(bflo(dd) * st[i][1], bfhi(dd) * st[i][3]);
      const u32 mm = pack_bf16(bflo(nn) * ca1, bfhi(nn) * ca1);
      hv[i] = pack_bf16(bflo(mm) + sa, bfhi(mm) + sa);
    }
    *(u32x4*)(smem + CV_H + c * CV_HB + 480 + 16 * tid) = hv;
    *(u32x4*)(smem + CV_X + c * 4096 + 16 * tid) = xv[c];
  }
  __syncthreads();

  const int ch = w >> 1, half = w & 1;
  const int r = lane & 15, q = lane >> 4;
  const char* abase = smem + CV_K + (2 * ch + ((r & 1) ^ 1)) * CV_KCOPY + 2 * (2030 - (r & ~1) + 8 * q) - 64 * (4 * half + 3 + 56);
  const char* bbase = smem + CV_H + ch * CV_HB + 2 * (224 + 16 * r + 8 * q) - 64 * (4 * half + 3);
  f32x4 acc[8] = {};
#pragma unroll
  for (int rho = 0; rho < 4; ++rho) {
    bf16x8 af[8], bfr[8];
#pragma unroll
    for (int i = 0; i < 8; ++i) {
      const u32* p = (const u32*)(abase + 64 * (3 - rho) + 512 * (7 - i));
      u32x4 v = {p[0], p[1], p[2], p[3]};
      af[i] = __builtin_bit_cast(bf16x8, v);
      bfr[i] = *(const bf16x8*)(bbase + 64 * (3 - rho) + 512 * i);
    }
#pragma unroll
    for (int m = 0; m < 8; ++m) {
#pragma unroll
      for (int o = 0; o <= m; ++o) {
        if (m + o < 8) {
          acc[m + o] = __builtin_amdgcn_mfma_f32_16x16x32_bf16(af[m], bfr[o], acc[m + o], 0, 0, 0);
          if (o != m) acc[m + o] = __builtin_amdgcn_mfma_f32_16x16x32_bf16(af[o], bfr[m], acc[m + o], 0, 0, 0);
        }
      }
    }
  }
  f32x4* red = (f32x4*)(smem + CV_RED);
  f32x4 mine[4];
#pragma unroll
  for (int i = 0; i < 4; ++i) {
    red[(w * 4 + i) * 64 + lane] = half ? acc[i] : acc[4 + i];
    mine[i] = half ? acc[4 + i] : acc[i];
  }
  __syncthreads();
  const int c = c0 + 8 * ch;
  const float ga = mod[2 * DM + c], dk = b2f(dsk[c]);
#pragma unroll
  for (int i = 0; i < 4; ++i) {
    const f32x4 y = mine[i] + red[((w ^ 1) * 4 + i) * 64 + lane];
    const int l0 = 256 * (4 * half + i) + 16 * r + 4 * q;
    const u32x2 hv = *(const u32x2*)(smem + CV_H + ch * CV_HB + 480 + 2 * l0);
    const u32x2 xx = *(const u32x2*)(smem + CV_X + ch * 4096 + 2 * l0);
    u32x2 o;
#pragma unroll
    for (int e = 0; e < 2; ++e) {
      const u32 yb = pack_bf16(y[2 * e], y[2 * e + 1]);
      const u32 hd = pack_bf16(bflo(hv[e]) * dk, bfhi(hv[e]) * dk);
      const u32 z = pack_bf16(bflo(yb) + bflo(hd), bfhi(yb) + bfhi(hd));
      const u32 gz = pack_bf16(ga * bflo(z), ga * bfhi(z));
      o[e] = pack_bf16(bflo(xx[e]) + bflo(gz), bfhi(xx[e]) + bfhi(gz));
    }
    *(u32x2*)(x1T + (size_t)c * SL + l0) = o;
  }
}

DEVI void k2_wprep_role(int b, char* smem, const u16* __restrict__ wg, const u16* __restrict__ wu, char* __restrict__ ws) {
  const int tid = threadIdx.x, lane = tid & 63, w = tid >> 6;
  const float* mod = (const float*)(ws + OFF_MOD);
  u16* Wp = (u16*)(ws + OFF_WP);
  const int ft = (b < 96) ? (4 * (b & 7) + ((b >> 3) & 3) + 32 * (b >> 5)) : (96 + 2 * (b & 7) + ((b >> 3) & 1));
  const int type = tid >> 7, kg = (tid >> 1) & 63, nh = tid & 1;
  const u16* src = (type ? wu : wg) + (size_t)(8 * kg) * DF + 16 * ft + 8 * nh;
  u32x4 r[8];
#pragma unroll
  for (int i = 0; i < 8; ++i) r[i] = *(const u32x4*)(src + (size_t)i * DF);
  float sc[8], sb[8];
  {
    const f32x4 a = *(const f32x4*)(mod + 4 * DM + 8 * kg), bq = *(const f32x4*)(mod + 4 * DM + 8 * kg + 4);
    const f32x4 c = *(const f32x4*)(mod + 3 * DM + 8 * kg), d = *(const f32x4*)(mod + 3 * DM + 8 * kg + 4);
#pragma unroll
    for (int i = 0; i < 4; ++i) {
      sc[i] = rb(1.f + a[i]);
      sc[4 + i] = rb(1.f + bq[i]);
      sb[i] = c[i];
      sb[4 + i] = d[i];
    }
  }
  float v[16];
  u16* dst = Wp + ((size_t)(ft * 33 + (kg >> 1)) * 64 + 32 * (kg & 1) + 16 * type + 4 * nh) * 8;
#pragma unroll
  for (int n = 0; n < 8; ++n) {
    u32x4 o;
    float cs = 0.f, bs = 0.f;
#pragma unroll
    for (int i = 0; i < 4; ++i) {
      const float w0 = (n & 1) ? bfhi(r[2 * i][n >> 1]) : bflo(r[2 * i][n >> 1]);
      const float w1 = (n & 1) ? bfhi(r[2 * i + 1][n >> 1]) : bflo(r[2 * i + 1][n >> 1]);
      o[i] = pack_bf16(w0 * sc[2 * i], w1 * sc[2 * i + 1]);
      cs += bflo(o[i]) + bfhi(o[i]);
      bs += sb[2 * i] * w0;
      bs += sb[2 * i + 1] * w1;
    }
    v[n] = cs;
    v[8 + n] = bs;
    *(u32x4*)(dst + ((n & 3) + 8 * (n >> 2)) * 8) = o;
  }
#pragma unroll
  for (int st = 0; st < 4; ++st) {
    const int m = 32 >> st, h = 8 >> st;
    const bool hi = lane & m;
#pragma unroll
    for (int i = 0; i < h; ++i) {
      const float kp = hi ? v[i + h] : v[i], sn = hi ? v[i] : v[i + h];
      v[i] = kp + __shfl_xor(sn, m);
    }
  }
  v[0] += __shfl_xor(v[0], 2);
  float* red = (float*)smem;
  if ((lane & 2) == 0) red[(w * 2 + nh) * 16 + (lane >> 2)] = v[0];
  __syncthreads();
  if (tid < 64) {
    u32x4 o = {0, 0, 0, 0};
    if (tid < 32) {
      const int ty = tid >> 4, rp = tid & 15;
      const int f = (rp & 3) + 4 * (rp >> 3) + 8 * ((rp >> 2) & 1);
      const int i0 = ((2 * ty) * 2 + (f >> 3)) * 16 + (f & 7), i1 = i0 + 32;
      const float cs = red[i0] + red[i1], bs = red[i0 + 8] + red[i1 + 8];
      const float c0 = rb(cs), c1 = rb(cs - c0), c2 = rb(cs - c0 - c1);
      const float b0 = rb(bs), b1 = rb(bs - b0), b2 = rb(bs - b0 - b1);
      o[0] = pack_bf16(c0, c1);
      o[1] = pack_bf16(c2, b0);
      o[2] = pack_bf16(b1, b2);
    }
    *(u32x4*)(Wp + ((size_t)(ft * 33 + 32) * 64 + tid) * 8) = o;
  }
}

__global__ __launch_bounds__(256) void k2_conv(const u16* __restrict__ dsk, const u16* __restrict__ wg, const u16* __restrict__ wu,
                                               char* __restrict__ ws) {
  __shared__ __attribute__((aligned(16))) char smem[K2_SMEM];
  const int b = blockIdx.x;
  if (b < 256) k2_conv_role(b, smem, dsk, ws);
  else k2_wprep_role(b - 256, smem, wg, wu, ws);
}

constexpr int K3_SF = 131072;
constexpr int K3_RS = K3_SF + 2048;
constexpr int K3_SMEM = K3_RS + 512;
constexpr int K3_PD = 6;

constexpr int k3_issued_before_step(int ks) {
  int n = 4 + K3_PD;
  for (int s = 0; s < ks; ++s) {
    if (s % 4 == 0 && s / 4 + 2 < 8) n += 2;
    if (s + K3_PD <= 32) n += 1;
  }
  return n;
}
constexpr int k3_pos_w(int j) {
  if (j < 4) return 2 + j;
  if (j < K3_PD) return 4 + j;
  return k3_issued_before_step(j - K3_PD + 1) - 1;
}
constexpr int k3_pos_g(int c) {
  if (c == 0) return 1;
  if (c == 1) return 7;
  return k3_issued_before_step(4 * (c - 2)) + 1;
}

DEVI void glds16(u32 voff, const void* sbase, u32 ldsaddr) {
  asm volatile("s_mov_b32 m0, %2\n\ts_nop 0\n\tglobal_load_lds_dwordx4 %0, %1" ::"v"(voff), "s"(sbase), "s"(ldsaddr) : "memory");
}
template <int N>
DEVI void wait_vm() { asm volatile("s_waitcnt vmcnt(%0)" ::"n"(N) : "memory"); }
template <int N>
DEVI void wait_vm_reg(u32x4& r) { asm volatile("s_waitcnt vmcnt(%1)" : "+v"(r) : "n"(N)); }
template <int OFF>
DEVI void gload16(u32x4& r, u32 voff, const void* sbase) {
  asm volatile("global_load_dwordx4 %0, %1, %2 offset:%3" : "=v"(r) : "v"(voff), "s"(sbase), "n"(OFF) : "memory");
}

DEVI bf16x8 tr_frag(const char* p0) {
  const bf16x4 a = __builtin_amdgcn_ds_read_tr16_b64_v4bf16((lds_bf16x4_p)(p0));
  const bf16x4 b = __builtin_amdgcn_ds_read_tr16_b64_v4bf16((lds_bf16x4_p)(p0 + 1024));
  return __builtin_shufflevector(a, b, 0, 1, 2, 3, 4, 5, 6, 7);
}

DEVI float gelu_mul(float g, float u) {
  const float k0 = -2.f * 0.7978845608028654f * 1.4426950408889634f, k1 = k0 * 0.044715f;
  const float e = __builtin_amdgcn_exp2f(g * (k0 + k1 * g * g));
  return g * u * __builtin_amdgcn_rcpf(1.f + e);
}

__global__ __launch_bounds__(512, 2) void k3_up(char* __restrict__ ws) {
  extern __shared__ __attribute__((aligned(16))) char smem[];
  const int tid = threadIdx.x, lane = tid & 63;
  const int w = __builtin_amdgcn_readfirstlane(tid >> 6);
  const int xcd = blockIdx.x & 7, bi = blockIdx.x >> 3;
  const int mt = 4 * (xcd & 3) + (bi & 3), fb = 8 * (xcd >> 2) + (bi >> 2);
  const char* x1T = ws + OFF_X1T;
  const u32 lds0 = (u32)(uintptr_t)(lds_char_p)smem;
  const u32 goff = (u32)((8 * w + (lane >> 4)) * 4096 + mt * 256 + 16 * ((lane & 15) ^ (4 * (lane >> 4)) ^ (2 * (w & 1))));
  auto G = [&](int c) {
    glds16(goff, x1T + (size_t)c * 262144, lds0 + (64 * c + 8 * w) * 256);
    glds16(goff, x1T + (size_t)c * 262144 + 16384, lds0 + (64 * c + 8 * w + 4) * 256);
  };
  const int q = (lane & 15) >> 2, p = lane & 3;

  if (w < 7) {
    const int ft = 7 * fb + w;
    const char* wsrc = ws + OFF_WP + (size_t)ft * 33 * 1024;
    const u32 woff = lane * 16;
    u32x4 wf[33];
    auto W = [&](auto J) {
      constexpr int j = decltype(J)::value;
      gload16<(j & 3) * 1024>(wf[j], woff, wsrc + (j >> 2) * 4096);
    };
    const int hh = lane >> 5, blk = (lane >> 4) & 1;
    const char* xb[4];
#pragma unroll
    for (int tt = 0; tt < 4; ++tt) xb[tt] = smem + (8 * hh + q) * 256 + 64 * (tt ^ q) + 32 * (blk ^ hh) + 8 * p;
    G(0);
    static_for<0, 4>(W);
    G(1);
    static_for<4, K3_PD>(W);
    f32x16 acc[4] = {};
    bf16x8 xf[2][3];
    wait_vm<k3_issued_before_step(0) - 1 - k3_pos_g(0)>();
    __builtin_amdgcn_s_barrier();
#pragma unroll
    for (int tt = 0; tt < 3; ++tt) xf[0][tt] = tr_frag(xb[tt]);
    static_for<0, 32>([&](auto KS) {
      constexpr int ks = decltype(KS)::value;
      if constexpr (ks % 4 == 0 && ks / 4 + 2 < 8) G(ks / 4 + 2);
      if constexpr (ks + K3_PD <= 32) W(std::integral_constant<int, ks + K3_PD>{});
      constexpr int issued = k3_issued_before_step(ks + 1);
      if constexpr (ks < 31) {
        if constexpr ((ks + 1) % 4 == 0) {
          wait_vm<issued - 1 - k3_pos_g((ks + 1) / 4)>();
          __builtin_amdgcn_s_barrier();
        }
#pragma unroll
        for (int tt = 0; tt < 3; ++tt) xf[(ks + 1) & 1][tt] = tr_frag(xb[tt] + (ks + 1) * 4096);
      }
      wait_vm_reg<issued - 1 - k3_pos_w(ks)>(wf[ks]);
      const bf16x8 a = __builtin_bit_cast(bf16x8, wf[ks]);
#pragma unroll
      for (int tt = 0; tt < 3; ++tt) acc[tt] = __builtin_amdgcn_mfma_f32_32x32x16_bf16(a, xf[ks & 1][tt], acc[tt], 0, 0, 0);
    });
    asm volatile("s_waitcnt vmcnt(0)" : "+v"(wf[32])::"memory");
    __builtin_amdgcn_s_barrier();
    asm volatile("" ::: "memory");
    const bf16x8 aux = __builtin_bit_cast(bf16x8, wf[32]);
    float rs[4];
#pragma unroll
    for (int tt = 0; tt < 3; ++tt) {
      const bf16x8 sf = *(const bf16x8*)(smem + K3_SF + tt * 512 + (lane & 31) * 16);
      rs[tt] = *(const float*)(smem + K3_RS + (32 * tt + (lane & 31)) * 4);
      acc[tt] = __builtin_amdgcn_mfma_f32_32x32x16_bf16(aux, sf, acc[tt], 0, 0, 0);
    }
    char* actp = ws + OFF_ACT + ((size_t)(4 * mt) * NFT + ft) * 1024 + lane * 16;
    u32x4 o;
    auto epi = [&](int tt, int i) {
      const float a0 = gelu_mul(acc[tt][2 * i] * rs[tt], acc[tt][8 + 2 * i] * rs[tt]);
      const float a1 = gelu_mul(acc[tt][2 * i + 1] * rs[tt], acc[tt][9 + 2 * i] * rs[tt]);
      o[i] = pack_bf16(a0, a1);
      if (i == 3) *(u32x4*)(actp + (size_t)tt * NFT * 1024) = o;
    };
    static_for<0, 32>([&](auto KS) {
      constexpr int ks = decltype(KS)::value;
      const bf16x8 b = tr_frag(xb[3] + ks * 4096);
      acc[3] = __builtin_amdgcn_mfma_f32_32x32x16_bf16(__builtin_bit_cast(bf16x8, wf[ks]), b, acc[3], 0, 0, 0);
      if constexpr (ks >= 4 && ks < 28 && (ks & 1) == 0) epi((ks - 4) / 8, ((ks - 4) / 2) & 3);
    });
    asm volatile("" ::: "memory");
    __builtin_amdgcn_s_barrier();
    asm volatile("" ::: "memory");
    {
      const bf16x8 sf = *(const bf16x8*)(smem + K3_SF + 3 * 512 + (lane & 31) * 16);
      rs[3] = *(const float*)(smem + K3_RS + (96 + (lane & 31)) * 4);
      acc[3] = __builtin_amdgcn_mfma_f32_32x32x16_bf16(aux, sf, acc[3], 0, 0, 0);
    }
#pragma unroll
    for (int i = 0; i < 4; ++i) epi(3, i);
  } else {
    const int g = lane >> 4;
    const char* sb[8];
#pragma unroll
    for (int c = 0; c < 8; ++c) sb[c] = smem + (8 * g + q) * 256 + 64 * ((c >> 1) ^ q) + 32 * ((c & 1) ^ (g & 1)) + 8 * p;
    const u32x4 onesu = {0x3f803f80u, 0x3f803f80u, 0x3f803f80u, 0x3f803f80u};
    const bf16x8 ones = __builtin_bit_cast(bf16x8, onesu);
    f32x4 aq[8] = {}, as[8] = {};
    auto fin = [&](int c) {
      const int e = lane & 3;
      const float sq = e == 0 ? aq[c][0] : e == 1 ? aq[c][1] : e == 2 ? aq[c][2] : aq[c][3];
      const float mean = as[c][0] * (1.f / DM);
      const float var = fmaxf(sq * (1.f / DM) - mean * mean, 0.f);
      const float sd = rb(sqrtf(rb(rb(var) + 1e-5f)));
      if (g == q) {
        const int tok = 16 * c + (lane & 15);
        const u32 mm = pack_bf16(-mean, -mean), ms = pack_bf16(-mean, sd), ss = pack_bf16(sd, sd);
        *(u32x4*)(smem + K3_SF + tok * 16) = u32x4{mm, ms, ss, 0};
        *(float*)(smem + K3_RS + tok * 4) = 1.f / sd;
      }
    };
    G(0);
    G(1);
    static_for<0, 8>([&](auto C) {
      constexpr int c = decltype(C)::value;
      wait_vm<(c < 7) ? 2 : 0>();
      __builtin_amdgcn_s_barrier();
      if constexpr (c + 2 < 8) G(c + 2);
#pragma unroll
      for (int k2 = 0; k2 < 2; ++k2)
#pragma unroll
        for (int t = 0; t < 6; ++t) {
          const bf16x8 f = tr_frag(sb[t] + (2 * c + k2) * 8192);
          aq[t] = __builtin_amdgcn_mfma_f32_16x16x32_bf16(f, f, aq[t], 0, 0, 0);
          as[t] = __builtin_amdgcn_mfma_f32_16x16x32_bf16(ones, f, as[t], 0, 0, 0);
        }
    });
#pragma unroll
    for (int t = 0; t < 6; ++t) fin(t);
    asm volatile("s_waitcnt lgkmcnt(0)" ::: "memory");
    __builtin_amdgcn_s_barrier();
    asm volatile("" ::: "memory");
#pragma unroll
    for (int k2 = 0; k2 < 16; ++k2)
#pragma unroll
      for (int t = 6; t < 8; ++t) {
        const bf16x8 f = tr_frag(sb[t] + k2 * 8192);
        aq[t] = __builtin_amdgcn_mfma_f32_16x16x32_bf16(f, f, aq[t], 0, 0, 0);
        as[t] = __builtin_amdgcn_mfma_f32_16x16x32_bf16(ones, f, as[t], 0, 0, 0);
      }
    fin(6);
    fin(7);
    asm volatile("s_waitcnt lgkmcnt(0)" ::: "memory");
    __builtin_amdgcn_s_barrier();
  }
}

constexpr int K4_PD = 8;

__global__ __launch_bounds__(256, 1) void k4_down(const char* __restrict__ ws, u16* __restrict__ out) {
  __shared__ __attribute__((aligned(16))) f32x4 red[4 * 3 * 4 * 64];
  const int tid = threadIdx.x, lane = tid & 63;
  const int w = __builtin_amdgcn_readfirstlane(tid >> 6);
  const int xcd = blockIdx.x & 7, bi = blockIdx.x >> 3;
  const int mt = 8 * (xcd & 3) + (bi & 7), nt = 4 * (xcd >> 2) + (bi >> 3);
  const char* ap = ws + OFF_WD + ((size_t)(2 * nt) * NFT + 28 * w) * 1024 + lane * 16;
  const char* bp = ws + OFF_ACT + ((size_t)(2 * mt) * NFT + 28 * w) * 1024 + lane * 16;
  bf16x8 fa[28][2], fb[28][2];
  auto ld = [&](int ks) {
#pragma unroll
    for (int i = 0; i < 2; ++i) {
      fa[ks][i] = *(const bf16x8*)(ap + (size_t)i * NFT * 1024 + ks * 1024);
      fb[ks][i] = *(const bf16x8*)(bp + (size_t)i * NFT * 1024 + ks * 1024);
    }
  };
#pragma unroll
  for (int ks = 0; ks < K4_PD; ++ks) ld(ks);
  const int qi = w >> 1, qj = w & 1, hh = lane >> 5;
  const int tok = 64 * mt + 32 * qj + (lane & 31), cb = 64 * nt + 32 * qi + 4 * hh;
  const u16* x1T = (const u16*)(ws + OFF_X1T);
  const float* mod = (const float*)(ws + OFF_MOD);
  u16 xr[16];
  f32x4 gb[4];
#pragma unroll
  for (int g = 0; g < 4; ++g) {
    gb[g] = *(const f32x4*)(mod + 5 * DM + cb + 8 * g);
#pragma unroll
    for (int e = 0; e < 4; ++e) xr[4 * g + e] = x1T[(size_t)(cb + 8 * g + e) * SL + tok];
  }
  __builtin_amdgcn_sched_barrier(0);
  f32x16 acc[4] = {};
#pragma unroll
  for (int ks = 0; ks < 28; ++ks) {
    if (ks + K4_PD < 28) ld(ks + K4_PD);
#pragma unroll
    for (int i = 0; i < 2; ++i)
#pragma unroll
      for (int j = 0; j < 2; ++j)
        acc[2 * i + j] = __builtin_amdgcn_mfma_f32_32x32x16_bf16(fa[ks][i], fb[ks][j], acc[2 * i + j], 0, 0, 0);
    __builtin_amdgcn_sched_barrier(0);
  }
  f32x16 own;
#pragma unroll
  for (int qd = 0; qd < 4; ++qd) {
    if (w == qd) {
      own = acc[qd];
    } else {
      const int slot = (w - qd - 1) & 3;
#pragma unroll
      for (int g = 0; g < 4; ++g)
        red[((qd * 3 + slot) * 4 + g) * 64 + lane] = f32x4{acc[qd][4 * g], acc[qd][4 * g + 1], acc[qd][4 * g + 2], acc[qd][4 * g + 3]};
    }
  }
  __syncthreads();
#pragma unroll
  for (int g = 0; g < 4; ++g) {
    f32x4 s = {own[4 * g], own[4 * g + 1], own[4 * g + 2], own[4 * g + 3]};
#pragma unroll
    for (int sl = 0; sl < 3; ++sl) s += red[((w * 3 + sl) * 4 + g) * 64 + lane];
    u32x2 o;
#pragma unroll
    for (int e = 0; e < 2; ++e) {
      const u32 ob = pack_bf16(s[2 * e], s[2 * e + 1]);
      const u32 gz = pack_bf16(gb[g][2 * e] * bflo(ob), gb[g][2 * e + 1] * bfhi(ob));
      o[e] = pack_bf16(b2f(xr[4 * g + 2 * e]) + bflo(gz), b2f(xr[4 * g + 2 * e + 1]) + bfhi(gz));
    }
    *(u32x2*)(out + (size_t)tok * DM + cb + 8 * g) = o;
  }
}

extern "C" void kernel_launch(void* const* d_in, const int* in_sizes, int n_in, void* d_out, int out_size, void* d_ws,
                              size_t ws_size, hipStream_t stream) {
  const u16* x = (const u16*)d_in[0];
  char* ws = (char*)d_ws;
  (void)hipFuncSetAttribute((const void*)k3_up, hipFuncAttributeMaxDynamicSharedMemorySize, K3_SMEM);
  k1_prep<<<K1_KGEN + K1_MOD + K1_LN + K1_WD, 256, 0, stream>>>(x, (const u16*)d_in[1], (const u16*)d_in[2], (const u16*)d_in[3],
                                                               (const u16*)d_in[4], (const u16*)d_in[5], (const u16*)d_in[10],
                                                               (const u16*)d_in[11], (const u16*)d_in[12], ws);
  k2_conv<<<256 + NFT, 256, 0, stream>>>((const u16*)d_in[6], (const u16*)d_in[8], (const u16*)d_in[9], ws);
  k3_up<<<256, 512, K3_SMEM, stream>>>(ws);
  k4_down<<<256, 256, 0, stream>>>(ws, (u16*)d_out);
}
```

```cpp
#include <hip/hip_runtime.h>
#include <hip/hip_bf16.h>
#include <stdint.h>
#include <type_traits>

typedef unsigned short u16;
typedef unsigned int u32;
typedef unsigned long long u64;
typedef __attribute__((ext_vector_type(8))) __bf16 bf16x8;
typedef __attribute__((ext_vector_type(4))) __bf16 bf16x4;
typedef __attribute__((ext_vector_type(2))) __bf16 bf16x2;
typedef __attribute__((ext_vector_type(2))) _Float16 f16x2;
typedef __attribute__((ext_vector_type(8))) _Float16 f16x8;
typedef __attribute__((ext_vector_type(2))) float f32x2;
typedef __attribute__((ext_vector_type(4))) float f32x4;
typedef __attribute__((ext_vector_type(16))) float f32x16;
typedef __attribute__((ext_vector_type(4))) u32 u32x4;
typedef __attribute__((ext_vector_type(2))) u32 u32x2;
typedef __attribute__((address_space(3))) bf16x4* lds_bf16x4_p;
typedef __attribute__((address_space(3))) char* lds_char_p;
typedef __attribute__((address_space(3))) u32* lds_u32_p;
typedef __amdgpu_buffer_rsrc_t rsrc_t;

#define DEVI __device__ __forceinline__

constexpr int SL = 2048, DM = 512, NS = 128, DF = 1792;
constexpr int NFT = DF / 16;
constexpr int SPIN_CAP = 1 << 15;
constexpr int CODE_BYTES = 38 * 1024;

constexpr u32 OFF_E = 0;
constexpr u32 OFF_GST = 4096;
constexpr u32 OFF_FX = 32768;
constexpr u32 OFF_FW = 81920;
constexpr u32 OFF_FA = 131072;
constexpr u32 OFF_GMA = 196608;
constexpr u32 OFF_GMB = OFF_GMA + 131072;
constexpr u32 OFF_GS2 = OFF_GMB + 131072;
constexpr u32 OFF_AUX = OFF_GS2 + 131072;
static_assert(OFF_AUX + 112 * 4096 <= (1u << 20), "control block");
constexpr u32 OFF_WD = 1u << 20;
constexpr u32 OFF_WP = OFF_WD + (2u << 20);
constexpr u32 OFF_X1 = OFF_WP + (4u << 20);
constexpr u32 OFF_ACT = OFF_X1 + (2u << 20);
constexpr u32 OFF_XG = OFF_ACT + (8u << 20);
constexpr u32 OFF_XS = OFF_XG + (4u << 20);
constexpr u32 WS_USED = OFF_XS + (2u << 20);

constexpr int KG_ROWB = 272;
constexpr int KG_WAVEB = 57 * KG_ROWB;
constexpr int L_KC = 4 * KG_WAVEB;
constexpr int CV_KCOPY = 4160;
constexpr int CV_K = 73728;
constexpr int CV_H = CV_K + 4 * CV_KCOPY;
constexpr int CV_HB = 2288 * 2;
constexpr int CV_X = CV_H + 2 * CV_HB;
constexpr int CV_P = 0;
constexpr int L_MD = CV_X + 2 * 4096;
static_assert(L_KC + 4 * 656 * 4 <= CV_K && L_MD + 512 <= 131072, "front LDS map");
constexpr int L_TR = 131072;
constexpr int L_RES = 131072;
constexpr int L_SPARE = L_RES + 8192;
constexpr int L_SF = L_SPARE + 16384;
constexpr int L_RS = L_SF + 2048;
constexpr int L_GB = L_RS + 2048;
constexpr int L_SYNC = L_GB + 256;
constexpr int SMEM = L_SYNC + 128;

#define DIAG_DECL
#define STAMP(i)
#define DIAG_END(wsp)

template <int I, int N, class F>
DEVI void static_for(F&& f) {
  if constexpr (I < N) {
    f(std::integral_constant<int, I>{});
    static_for<I + 1, N>(f);
  }
}

DEVI float bflo(u32 v) { return __uint_as_float(v << 16); }
DEVI float bfhi(u32 v) { return __uint_as_float(v & 0xffff0000u); }
DEVI float b2f(u16 v) { return __uint_as_float(((u32)v) << 16); }
DEVI u32 pack_bf16(float a, float b) {
  f32x2 v = {a, b};
  return __builtin_bit_cast(u32, __builtin_convertvector(v, bf16x2));
}
DEVI float rb(float a) { return bflo(pack_bf16(a, a)); }
DEVI u32 pack_f16(f32x2 v) { return __builtin_bit_cast(u32, __builtin_convertvector(v, f16x2)); }
DEVI u32 perm_lo(u32 a, u32 b) { return __builtin_amdgcn_perm(b, a, 0x05040100u); }
DEVI u32 perm_hi(u32 a, u32 b) { return __builtin_amdgcn_perm(b, a, 0x07060302u); }
DEVI float dot2(u32 a, u32 b, float c) {
  return __builtin_amdgcn_fdot2_f32_bf16(__builtin_bit_cast(bf16x2, a), __builtin_bit_cast(bf16x2, b), c, false);
}
constexpr u32 ONES2 = 0x3f803f80u;

template <int CTRL>
DEVI float dpp(float v) { return __int_as_float(__builtin_amdgcn_update_dpp(0, __float_as_int(v), CTRL, 0xf, 0xf, true)); }
DEVI float row_sum(float v) {
  v += dpp<0xB1>(v); v += dpp<0x4E>(v); v += dpp<0x141>(v); v += dpp<0x140>(v);
  return v;
}
DEVI float rs32(float a, float b) {
  auto r = __builtin_amdgcn_permlane32_swap(__float_as_uint(a), __float_as_uint(b), false, false);
  return __uint_as_float(r[0]) + __uint_as_float(r[1]);
}
DEVI float rs16(float a, float b) {
  auto r = __builtin_amdgcn_permlane16_swap(__float_as_uint(a), __float_as_uint(b), false, false);
  return __uint_as_float(r[0]) + __uint_as_float(r[1]);
}
DEVI float wave_max(float v) {
  v = fmaxf(v, dpp<0xB1>(v)); v = fmaxf(v, dpp<0x4E>(v)); v = fmaxf(v, dpp<0x141>(v)); v = fmaxf(v, dpp<0x140>(v));
  auto r = __builtin_amdgcn_permlane16_swap(__float_as_uint(v), __float_as_uint(v), false, false);
  v = fmaxf(__uint_as_float(r[0]), __uint_as_float(r[1]));
  auto s = __builtin_amdgcn_permlane32_swap(__float_as_uint(v), __float_as_uint(v), false, false);
  return fmaxf(__uint_as_float(s[0]), __uint_as_float(s[1]));
}

DEVI f32x2 cmul(f32x2 a, f32x2 b) {
  const f32x2 t = a.yy * f32x2{-b.y, b.x};
  return __builtin_elementwise_fma(a.xx, b, t);
}
DEVI f32x2 cexpk(float zr, float zi, float k) {
  const float mag = __builtin_amdgcn_exp2f(zr * (k * 1.4426950408889634f));
  const float rev = __builtin_amdgcn_fractf(zi * (k * 0.15915494309189535f));
  return f32x2{mag * __builtin_amdgcn_cosf(rev), mag * __builtin_amdgcn_sinf(rev)};
}

DEVI u32x4 ld16c(rsrc_t r, u32 off) { return __builtin_amdgcn_raw_buffer_load_b128(r, off, 0, 16); }
DEVI u32x2 ld8c(rsrc_t r, u32 off) { return __builtin_amdgcn_raw_buffer_load_b64(r, off, 0, 16); }
DEVI u32 ld4c(rsrc_t r, u32 off) { return __builtin_amdgcn_raw_buffer_load_b32(r, off, 0, 16); }
DEVI void st16c(rsrc_t r, u32 off, u32x4 v) { __builtin_amdgcn_raw_buffer_store_b128(v, r, off, 0, 16); }
DEVI void st8c(rsrc_t r, u32 off, u32x2 v) { __builtin_amdgcn_raw_buffer_store_b64(v, r, off, 0, 16); }
DEVI void st4c(rsrc_t r, u32 off, u32 v) { __builtin_amdgcn_raw_buffer_store_b32(v, r, off, 0, 16); }
#define MEMFENCE() asm volatile("" ::: "memory")
DEVI void drain_vm() {
  __builtin_amdgcn_s_waitcnt(0x0F70);
  asm volatile("s_waitcnt vmcnt(0)" ::: "memory");
}

template <int N>
DEVI float gather_sum(rsrc_t r, u32 off, u32 stride, u32 tag) {
  float v = 0.f;
#pragma unroll 1
  for (int spin = 0; spin < SPIN_CAP; ++spin) {
    u32x2 g[N];
#pragma unroll
    for (int i = 0; i < N; ++i) g[i] = ld8c(r, off + i * stride);
    u32 bad = 0;
    v = 0.f;
#pragma unroll
    for (int i = 0; i < N; ++i) {
      v += __uint_as_float(g[i][0]);
      bad |= g[i][1] ^ tag;
    }
    if (__all(bad == 0)) break;
    __builtin_amdgcn_s_sleep(2);
    MEMFENCE();
  }
  MEMFENCE();
  return v;
}
DEVI float sum_hi4(float v) {
  v += dpp<0x124>(v);
  v += dpp<0x128>(v);
  v = rs16(v, v);
  return rs32(v, v);
}
DEVI u32 silu2(u32 c) {
  const float a = bflo(c), b = bfhi(c);
  return pack_bf16(a * __builtin_amdgcn_rcpf(1.f + __expf(-a)), b * __builtin_amdgcn_rcpf(1.f + __expf(-b)));
}
DEVI float rdlane(float v, int l) { return __int_as_float(__builtin_amdgcn_readlane(__float_as_int(v), l)); }
DEVI void poll_flag(rsrc_t r, u32 off, u32 tag) {
#pragma unroll 1
  for (int spin = 0; spin < SPIN_CAP; ++spin) {
    const u32 f = ld4c(r, off);
    if (__all(f == tag)) break;
    __builtin_amdgcn_s_sleep(1);
    MEMFENCE();
  }
  MEMFENCE();
}

DEVI void lds_set(char* smem, int grp, int word) {
  MEMFENCE();
  *(__attribute__((address_space(3))) volatile u32*)(smem + L_SYNC + 16 * grp + 4 * word) = 1u;
  MEMFENCE();
}
DEVI u32 lds_arrive(char* smem, int grp) {
  u32 old;
  const u32 addr = (u32)(uintptr_t)(lds_char_p)(smem + L_SYNC + 16 * grp);
  asm volatile("s_mov_b64 exec, 1\n\tds_add_rtn_u32 %0, %1, %2\n\ts_mov_b64 exec, -1\n\ts_waitcnt lgkmcnt(0)"
               : "=&v"(old) : "v"(addr), "v"(1u) : "memory");
  return (u32)__builtin_amdgcn_readfirstlane((int)old);
}
template <int MASK>
DEVI void lds_wait(char* smem, int grp) {
  typedef __attribute__((address_space(3))) volatile u32x4* lds_u32x4_p;
#pragma unroll 1
  for (int spin = 0; spin < (1 << 22); ++spin) {
    MEMFENCE();
    const u32x4 v = *(lds_u32x4_p)(smem + L_SYNC + 16 * grp);
    const u32 a = ((MASK & 1) ? v[0] : 1u) & ((MASK & 2) ? v[1] : 1u) & ((MASK & 4) ? v[2] : 1u) & ((MASK & 8) ? v[3] : 1u);
    if (__builtin_amdgcn_readfirstlane((int)a)) break;
    __builtin_amdgcn_s_sleep(1);
  }
  MEMFENCE();
}

DEVI void glds16(u32 voff, const void* sbase, u32 ldsaddr) {
  asm volatile("s_mov_b32 m0, %2\n\ts_nop 0\n\tglobal_load_lds_dwordx4 %0, %1 sc1" ::"v"(voff), "s"(sbase), "s"(ldsaddr) : "memory");
}
template <int N>
DEVI void wait_vm() { asm volatile("s_waitcnt vmcnt(%0)" ::"n"(N) : "memory"); }
template <int N>
DEVI void wait_vm_reg(u32x4& r) { asm volatile("s_waitcnt vmcnt(%1)" : "+v"(r) : "n"(N)); }
template <class T>
DEVI void pin(T& r) { asm volatile("" : "+v"(r)); }
template <int OFF>
DEVI void gload16(u32x4& r, u32 voff, const void* sbase) {
  asm volatile("global_load_dwordx4 %0, %1, %2 offset:%3 sc1" : "=v"(r) : "v"(voff), "s"(sbase), "n"(OFF) : "memory");
}
DEVI void gstore16(u32x4 v, u32 voff, const void* sbase) {
  asm volatile("global_store_dwordx4 %0, %1, %2 sc1\n\ts_nop 1" ::"v"(voff), "v"(v), "s"(sbase) : "memory");
}
DEVI void barrier() {
  MEMFENCE();
  __builtin_amdgcn_s_barrier();
  MEMFENCE();
}
DEVI void lds_barrier() {
  asm volatile("s_waitcnt lgkmcnt(0)" ::: "memory");
  __builtin_amdgcn_s_barrier();
  MEMFENCE();
}
DEVI bf16x4 tr_read(const char* p) { return __builtin_amdgcn_ds_read_tr16_b64_v4bf16((lds_bf16x4_p)(p)); }
DEVI bf16x8 tr_frag(const char* p0, const char* p1) {
  return __builtin_shufflevector(tr_read(p0), tr_read(p1), 0, 1, 2, 3, 4, 5, 6, 7);
}
DEVI bf16x8 tr_frag_at(u32 a0, u32 a1) {
  return __builtin_shufflevector(__builtin_amdgcn_ds_read_tr16_b64_v4bf16((lds_bf16x4_p)(uintptr_t)a0),
                                 __builtin_amdgcn_ds_read_tr16_b64_v4bf16((lds_bf16x4_p)(uintptr_t)a1), 0, 1, 2, 3, 4, 5, 6, 7);
}
DEVI bf16x8 as_frag(u32x4 v) { return __builtin_bit_cast(bf16x8, v); }
#define MFMA32(a, b, c) __builtin_amdgcn_mfma_f32_32x32x16_bf16(a, b, c, 0, 0, 0)
#define MFMA16(a, b, c) __builtin_amdgcn_mfma_f32_16x16x32_bf16(a, b, c, 0, 0, 0)

DEVI void transpose8(const u32x4 (&r)[8], u32x4 (&o)[8]) {
#pragma unroll
  for (int k = 0; k < 8; ++k)
#pragma unroll
    for (int i = 0; i < 4; ++i)
      o[k][i] = (k & 1) ? perm_hi(r[2 * i][k >> 1], r[2 * i + 1][k >> 1]) : perm_lo(r[2 * i][k >> 1], r[2 * i + 1][k >> 1]);
}
DEVI u32 ln_pack(float s, float q) {
  const float mean = s * (1.f / DM);
  const float var = fmaxf(q * (1.f / DM) - mean * mean, 0.f);
  return pack_bf16(mean, __builtin_amdgcn_sqrtf(rb(rb(var) + 1e-5f)));
}

struct Params {
  const u16 *x, *la, *ai, *cr, *ci, *ldt, *dsk, *wg, *wu, *wd, *c, *wm;
  u16* out;
  char* ws;
};

__global__ __launch_bounds__(512, 2) void fused_block(Params P) {
  extern __shared__ __attribute__((aligned(16))) char smem[];
  DIAG_DECL
  const int tid = threadIdx.x, lane = tid & 63;
  const int w = __builtin_amdgcn_readfirstlane(tid >> 6);
  const int b = blockIdx.x, xcd = b & 7, bi = b >> 3;
  char* const ws = P.ws;
  const rsrc_t R = __builtin_amdgcn_make_buffer_rsrc(ws, 0, WS_USED, 0x00020000);
  const int mt = 8 * (xcd & 1) + (bi & 7), fb = 4 * (xcd >> 1) + (bi >> 3);
  const int m2 = 2 * xcd + (bi & 1), nt = bi >> 2, te = (bi >> 1) & 1, tl = 3 - te;
  const int ft = 7 * fb + w;
  const u32 l16 = lane * 16;
  const char* wsrc = ws + OFF_WP + (size_t)ft * 32768;
  u32x4 wf[32];
  u32 code_touch = 0;
  {
    const __attribute__((address_space(1))) char* pc;
    asm volatile("s_getpc_b64 %0" : "=s"(pc));
    if (w >= 6)
      code_touch = *(const __attribute__((address_space(1))) u32*)(pc + min((2 * (bi % 3) + (w & 1)) * 8192 + lane * 128, CODE_BYTES));
  }
  if (tid < 32) *(u32*)(smem + L_SYNC + 4 * tid) = 0;
  STAMP(32);
  u32x4 pl[8];
  u32 ps[8];
  if (w < 4) {
    const int d = 64 * xcd + 2 * bi + (w >> 1), n = 64 * (w & 1) + lane;
    ps[0] = P.la[d * NS + n];
    ps[1] = P.ai[d * NS + n];
    ps[2] = P.cr[d * NS + n];
    ps[3] = P.ci[d * NS + n];
    ps[4] = P.cr[d * NS + (n ^ 64)];
    ps[5] = P.ci[d * NS + (n ^ 64)];
    ps[6] = P.ldt[d];
    ps[7] = P.dsk[(d & ~1) + ((lane >> 2) & 1)];
  } else if (w == 4) {
#pragma unroll
    for (int i = 0; i < 8; ++i) pl[i] = *(const u32x4*)(P.x + (size_t)(8 * b + 2 * (lane & 3) + (i & 1)) * DM + 8 * (lane >> 2) + 128 * (i >> 1));
  } else if (w == 5) {
    const int ra = 32 * (b & 15) + 4 * (lane >> 3);
    const u32x2 cv = *(const u32x2*)(P.c + ra);
    ps[0] = cv[0];
    ps[1] = cv[1];
#pragma unroll
    for (int i = 0; i < 4; ++i) pl[i] = *(const u32x4*)(P.wm + (size_t)(ra + i) * 3072 + 64 * (b >> 4) + 8 * (lane & 7));
  }
  lds_barrier();
  STAMP(35);
  u32 tag;
  auto read_tag = [&]() {
    u32 e;
    STAMP(33);
    asm volatile("s_load_dword %0, %1, 0x0\n\ts_waitcnt lgkmcnt(0)" : "=s"(e) : "s"(ws + OFF_E) : "memory");
    tag = e + 0x9E3779B9u;
    STAMP(34);
  };
  auto load_w = [&](auto J) {
    constexpr int j = decltype(J)::value;
    gload16<(j & 3) * 1024>(wf[j], l16, wsrc + (j >> 2) * 4096);
  };

  const u32 lds0 = (u32)(uintptr_t)(lds_char_p)smem;
  const char* wdg = ws + OFF_WD + ((size_t)(2 * nt) * NFT + 14 * w) * 1024;
  const char* ag = ws + OFF_ACT + ((size_t)(4 * m2) * NFT + 14 * w) * 1024;
  auto stage_lds = [&](int j) { return j < 16 ? (j >> 2) * 32768 + w * 4096 + (j & 3) * 1024 : L_SPARE + (j - 16) * 8192 + w * 1024; };
  auto stage_wd = [&](int j) { glds16(l16, wdg + (size_t)(j & 1) * NFT * 1024 + (j >> 1) * 1024, lds0 + stage_lds(j)); };
  auto stamp_t = [&](int i) { STAMP(i); };
  const int q4 = (lane & 15) >> 2, p4 = lane & 3, hh = lane >> 5, blk = (lane >> 4) & 1;
  u32x4 wr[10], ae[14];
  auto load_wr = [&](auto J) {
    constexpr int j = 18 + decltype(J)::value;
    gload16<0>(wr[j - 18], l16, wdg + (size_t)(j & 1) * NFT * 1024 + (j >> 1) * 1024);
  };
  auto load_ae = [&](auto J) {
    constexpr int j = decltype(J)::value;
    gload16<(j & 3) * 1024>(ae[j], l16, ag + (size_t)te * NFT * 1024 + (j >> 2) * 4096);
  };

  auto down_phase = [&](auto) {
    stamp_t(28);
    bf16x8 wd[28];
#pragma unroll
    for (int j = 0; j < 28; ++j) wd[j] = j < 18 ? *(const bf16x8*)(smem + stage_lds(j) + l16) : as_frag(wr[j - 18]);
    auto put_partial = [&](int qd, const f32x16& a) {
#pragma unroll
      for (int g = 0; g < 4; ++g)
        *(f32x4*)(smem + qd * 32768 + w * 4096 + g * 1024 + l16) = f32x4{a[4 * g], a[4 * g + 1], a[4 * g + 2], a[4 * g + 3]};
    };
    u32x4 al[14];
    auto load_al = [&](auto J) {
      constexpr int j = decltype(J)::value;
      gload16<(j & 3) * 1024>(al[j], l16, ag + (size_t)tl * NFT * 1024 + (j >> 2) * 4096);
    };
    f32x16 a0 = {}, a1 = {};
    static_for<0, 14>([&](auto J) {
      constexpr int j = decltype(J)::value;
      wait_vm_reg<13 - j>(ae[j]);
      a0 = MFMA32(wd[2 * j], as_frag(ae[j]), a0);
      a1 = MFMA32(wd[2 * j + 1], as_frag(ae[j]), a1);
    });
    pin(a0);
    pin(a1);
    stamp_t(13);
    lds_wait<1>(smem, 6);
    stamp_t(29);
    static_for<0, 14>(load_al);
    put_partial(0, a0);
    put_partial(2, a1);
    a0 = f32x16{};
    a1 = f32x16{};
    static_for<0, 14>([&](auto J) {
      constexpr int j = decltype(J)::value;
      wait_vm_reg<13 - j>(al[j]);
      a0 = MFMA32(wd[2 * j], as_frag(al[j]), a0);
      a1 = MFMA32(wd[2 * j + 1], as_frag(al[j]), a1);
    });
    stamp_t(14);
    put_partial(1, a0);
    put_partial(3, a1);
    stamp_t(15);
    lds_barrier();
    stamp_t(30);
    const int qi = w >> 2, qj = (w >> 1) & 1, g0 = 2 * (w & 1);
    u32x2 o[2];
#pragma unroll
    for (int gg = 0; gg < 2; ++gg) {
      f32x4 s = {0.f, 0.f, 0.f, 0.f};
#pragma unroll
      for (int v = 0; v < 8; ++v) s += *(const f32x4*)(smem + (w >> 1) * 32768 + v * 4096 + (g0 + gg) * 1024 + l16);
      const int cl = 32 * qi + 8 * (g0 + gg) + 4 * hh;
      const f32x4 gb = *(const f32x4*)(smem + L_GB + 4 * cl);
      const u32x2 xr = __builtin_bit_cast(u32x2, tr_read(smem + L_RES + qj * 4096 + (cl + q4) * 64 + 32 * (blk ^ hh) + 8 * p4));
#pragma unroll
      for (int e = 0; e < 2; ++e) {
        o[gg][e] = pack_bf16(bflo(xr[e]) + gb[2 * e] * s[2 * e], bfhi(xr[e]) + gb[2 * e + 1] * s[2 * e + 1]);
      }
    }
    auto s0 = __builtin_amdgcn_permlane32_swap(o[0][0], o[1][0], false, false);
    auto s1 = __builtin_amdgcn_permlane32_swap(o[0][1], o[1][1], false, false);
    const int tok = 128 * m2 + 32 * (qj ? tl : te) + (lane & 31);
    *(u32x4*)(P.out + (size_t)tok * DM + 64 * nt + 32 * qi + 8 * g0 + 8 * hh) = u32x4{s0[0], s1[0], s0[1], s1[1]};
  };

  auto x1_arrive = [&]() {
    if (lds_arrive(smem, 4) == 4) {
      if (lane < 8) st4c(R, OFF_FX + 4096 * lane + 16 * b, tag);
      lds_set(smem, 4, 1);
    }
  };

  if (w < 4) {
    const int ch = w >> 1, half = w & 1;
    const int c0 = 64 * xcd + 2 * bi;
    const int n = 64 * half + lane;
    int kidx[2][9];
#pragma unroll
    for (int i = 0; i < 2; ++i)
#pragma unroll
      for (int e = 0; e < 5; ++e) {
        const int l0 = 2047 - 8 * (n + 128 * i) - 2 * e, l1 = l0 - 1;
        const u32 pb = pack_bf16((float)l0, (float)l1);
        kidx[i][2 * e] = L_KC + 4 * (w * 656 + ((l0 < 128) ? max(l0, 0) : (int)(pb & 0xffffu) - 133 * 128));
        if (e < 4) kidx[i][2 * e + 1] = L_KC + 4 * (w * 656 + ((l1 < 128) ? l1 : (int)(pb >> 16) - 133 * 128));
      }
#pragma unroll
    for (int i = 0; i < 2; ++i)
#pragma unroll
      for (int e = 0; e < 9; ++e) asm volatile("" : "+v"(kidx[i][e]));
    if (tid < 60) {
      *(u32x4*)(smem + CV_H + (tid / 30) * CV_HB + (tid % 30) * 16) = u32x4{0, 0, 0, 0};
    } else if (tid >= 64 && tid < 72) {
      *(u32x4*)(smem + CV_K + ((tid - 64) >> 1) * CV_KCOPY + 4096 + (tid & 1) * 16) = u32x4{0, 0, 0, 0};
    }
    read_tag();
#pragma unroll
    for (int i = 0; i < 8; ++i) pin(ps[i]);
    const float v_cr = bflo(ps[2]), v_ci = bflo(ps[3]), o_cr = bflo(ps[4]), o_ci = bflo(ps[5]);
    const float dt = rb(expf(bflo(ps[6])));
    const float zr = -rb(expf(bflo(ps[0]))) * dt, zi = bflo(ps[1]) * dt;
    STAMP(3);
    const float mx = wave_max(fmaxf(fmaxf(fabsf(v_cr), fabsf(v_ci)), fmaxf(fabsf(o_cr), fabsf(o_ci))));
    const int ex = min(max((int)(__float_as_uint(mx) >> 23), 1), 252);
    const float sdn = __uint_as_float((u32)(254 - ex) << 23);
    const float sup = __uint_as_float((u32)(ex + 1) << 23) * dt;
    {
      char* base = smem + w * KG_WAVEB + lane * 4;
      auto put = [&](int row, f32x2 v) { *(u32*)(base + row * KG_ROWB) = pack_f16(v); };
      const f32x2 e8 = cexpk(zr, zi, 8.f), e16 = cexpk(zr, zi, 16.f);
      const f32x2 e1 = cexpk(zr, -zi, 1.f), e2 = cexpk(zr, -zi, 2.f);
      const f32x2 e128 = cexpk(zr, -zi, 128.f), e256 = cexpk(zr, -zi, 256.f);
      f32x2 f0 = e16, f1 = e8;
      put(41, f32x2{1.f, 0.f});
      put(42, e8);
#pragma unroll
      for (int j = 2; j < 16; j += 2) {
        f1 = cmul(f1, e16);
        put(41 + j, f0);
        put(42 + j, f1);
        if (j < 14) f0 = cmul(f0, e16);
      }
      const f32x2 e4 = cmul(e2, e2);
      f32x2 g[2];
      g[0] = f32x2{v_cr * sdn, -v_ci * sdn};
      g[1] = cmul(g[0], e128);
#pragma unroll
      for (int i = 0; i < 17; ++i) {
        const f32x2 gi = g[i & 1];
        if (i < 2) {
          f32x2 a = gi, c = cmul(gi, e1);
#pragma unroll
          for (int e = 0; e < 8; e += 2) {
            put(8 * i + e, a);
            put(8 * i + e + 1, c);
            if (e < 6) { a = cmul(a, e2); c = cmul(c, e2); }
          }
        } else if (i < 4) {
          const f32x2 c = cmul(gi, e2);
          put(16 + 4 * (i - 2), gi);
          put(17 + 4 * (i - 2), c);
          put(18 + 4 * (i - 2), cmul(gi, e4));
          put(19 + 4 * (i - 2), cmul(c, e4));
        } else if (i < 8) {
          put(24 + 2 * (i - 4), gi);
          put(25 + 2 * (i - 4), cmul(gi, e4));
        } else {
          put(32 + (i - 8), gi);
        }
        if (i + 2 < 17) g[i & 1] = cmul(gi, e256);
      }
    }
    lds_set(smem, 0, w);
    STAMP(4);
    const int r = lane & 15, q = lane >> 4;
    {
      f32x4 acc[3] = {};
#pragma unroll
      for (int hf = 0; hf < 2; ++hf) {
        if (hf == 0) {
          if (ch) lds_wait<12>(smem, 0);
          else lds_wait<3>(smem, 0);
        }
        const char* rbase = smem + (2 * ch + hf) * KG_WAVEB + r * KG_ROWB + q * 16;
        f16x8 fr[4][4];
#pragma unroll
        for (int ks = 0; ks < 4; ++ks) {
          fr[ks][3] = *(const f16x8*)(rbase + ks * 64 + 41 * KG_ROWB);
#pragma unroll
          for (int t = 0; t < 3; ++t) fr[ks][t] = *(const f16x8*)(rbase + ks * 64 + 16 * t * KG_ROWB);
        }
        __builtin_amdgcn_sched_barrier(0);
#pragma unroll
        for (int ks = 0; ks < 4; ++ks)
#pragma unroll
          for (int t = 0; t < 3; ++t) acc[t] = __builtin_amdgcn_mfma_f32_16x16x32_f16(fr[ks][t], fr[ks][3], acc[t], 0, 0, 0);
      }
      pin(acc[0]); pin(acc[1]); pin(acc[2]);
      STAMP(12);
      float* kc = (float*)(smem + L_KC) + w * 656;
      *(f32x4*)(kc + 128 * (q >> 1) + 4 * (q & 1) + 8 * r) = acc[0] * sup;
      if (q < 2) {
        *(f32x4*)(kc + 256 + 64 * q + 4 * r) = acc[1] * sup;
      } else {
        *(f32x2*)(kc + 384 + 64 * (q - 2) + 2 * r) = f32x2{acc[1][0], acc[1][1]} * sup;
        *(f32x2*)(kc + 416 + 64 * (q - 2) + 2 * r) = f32x2{acc[1][2], acc[1][3]} * sup;
      }
#pragma unroll
      for (int e = 0; e < 4; ++e)
        if (4 * q + e <= 8) kc[512 + 16 * (4 * q + e) + r] = acc[2][e] * sup;
      MEMFENCE();
      float kv[2][9];
#pragma unroll
      for (int i = 0; i < 2; ++i)
#pragma unroll
        for (int e = 0; e < 9; ++e) kv[i][e] = *(const float*)(smem + kidx[i][e]);
      if (n == 127) kv[1][8] = 0.f;
#pragma unroll
      for (int i = 0; i < 2; ++i) {
        const int u0 = 8 * (n + 128 * i);
        u32x4 o, s;
#pragma unroll
        for (int e = 0; e < 4; ++e) {
          o[e] = pack_bf16(kv[i][2 * e], kv[i][2 * e + 1]);
          s[e] = pack_bf16(kv[i][2 * e + 1], kv[i][2 * e + 2]);
        }
        *(u32x4*)(smem + CV_K + (2 * ch) * CV_KCOPY + 2 * u0) = o;
        *(u32x4*)(smem + CV_K + (2 * ch + 1) * CV_KCOPY + 2 * u0) = s;
      }
    }
    if (w == 0) lds_set(smem, 5, 1);
    STAMP(5);
    {
      const int pr = c0 >> 1;
      const u32 xo = OFF_XG + (tid >> 2) * 16384 + (((pr >> 6) * 4 + (pr & 3)) * 16 + ((pr >> 2) & 15)) * 64 + (tid & 3) * 16;
      const u32 mo = OFF_GMA + (lane >> 2) * 8192 + 8 * (((lane >> 1) & 1) * DM + c0 + (lane & 1));
      u32x4 gx[4], gs[4];
      u32x2 gm;
#pragma unroll 1
      for (int spin = 0; spin < SPIN_CAP; ++spin) {
        gm = ld8c(R, mo);
        u32 bad = 0;
#pragma unroll
        for (int i = 0; i < 4; ++i) {
          gx[i] = ld16c(R, xo + i * (64 * 16384));
          gs[i] = ld16c(R, OFF_GST + 16 * tid + 4096 * i);
        }
#pragma unroll
        for (int i = 0; i < 4; ++i) bad |= (gx[i][1] ^ tag) | (gx[i][3] ^ tag) | (gs[i][1] ^ tag) | (gs[i][3] ^ tag) | (gm[1] ^ tag);
        if (bad == 0) break;
        __builtin_amdgcn_s_sleep(2);
        MEMFENCE();
      }
      MEMFENCE();
      STAMP(6);
      const float md = rb(sum_hi4(__uint_as_float(gm[0])));
      const float sa[2] = {rdlane(md, 0), rdlane(md, 1)}, ca1[2] = {rb(1.f + rdlane(md, 2)), rb(1.f + rdlane(md, 3))};
#pragma unroll
      for (int i = 0; i < 4; ++i) {
        const float r0 = __builtin_amdgcn_rcpf(bfhi(gs[i][0])), r1 = __builtin_amdgcn_rcpf(bfhi(gs[i][2]));
        const float m0 = bflo(gs[i][0]), m1 = bflo(gs[i][2]);
        *(u32*)(smem + CV_H + 480 + 1024 * i + 4 * tid) =
            pack_bf16((bflo(gx[i][0]) - m0) * r0 * ca1[0] + sa[0], (bflo(gx[i][2]) - m1) * r1 * ca1[0] + sa[0]);
        *(u32*)(smem + CV_H + CV_HB + 480 + 1024 * i + 4 * tid) =
            pack_bf16((bfhi(gx[i][0]) - m0) * r0 * ca1[1] + sa[1], (bfhi(gx[i][2]) - m1) * r1 * ca1[1] + sa[1]);
      }
      lds_set(smem, 2, w);
#pragma unroll
      for (int i = 0; i < 4; ++i) {
        *(u32*)(smem + CV_X + 1024 * i + 4 * tid) = perm_lo(gx[i][0], gx[i][2]);
        *(u32*)(smem + CV_X + 4096 + 1024 * i + 4 * tid) = perm_hi(gx[i][0], gx[i][2]);
      }
    }
    const u32 eo = OFF_GMB + ((lane >> 3) & 7) * 16384 + 8 * (((lane >> 1) & 1) * 2 * DM + c0 + ((lane >> 2) & 1));
    const u32x2 eg = ld8c(R, eo);
    lds_wait<15>(smem, 2);
    STAMP(7);
    const char* abase = smem + CV_K + (2 * ch + ((r & 1) ^ 1)) * CV_KCOPY + 2 * (2030 - (r & ~1) + 8 * q) - 64 * (4 * half + 3 + 56);
    const char* bbase = smem + CV_H + ch * CV_HB + 2 * (224 + 16 * r + 8 * q) - 64 * (4 * half + 3);
    f32x4 acc[8] = {};
    bf16x8 af[2][8], bfr[2][8];
    u32 ab[4];
#pragma unroll
    for (int i = 0; i < 4; ++i) {
      ab[i] = (u32)(uintptr_t)(lds_char_p)abase + 1024 * i;
      pin(ab[i]);
    }
    auto ldfr = [&](int rho, int i) {
      const lds_u32_p p = (lds_u32_p)(uintptr_t)(ab[(7 - i) >> 1] + 64 * (3 - rho) + 512 * ((7 - i) & 1));
      af[rho & 1][i] = as_frag(u32x4{p[0], p[1], p[2], p[3]});
      bfr[rho & 1][i] = *(const bf16x8*)(bbase + 64 * (3 - rho) + 512 * i);
    };
#pragma unroll
    for (int i = 0; i < 4; ++i) ldfr(0, i);
    static_for<0, 32>([&](auto S) {
      constexpr int st = decltype(S)::value, rho = st >> 3, m = st & 7;
      if constexpr (st + 4 < 32) ldfr((st + 4) >> 3, (st + 4) & 7);
#pragma unroll
      for (int o = 0; o <= m; ++o) {
        if (m + o < 8) {
          acc[m + o] = MFMA16(af[rho & 1][m], bfr[rho & 1][o], acc[m + o]);
          if (o != m) acc[m + o] = MFMA16(af[rho & 1][o], bfr[rho & 1][m], acc[m + o]);
        }
      }
#pragma unroll
      for (int i = 0; i < 3; ++i) {
        __builtin_amdgcn_sched_group_barrier(0x008, 1, 0);
        __builtin_amdgcn_sched_group_barrier(0x100, 1, 0);
      }
      __builtin_amdgcn_sched_barrier(0);
    });
    STAMP(8);
#pragma unroll
    for (int t = 0; t < 8; ++t) *(f32x4*)(smem + CV_P + w * 8192 + ch * 16 + 1024 * t + 64 * r + 16 * q) = acc[t];
    lds_set(smem, 3, w);
    {
      float v = __uint_as_float(eg[0]);
      if (!__all(eg[1] == tag)) v = gather_sum<1>(R, eo, 0, tag);
      v += dpp<0x128>(v);
      v = rs16(v, v);
      v = rb(rs32(v, v));
      const float ga = dpp<0x00>(v), cb1 = rb(1.f + dpp<0xAA>(v)), dk = bflo(ps[7]);
      const int c = (lane >> 2) & 1;
      lds_wait<15>(smem, 3);
      STAMP(11);
#pragma unroll
      for (int i = 0; i < 2; ++i) {
        const int e = tid + 256 * i;
        const int l0 = 32 * (e >> 3) + 8 * ((e & 3) ^ (2 * ((c0 >> 2) & 1)));
        const char* pp = smem + CV_P + c * (16384 + 16) + 4 * l0;
        const f32x4 y0 = *(const f32x4*)(pp) + *(const f32x4*)(pp + 8192);
        const f32x4 y1 = *(const f32x4*)(pp + 16) + *(const f32x4*)(pp + 8192 + 16);
        const u32x4 hv = *(const u32x4*)(smem + CV_H + c * CV_HB + 480 + 2 * l0);
        const u32x4 xx = *(const u32x4*)(smem + CV_X + c * 4096 + 2 * l0);
        u32x4 o, os;
#pragma unroll
        for (int k = 0; k < 4; ++k) {
          const u32 yb = k < 2 ? pack_bf16(y0[2 * k], y0[2 * k + 1]) : pack_bf16(y1[2 * k - 4], y1[2 * k - 3]);
          const u32 hd = pack_bf16(bflo(hv[k]) * dk, bfhi(hv[k]) * dk);
          const u32 z = pack_bf16(bflo(yb) + bflo(hd), bfhi(yb) + bfhi(hd));
          const u32 gz = pack_bf16(ga * bflo(z), ga * bfhi(z));
          o[k] = pack_bf16(bflo(xx[k]) + bflo(gz), bfhi(xx[k]) + bfhi(gz));
          os[k] = pack_bf16(bflo(o[k]) * cb1, bfhi(o[k]) * cb1);
        }
        const u32 go = (e >> 3) * 32768 + c0 * 64 + (e & 7) * 16;
        st16c(R, OFF_XS + go, os);
        st16c(R, OFF_X1 + go, o);
      }
      STAMP(16);
      drain_vm();
      x1_arrive();
      STAMP(9);
    }
  } else {
    if (w >= 6) {
      const int un = min(b, 223);
      const u16* src = (((un >> 2) & 1) ? P.wu : P.wg) + (size_t)(64 * (2 * (un & 3) + (w & 1)) + 8 * (lane >> 3)) * DF + 64 * (un >> 3) + 8 * (lane & 7);
#pragma unroll
      for (int i = 0; i < 8; ++i) pl[i] = *(const u32x4*)(src + i * DF);
    }
    read_tag();
    if (w == 4) {
      const int tp = lane & 3;
      auto r = [&](int a, int e) { return pl[2 * a + e]; };
      STAMP(6);
      float s[2] = {0.f, 0.f}, qq[2] = {0.f, 0.f};
#pragma unroll
      for (int a = 0; a < 4; ++a)
#pragma unroll
        for (int k = 0; k < 4; ++k)
#pragma unroll
          for (int e = 0; e < 2; ++e) {
            s[e] = dot2(r(a, e)[k], ONES2, s[e]);
            qq[e] = dot2(r(a, e)[k], r(a, e)[k], qq[e]);
          }
      STAMP(3);
      {
        float v = rs16(rs32(s[0], qq[0]), rs32(s[1], qq[1]));
        v += dpp<0x124>(v);
        v += dpp<0x128>(v);
        auto sw = __builtin_amdgcn_permlane32_swap(__float_as_uint(v), __float_as_uint(v), false, false);
        const u32 pk = ln_pack(__uint_as_float(sw[0]), __uint_as_float(sw[1]));
        if (lane < 32 && (lane & 15) < 4) st8c(R, OFF_GST + 64 * b + 16 * tp + 8 * (lane >> 4), u32x2{pk, tag});
      }
#pragma unroll
      for (int a = 0; a < 4; ++a)
#pragma unroll
        for (int k = 0; k < 4; ++k) st16c(R, OFF_XG + b * 16384 + (a * 4 + k) * 1024 + l16, u32x4{r(a, 0)[k], tag, r(a, 1)[k], tag});
      STAMP(4);
      {
        const int fg = 28 * xcd + min(bi, 27);
        u32x4 wv[8], o[8];
#pragma unroll
        for (int i = 0; i < 8; ++i) wv[i] = *(const u32x4*)(P.wd + (size_t)(8 * fg + i) * DM + 8 * lane);
        transpose8(wv, o);
        STAMP(7);
        char* tb = smem + L_TR;
#pragma unroll
        for (int k = 0; k < 8; ++k) *(u32x4*)(tb + lane * 128 + ((k ^ lane) & 7) * 16) = o[k];
        MEMFENCE();
#pragma unroll
        for (int k = 0; k < 8; ++k) o[k] = *(const u32x4*)(tb + (8 * k + (lane >> 3)) * 128 + ((lane ^ (lane >> 3)) & 7) * 16);
        lds_wait<15>(smem, 2);
        if (bi < 28) {
#pragma unroll
          for (int k = 0; k < 8; ++k) st16c(R, OFF_WD + (2 * k + (lane >> 5)) * (NFT * 1024) + fg * 512 + (lane & 31) * 16, o[k]);
        }
      }
    } else if (w == 5) {
      auto part = [&](auto NP, const u32x4* wv, const u32* sl, u32 dst) {
        float acc[8] = {};
#pragma unroll
        for (int j = 0; j < decltype(NP)::value; ++j)
#pragma unroll
          for (int k = 0; k < 4; ++k) {
            acc[2 * k] = dot2(perm_lo(wv[2 * j][k], wv[2 * j + 1][k]), sl[j], acc[2 * k]);
            acc[2 * k + 1] = dot2(perm_hi(wv[2 * j][k], wv[2 * j + 1][k]), sl[j], acc[2 * k + 1]);
          }
#pragma unroll
        for (int i = 0; i < 4; ++i) acc[i] = rs32(acc[i], acc[i + 4]);
#pragma unroll
        for (int i = 0; i < 2; ++i) {
          acc[i] = rs16(acc[i], acc[i + 2]);
          acc[i] += dpp<0x128>(acc[i]);
        }
        if ((lane & 8) == 0)
          st16c(R, dst + 8 * (8 * (lane & 7) + 2 * (lane >> 4)), u32x4{__float_as_uint(acc[0]), tag, __float_as_uint(acc[1]), tag});
      };
      pin(ps[0]);
      pin(ps[1]);
      const u32 sa_[2] = {silu2(ps[0]), silu2(ps[1])};
      part(std::integral_constant<int, 2>{}, pl, sa_, OFF_GMA + (b & 15) * 8192 + 512 * (b >> 4));
      STAMP(3);
      const int rw = 64 * (b & 7) + 8 * (lane >> 3);
      const u32x4 cv = *(const u32x4*)(P.c + rw);
#pragma unroll
      for (int i = 0; i < 8; ++i) pl[i] = *(const u32x4*)(P.wm + (size_t)(rw + i) * 3072 + 1024 + 64 * (b >> 3) + 8 * (lane & 7));
      const u32 sb_[4] = {silu2(cv[0]), silu2(cv[1]), silu2(cv[2]), silu2(cv[3])};
      part(std::integral_constant<int, 4>{}, pl, sb_, OFF_GMB + (b & 7) * 16384 + 512 * (b >> 3));
      STAMP(4);
      lds_wait<2>(smem, 5);
#pragma unroll 1
      for (int spin = 0; spin < SPIN_CAP; ++spin) {
        const u32 f = ld4c(R, OFF_FW + min(((7 * fb) >> 2) * 16 + lane, ((7 * fb + 6) >> 2) * 16 + 15) * 64);
        if (__all(f == tag)) break;
        __builtin_amdgcn_s_sleep(12);
        MEMFENCE();
      }
      lds_set(smem, 5, 0);
      STAMP(5);
    } else {
      const int un = min(b, 223), cg = un >> 3, type = (un >> 2) & 1, k8 = 2 * (un & 3) + (w & 1);
      const int kg = lane >> 3, cp = lane & 7;
      u32x4 r[8], t[8];
#pragma unroll
      for (int i = 0; i < 8; ++i) r[i] = pl[i];
      if (b < 224) {
        transpose8(r, t);
        STAMP(3);
        char* tb = smem + L_TR + (1 + (w & 1)) * 8192;
#pragma unroll
        for (int n = 0; n < 8; ++n)
          *(u32x4*)(tb + ((cp >> 1) * 8 + kg) * 256 + (((n & 3) + 8 * (n >> 2) + 4 * (cp & 1)) ^ (cp >> 1)) * 16) = t[n];
        MEMFENCE();
#pragma unroll
        for (int k = 0; k < 8; ++k) {
          const int row = 4 * k + (lane >> 4), fl = k >> 1;
          const u32x4 o = *(const u32x4*)(tb + row * 256 + ((lane & 15) ^ fl) * 16);
          st16c(R, OFF_WP + (((4 * cg + fl) * 32 + 4 * k8) * 64 + (row & 7) * 32 + 16 * type + (lane & 15)) * 16, o);
        }
        drain_vm();
        if (lane == 0) st4c(R, OFF_FW + (cg * 16 + type * 8 + k8) * 64, tag);
        STAMP(4);
      }
      if (w == 6) lds_set(smem, 5, 2);
      if (b < 224) {
        if (w == 7) {
          lds_wait<15>(smem, 2);
          u32x4 g[16];
#pragma unroll 1
          for (int spin = 0; spin < SPIN_CAP; ++spin) {
            u32 bad = 0;
#pragma unroll
            for (int i = 0; i < 16; ++i) g[i] = ld16c(R, OFF_GMB + (i >> 1) * 16384 + 8 * ((1 + (i & 1)) * DM + 128 * (b & 3)) + l16);
#pragma unroll
            for (int i = 0; i < 16; ++i) bad |= (g[i][1] ^ tag) | (g[i][3] ^ tag);
            if (__all(bad == 0)) break;
            __builtin_amdgcn_s_sleep(2);
            MEMFENCE();
          }
          float v[4] = {};
#pragma unroll
          for (int i = 0; i < 16; ++i) {
            v[2 * (i & 1)] += __uint_as_float(g[i][0]);
            v[2 * (i & 1) + 1] += __uint_as_float(g[i][2]);
          }
          *(u32*)(smem + L_MD + 4 * lane) = pack_bf16(v[0], v[1]);
          *(u32*)(smem + L_MD + 256 + 4 * lane) = pack_bf16(1.f + rb(v[2]), 1.f + rb(v[3]));
          lds_wait<4>(smem, 5);
#pragma unroll 1
          for (int u = 0; u < 2; ++u) {
            const u32x4 sb = *(const u32x4*)(smem + L_MD + 128 * u + 16 * kg), sc = *(const u32x4*)(smem + L_MD + 256 + 128 * u + 16 * kg);
            float cs[8], bs[8];
#pragma unroll
            for (int n = 0; n < 8; ++n) {
              const u32x4 tv = *(const u32x4*)(smem + L_TR + (1 + u) * 8192 + ((cp >> 1) * 8 + kg) * 256 +
                                               (((n & 3) + 8 * (n >> 2) + 4 * (cp & 1)) ^ (cp >> 1)) * 16);
              cs[n] = 0.f;
              bs[n] = 0.f;
#pragma unroll
              for (int i = 0; i < 4; ++i) {
                cs[n] = dot2(tv[i], sc[i], cs[n]);
                bs[n] = dot2(tv[i], sb[i], bs[n]);
              }
            }
#pragma unroll
            for (int i = 0; i < 4; ++i) { cs[i] = rs32(cs[i], cs[i + 4]); bs[i] = rs32(bs[i], bs[i + 4]); }
#pragma unroll
            for (int i = 0; i < 2; ++i) {
              cs[i] = rs16(cs[i], cs[i + 2]);
              bs[i] = rs16(bs[i], bs[i + 2]);
              cs[i] += dpp<0x128>(cs[i]);
              bs[i] += dpp<0x128>(bs[i]);
            }
            const int odd = (lane >> 3) & 1, n = 2 * (lane >> 4) + odd;
            const int rho = 16 * type + (n & 3) + 8 * (n >> 2) + 4 * (cp & 1);
            st16c(R, OFF_AUX + ((4 * cg + (cp >> 1)) * 8 + 2 * (b & 3) + u) * 512 + rho * 16,
                  u32x4{__float_as_uint(odd ? cs[1] : cs[0]), tag, __float_as_uint(odd ? bs[1] : bs[0]), tag});
          }
          STAMP(5);
        }
      }
      drain_vm();
    }
    pin(code_touch);
    if (w == 7) {
    const char* xsg = ws + OFF_XS + (size_t)(4 * mt) * 32768;
    const char* x1g = ws + OFF_X1 + (size_t)(4 * mt) * 32768;
    auto dma = [&](const char* src, int dst, int n) {
#pragma unroll 4
      for (int i = 0; i < n; ++i) glds16(l16, src + i * 1024, lds0 + dst + i * 1024);
    };
    const u32 fao = OFF_FA + mt * 4096;
    lds_wait<4>(smem, 5);
    lds_wait<15>(smem, 1);
#pragma unroll 1
    for (int spin = 0; spin < SPIN_CAP; ++spin) {
      u32 f[4];
#pragma unroll
      for (int i = 0; i < 4; ++i) f[i] = ld4c(R, OFF_FX + 4096 * xcd + (lane + 64 * i) * 16);
      if (__all(((f[0] ^ tag) | (f[1] ^ tag) | (f[2] ^ tag) | (f[3] ^ tag)) == 0)) break;
      __builtin_amdgcn_s_sleep(1);
      MEMFENCE();
    }
    drain_vm();
    STAMP(10);
    lds_set(smem, 6, 1);
    if (b == 0 && lane == 0) st4c(R, OFF_E, tag);
    dma(x1g + (fb >> 2) * 32768 + (fb & 3) * 8192, L_SPARE, 8);
    barrier();
    STAMP(17);
    dma(xsg + 35840, 35840, 13);
    wait_vm<13>();
    {
      const int g = lane >> 4;
      const char* sb = smem + L_SPARE + (8 * (g >> 1) + 4 * (g & 1) + q4) * 64 + 8 * p4;
      const bf16x8 ones = as_frag(u32x4{ONES2, ONES2, ONES2, ONES2});
      bf16x8 f[2][4];
#pragma unroll
      for (int k2 = 0; k2 < 4; ++k2)
#pragma unroll
        for (int c = 0; c < 2; ++c) {
          const char* p = sb + 32 * (c ^ (g & 1)) + k2 * 2048;
          f[c][k2] = tr_frag(p, p + 1024);
        }
      __builtin_amdgcn_sched_barrier(0);
#pragma unroll
      for (int c = 0; c < 2; ++c) {
        f32x4 aq = {}, as = {};
#pragma unroll
        for (int k2 = 0; k2 < 4; ++k2) {
          aq = MFMA16(f[c][k2], f[c][k2], aq);
          as = MFMA16(ones, f[c][k2], as);
        }
        const float sq = p4 == 0 ? aq[0] : p4 == 1 ? aq[1] : p4 == 2 ? aq[2] : aq[3];
        if (g == q4)
          st16c(R, OFF_GS2 + ((mt * 4 + (fb & 3)) * 128 + 32 * (fb >> 2) + 16 * c + (lane & 15)) * 16,
                u32x4{__float_as_uint(as[0]), tag, __float_as_uint(sq), tag});
      }
    }
    dma(xsg + 49152, 49152, 16);
    lds_barrier();
    dma(xsg + 65536, 65536, 16);
    {
      u32x4 g[2][4];
#pragma unroll 1
      for (int spin = 0; spin < SPIN_CAP; ++spin) {
        u32 bad = 0;
#pragma unroll
        for (int i = 0; i < 8; ++i) g[i & 1][i >> 1] = ld16c(R, OFF_GS2 + mt * 8192 + i * 1024 + l16);
#pragma unroll
        for (int i = 0; i < 8; ++i) bad |= (g[i & 1][i >> 1][1] ^ tag) | (g[i & 1][i >> 1][3] ^ tag);
        drain_vm();
        if (spin == 0) {
          stamp_t(18);
          barrier();
          stamp_t(19);
        }
        if (__all(bad == 0)) break;
        MEMFENCE();
      }
#pragma unroll
      for (int c = 0; c < 2; ++c) {
        const float s1 = (__uint_as_float(g[c][0][0]) + __uint_as_float(g[c][1][0])) + (__uint_as_float(g[c][2][0]) + __uint_as_float(g[c][3][0]));
        const float s2 = (__uint_as_float(g[c][0][2]) + __uint_as_float(g[c][1][2])) + (__uint_as_float(g[c][2][2]) + __uint_as_float(g[c][3][2]));
        const u32 ms = ln_pack(s1, s2) ^ 0x8000u;
        *(u32x4*)(smem + L_SF + (64 * c + lane) * 16) = u32x4{perm_lo(ms, ms), ms, perm_hi(ms, ms), 0};
        const float k0 = -2.f * 0.7978845608028654f * 1.4426950408889634f, k1 = k0 * 0.044715f;
        const float sd = bfhi(ms), rs = __builtin_amdgcn_rcpf(sd);
        *(f32x4*)(smem + L_RS + (64 * c + lane) * 16) = f32x4{k0 * rs, k1 * rs * rs * rs, sd * sd, 0.f};
      }
    }
    lds_set(smem, 7, 0);
    stamp_t(12);
    dma(xsg + 81920, 81920, 48);
    wait_vm<24>();
    stamp_t(20);
    lds_barrier();
    stamp_t(21);
    dma(ws + OFF_X1 + (size_t)(4 * m2 + te) * 32768 + nt * 4096, L_RES, 4);
    dma(ws + OFF_X1 + (size_t)(4 * m2 + tl) * 32768 + nt * 4096, L_RES + 4096, 4);
#pragma unroll
    for (int j = 0; j < 8; ++j) stage_wd(j);
    stage_wd(16);
    stage_wd(17);
    static_for<0, 10>(load_wr);
    *(float*)(smem + L_GB + 4 * lane) = rb(gather_sum<8>(R, OFF_GMB + 8 * (3 * DM + 64 * nt + lane), 16384, tag));
    drain_vm();
#pragma unroll
    for (int j = 0; j < 10; ++j) pin(wr[j]);
    stamp_t(22);
    lds_barrier();
    stamp_t(23);
    if (lane < 2) st4c(R, fao + lane * 1024 + fb * 64, tag);
#pragma unroll
    for (int j = 8; j < 12; ++j) stage_wd(j);
    __builtin_amdgcn_s_sleep(16);
    poll_flag(R, OFF_FA + m2 * 4096 + te * 1024 + (lane & 15) * 64, tag);
    static_for<0, 14>(load_ae);
    stamp_t(24);
    barrier();
    stamp_t(25);
    if (lane == 0) st4c(R, fao + 2 * 1024 + fb * 64, tag);
#pragma unroll
    for (int j = 12; j < 16; ++j) stage_wd(j);
    barrier();
    barrier();
    if (lane == 0) st4c(R, fao + 3 * 1024 + fb * 64, tag);
    __builtin_amdgcn_s_sleep(20);
    poll_flag(R, OFF_FA + m2 * 4096 + tl * 1024 + (lane & 15) * 64, tag);
    lds_set(smem, 6, 0);
    drain_vm();
    down_phase(std::integral_constant<int, 1>{});
  }
  }

  if (w < 7) {
    if (w >= 4) lds_wait<15>(smem, 2);
    else lds_wait<2>(smem, 4);
    lds_wait<1>(smem, 5);
    static_for<0, 32>(load_w);
    if (w >= 4) {
      drain_vm();
      if (w == 4) x1_arrive();
      STAMP(9);
      lds_wait<2>(smem, 4);
    }
    u32x4 aux;
    {
      u32x4 g[4];
#pragma unroll 1
      for (int spin = 0; spin < SPIN_CAP; ++spin) {
        u32 bad = 0;
#pragma unroll
        for (int i = 0; i < 4; ++i) g[i] = ld16c(R, OFF_AUX + (ft * 8 + 4 * hh + i) * 512 + (lane & 31) * 16);
#pragma unroll
        for (int i = 0; i < 4; ++i) bad |= (g[i][1] ^ tag) | (g[i][3] ^ tag);
        if (__all(bad == 0)) break;
        __builtin_amdgcn_s_sleep(2);
        MEMFENCE();
      }
      drain_vm();
      if (w < 4) lds_set(smem, 1, w);
#pragma unroll
      for (int j = 0; j < 32; ++j) pin(wf[j]);
      const float c4 = (__uint_as_float(g[0][0]) + __uint_as_float(g[1][0])) + (__uint_as_float(g[2][0]) + __uint_as_float(g[3][0]));
      const float s4 = (__uint_as_float(g[0][2]) + __uint_as_float(g[1][2])) + (__uint_as_float(g[2][2]) + __uint_as_float(g[3][2]));
      const float c = rs32(c4, c4), sf = rs32(s4, s4);
      const float c0 = rb(c), c1 = rb(c - c0), b0 = rb(sf), b1 = rb(sf - b0);
      aux = u32x4{pack_bf16(c0, c1), pack_bf16(c - c0 - c1, b0), pack_bf16(b1, sf - b0 - b1), 0};
      if (hh) aux = u32x4{0, 0, 0, 0};
    }
    STAMP(10);
    u32 xb[2][2];
#pragma unroll
    for (int i = 0; i < 2; ++i) {
      xb[i][0] = lds0 + 65536 * i + (8 * hh + q4) * 64 + 32 * blk + 8 * p4;
      xb[i][1] = lds0 + 65536 * i + (8 * hh + 4 + q4) * 64 + 32 * (blk ^ 1) + 8 * p4;
      pin(xb[i][0]);
      pin(xb[i][1]);
    }
    auto xfrag = [&](int s) { return tr_frag_at(xb[s >> 6][0] + (s & 63) * 1024, xb[s >> 6][1] + (s & 63) * 1024); };
    lds_wait<2>(smem, 6);
#pragma unroll
    for (int i = 0; i < 5; ++i) glds16(l16, ws + OFF_XS + (size_t)(4 * mt) * 32768 + (7 * i + w) * 1024, lds0 + (7 * i + w) * 1024);
    wait_vm<2>();
    barrier();
    STAMP(17);
    constexpr int XD = 3;
    bf16x8 xf[4];
#pragma unroll
    for (int s = 0; s < XD; ++s) xf[s] = xfrag(s);
    const char* actg = ws + OFF_ACT + ((size_t)(4 * mt) * NFT + ft) * 1024;
    f32x16 acc[2];
    f32x4 gc;
    float ev = 0.f, gu = 0.f, prev = 0.f;
    u32x4 o;
    auto epi_a = [&](int t, int i) {
      const float a = acc[t & 1][i];
      ev = __builtin_amdgcn_exp2f(a * __builtin_fmaf(a * a, gc[1], gc[0]));
      gu = a * acc[t & 1][8 + i];
      pin(ev);
      pin(gu);
    };
    auto epi_b = [&](int t, int i) {
      float v = gu * __builtin_amdgcn_rcpf(__builtin_fmaf(ev, gc[2], gc[2]));
      pin(v);
      if (i & 1) o[i >> 1] = pack_bf16(prev, v);
      else prev = v;
      if (i == 7) gstore16(o, l16, actg + (size_t)t * NFT * 1024);
    };
    auto fold = [&](int t) {
      const bf16x8 sf = *(const bf16x8*)(smem + L_SF + t * 512 + (lane & 31) * 16);
      gc = *(const f32x4*)(smem + L_RS + t * 512 + (lane & 31) * 16);
      acc[t & 1] = MFMA32(as_frag(aux), sf, acc[t & 1]);
    };
    static_for<0, 4>([&](auto T) {
      constexpr int t = decltype(T)::value;
      constexpr int e0 = t == 1 ? 10 : 3;
      static_for<0, 32>([&](auto KS) {
        constexpr int ks = decltype(KS)::value, s = 32 * t + ks;
        if constexpr (t == 0 && ks == 21 - XD) {
          wait_vm<0>();
          barrier();
        }
        if constexpr (t == 1 && ks == e0 - 3) lds_wait<1>(smem, 7);
        if constexpr (s + XD < 128) xf[(s + XD) & 3] = xfrag(s + XD);
        if constexpr (ks == 0) acc[t & 1] = MFMA32(as_frag(wf[0]), xf[s & 3], f32x16{});
        else acc[t & 1] = MFMA32(as_frag(wf[ks]), xf[s & 3], acc[t & 1]);
        pin(acc[t & 1]);
        if constexpr (t > 0) {
          if constexpr (ks == e0 - 3) fold(t - 1);
          if constexpr (ks >= e0 && ks < e0 + 16) {
            if constexpr (((ks - e0) & 1) == 0) epi_a(t - 1, (ks - e0) >> 1);
            else epi_b(t - 1, (ks - e0) >> 1);
          }
        }
        if constexpr (t == 1 && ks < 6) stage_wd(ks < 4 ? ks : 12 + ks);
        if constexpr (t >= 2 && ks >= 20 && ks < 28 && (ks & 1) == 0) stage_wd(4 * (t - 1) + (ks - 20) / 2);
        if constexpr (t >= 2 && ks < 18 && (ks & 3) == 0) load_wr(std::integral_constant<int, 5 * (t - 2) + ks / 4>{});
        __builtin_amdgcn_sched_barrier(0);
      });
      stamp_t(18 + 2 * t);
      if constexpr (t >= 2) wait_vm<4>();
      lds_barrier();
      stamp_t(19 + 2 * t);
    });
    fold(3);
#pragma unroll
    for (int i = 0; i < 8; i += 2) {
      float v[2];
#pragma unroll
      for (int e = 0; e < 2; ++e) {
        const float a = acc[1][i + e];
        const float ex = __builtin_amdgcn_exp2f(a * __builtin_fmaf(a * a, gc[1], gc[0]));
        v[e] = a * acc[1][8 + i + e] * __builtin_amdgcn_rcpf(__builtin_fmaf(ex, gc[2], gc[2]));
      }
      o[i >> 1] = pack_bf16(v[0], v[1]);
    }
    gstore16(o, l16, actg + (size_t)3 * NFT * 1024);
    STAMP(26);
    barrier();
#pragma unroll
    for (int j = 12; j < 16; ++j) stage_wd(j);
    static_for<0, 4>(load_ae);
    wait_vm<8>();
    STAMP(27);
    barrier();
    static_for<4, 14>(load_ae);
    wait_vm<14>();
#pragma unroll
    for (int j = 0; j < 10; ++j) pin(wr[j]);
    down_phase(std::integral_constant<int, 0>{});
  }
  DIAG_END(ws);
}

extern "C" void kernel_launch(void* const* d_in, const int* in_sizes, int n_in, void* d_out, int out_size, void* d_ws,
                              size_t ws_size, hipStream_t stream) {
  Params p;
  p.x = (const u16*)d_in[0];
  p.la = (const u16*)d_in[1];
  p.ai = (const u16*)d_in[2];
  p.cr = (const u16*)d_in[3];
  p.ci = (const u16*)d_in[4];
  p.ldt = (const u16*)d_in[5];
  p.dsk = (const u16*)d_in[6];
  p.wg = (const u16*)d_in[8];
  p.wu = (const u16*)d_in[9];
  p.wd = (const u16*)d_in[10];
  p.c = (const u16*)d_in[11];
  p.wm = (const u16*)d_in[12];
  p.out = (u16*)d_out;
  p.ws = (char*)d_ws;
  (void)hipFuncSetAttribute((const void*)fused_block, hipFuncAttributeMaxDynamicSharedMemorySize, SMEM);
  fused_block<<<256, 512, SMEM, stream>>>(p);
}
```

```cpp
#include <hip/hip_runtime.h>
#include <hip/hip_bf16.h>
#include <stdint.h>
#include <type_traits>

typedef unsigned short u16;
typedef unsigned int u32;
typedef unsigned long long u64;
typedef __attribute__((ext_vector_type(8))) __bf16 bf16x8;
typedef __attribute__((ext_vector_type(4))) __bf16 bf16x4;
typedef __attribute__((ext_vector_type(2))) __bf16 bf16x2;
typedef __attribute__((ext_vector_type(2))) _Float16 f16x2;
typedef __attribute__((ext_vector_type(8))) _Float16 f16x8;
typedef __attribute__((ext_vector_type(2))) float f32x2;
typedef __attribute__((ext_vector_type(4))) float f32x4;
typedef __attribute__((ext_vector_type(16))) float f32x16;
typedef __attribute__((ext_vector_type(4))) u32 u32x4;
typedef __attribute__((ext_vector_type(2))) u32 u32x2;
typedef __attribute__((address_space(3))) bf16x4* lds_bf16x4_p;
typedef __attribute__((address_space(3))) char* lds_char_p;
typedef __attribute__((address_space(3))) u32* lds_u32_p;
typedef __amdgpu_buffer_rsrc_t rsrc_t;

#define DEVI __device__ __forceinline__

constexpr int SL = 2048, DM = 512, NS = 128, DF = 1792;
constexpr int NFT = DF / 16;
constexpr int SPIN_CAP = 1 << 15;
#define GDELAY 2
constexpr int CODE_BYTES = 38 * 1024;

constexpr u32 OFF_E = 0;
constexpr u32 OFF_GST = 4096;
constexpr u32 OFF_FX = 32768;
constexpr u32 OFF_FW = 81920;
constexpr u32 OFF_FA = 131072;
constexpr u32 OFF_FM = 114688;
constexpr u32 OFF_GS2 = 196608;
constexpr u32 OFF_WD = 1u << 20;
constexpr u32 OFF_WP = OFF_WD + (2u << 20);
constexpr u32 OFF_X1 = OFF_WP + (4u << 20);
constexpr u32 OFF_ACT = OFF_X1 + (2u << 20);
constexpr u32 OFF_XG = OFF_ACT + (8u << 20);
constexpr u32 OFF_XS = OFF_XG + (4u << 20);
constexpr u32 OFF_GM = OFF_XS + (2u << 20);
constexpr u32 WS_USED = OFF_GM + 16 * 24576;

constexpr int KG_ROWB = 272;
constexpr int KG_WAVEB = 57 * KG_ROWB;
constexpr int L_KC = 4 * KG_WAVEB;
constexpr int CV_KCOPY = 4160;
constexpr int CV_K = 96576;
constexpr int CV_H = CV_K + 4 * CV_KCOPY;
constexpr int CV_HB = 2288 * 2;
constexpr int CV_X = CV_H + 2 * CV_HB;
constexpr int CV_P = 0;
constexpr int L_PARK = 33792;
constexpr int NPARK = 15;
static_assert(L_PARK + 4 * NPARK * 1024 <= CV_K, "parking");
static_assert(L_KC + 4 * 656 * 4 <= CV_K && CV_X + 8192 <= 131072, "front LDS map");
constexpr int L_TR = 131072;
constexpr int L_RES = 131072;
constexpr int L_SPARE = L_RES + 8192;
constexpr int L_SF = L_SPARE + 16384;
constexpr int L_RS = L_SF + 2048;
constexpr int L_V = L_RS;
constexpr int L_GB = L_RS + 2048;
constexpr int L_SYNC = L_GB + 256;
constexpr int L_EP = L_SYNC + 192;
constexpr int SMEM = L_EP + 16;

#define DIAG_DECL
#define STAMP(i)
#define RSTAMP(i)
#define DIAG_END(wsp)

template <int I, int N, class F>
DEVI void static_for(F&& f) {
  if constexpr (I < N) {
    f(std::integral_constant<int, I>{});
    static_for<I + 1, N>(f);
  }
}

DEVI float bflo(u32 v) { return __uint_as_float(v << 16); }
DEVI float bfhi(u32 v) { return __uint_as_float(v & 0xffff0000u); }
DEVI float b2f(u16 v) { return __uint_as_float(((u32)v) << 16); }
DEVI u32 pack_bf16(float a, float b) {
  f32x2 v = {a, b};
  return __builtin_bit_cast(u32, __builtin_convertvector(v, bf16x2));
}
DEVI float rb(float a) { return bflo(pack_bf16(a, a)); }
DEVI u32 pack_f16(f32x2 v) { return __builtin_bit_cast(u32, __builtin_convertvector(v, f16x2)); }
DEVI u32 perm_lo(u32 a, u32 b) { return __builtin_amdgcn_perm(b, a, 0x05040100u); }
DEVI u32 perm_hi(u32 a, u32 b) { return __builtin_amdgcn_perm(b, a, 0x07060302u); }
DEVI float dot2(u32 a, u32 b, float c) {
  return __builtin_amdgcn_fdot2_f32_bf16(__builtin_bit_cast(bf16x2, a), __builtin_bit_cast(bf16x2, b), c, false);
}
constexpr u32 ONES2 = 0x3f803f80u;

template <int CTRL>
DEVI float dpp(float v) { return __int_as_float(__builtin_amdgcn_update_dpp(0, __float_as_int(v), CTRL, 0xf, 0xf, true)); }
DEVI float row_sum(float v) {
  v += dpp<0xB1>(v); v += dpp<0x4E>(v); v += dpp<0x141>(v); v += dpp<0x140>(v);
  return v;
}
DEVI float rs32(float a, float b) {
  auto r = __builtin_amdgcn_permlane32_swap(__float_as_uint(a), __float_as_uint(b), false, false);
  return __uint_as_float(r[0]) + __uint_as_float(r[1]);
}
DEVI float rs16(float a, float b) {
  auto r = __builtin_amdgcn_permlane16_swap(__float_as_uint(a), __float_as_uint(b), false, false);
  return __uint_as_float(r[0]) + __uint_as_float(r[1]);
}
DEVI float wave_max(float v) {
  v = fmaxf(v, dpp<0xB1>(v)); v = fmaxf(v, dpp<0x4E>(v)); v = fmaxf(v, dpp<0x141>(v)); v = fmaxf(v, dpp<0x140>(v));
  auto r = __builtin_amdgcn_permlane16_swap(__float_as_uint(v), __float_as_uint(v), false, false);
  v = fmaxf(__uint_as_float(r[0]), __uint_as_float(r[1]));
  auto s = __builtin_amdgcn_permlane32_swap(__float_as_uint(v), __float_as_uint(v), false, false);
  return fmaxf(__uint_as_float(s[0]), __uint_as_float(s[1]));
}

DEVI f32x2 cmul(f32x2 a, f32x2 b) {
  const f32x2 t = a.yy * f32x2{-b.y, b.x};
  return __builtin_elementwise_fma(a.xx, b, t);
}
DEVI f32x2 cexpk(float zr, float zi, float k) {
  const float mag = __builtin_amdgcn_exp2f(zr * (k * 1.4426950408889634f));
  const float rev = __builtin_amdgcn_fractf(zi * (k * 0.15915494309189535f));
  return f32x2{mag * __builtin_amdgcn_cosf(rev), mag * __builtin_amdgcn_sinf(rev)};
}

DEVI u32x4 ld16c(rsrc_t r, u32 off) { return __builtin_amdgcn_raw_buffer_load_b128(r, off, 0, 16); }
DEVI u32x2 ld8c(rsrc_t r, u32 off) { return __builtin_amdgcn_raw_buffer_load_b64(r, off, 0, 16); }
DEVI u32 ld4c(rsrc_t r, u32 off) { return __builtin_amdgcn_raw_buffer_load_b32(r, off, 0, 16); }
DEVI void st16c(rsrc_t r, u32 off, u32x4 v) { __builtin_amdgcn_raw_buffer_store_b128(v, r, off, 0, 16); }
DEVI void st8c(rsrc_t r, u32 off, u32x2 v) { __builtin_amdgcn_raw_buffer_store_b64(v, r, off, 0, 16); }
DEVI void st4c(rsrc_t r, u32 off, u32 v) { __builtin_amdgcn_raw_buffer_store_b32(v, r, off, 0, 16); }
#define MEMFENCE() asm volatile("" ::: "memory")
DEVI void drain_vm() {
  __builtin_amdgcn_s_waitcnt(0x0F70);
  asm volatile("s_waitcnt vmcnt(0)" ::: "memory");
}

template <int N>
DEVI float gather_sum(rsrc_t r, u32 off, u32 stride, u32 tag) {
  float v = 0.f;
#pragma unroll 1
  for (int spin = 0; spin < SPIN_CAP; ++spin) {
    u32x2 g[N];
#pragma unroll
    for (int i = 0; i < N; ++i) g[i] = ld8c(r, off + i * stride);
    u32 bad = 0;
    v = 0.f;
#pragma unroll
    for (int i = 0; i < N; ++i) {
      v += __uint_as_float(g[i][0]);
      bad |= g[i][1] ^ tag;
    }
    if (__all(bad == 0)) break;
    __builtin_amdgcn_s_sleep(2);
    MEMFENCE();
  }
  MEMFENCE();
  return v;
}
DEVI float sum_hi4(float v) {
  v += dpp<0x124>(v);
  v += dpp<0x128>(v);
  v = rs16(v, v);
  return rs32(v, v);
}
DEVI u32 silu2(u32 c) {
  const float a = bflo(c), b = bfhi(c);
  return pack_bf16(a * __builtin_amdgcn_rcpf(1.f + __expf(-a)), b * __builtin_amdgcn_rcpf(1.f + __expf(-b)));
}
DEVI float rdlane(float v, int l) { return __int_as_float(__builtin_amdgcn_readlane(__float_as_int(v), l)); }
DEVI void poll_flag(rsrc_t r, u32 off, u32 tag) {
#pragma unroll 1
  for (int spin = 0; spin < SPIN_CAP; ++spin) {
    const u32 f = ld4c(r, off);
    if (__all(f == tag)) break;
    __builtin_amdgcn_s_sleep(1);
    MEMFENCE();
  }
  MEMFENCE();
}

DEVI void lds_set(char* smem, int grp, int word) {
  MEMFENCE();
  *(__attribute__((address_space(3))) volatile u32*)(smem + L_SYNC + 16 * grp + 4 * word) = 1u;
  MEMFENCE();
}
DEVI u32 lds_arrive(char* smem, int grp) {
  u32 old;
  const u32 addr = (u32)(uintptr_t)(lds_char_p)(smem + L_SYNC + 16 * grp);
  asm volatile("s_mov_b64 exec, 1\n\tds_add_rtn_u32 %0, %1, %2\n\ts_mov_b64 exec, -1\n\ts_waitcnt lgkmcnt(0)"
               : "=&v"(old) : "v"(addr), "v"(1u) : "memory");
  return (u32)__builtin_amdgcn_readfirstlane((int)old);
}
template <int MASK>
DEVI void lds_wait(char* smem, int grp) {
  typedef __attribute__((address_space(3))) volatile u32x4* lds_u32x4_p;
#pragma unroll 1
  for (int spin = 0; spin < (1 << 22); ++spin) {
    MEMFENCE();
    const u32x4 v = *(lds_u32x4_p)(smem + L_SYNC + 16 * grp);
    const u32 a = ((MASK & 1) ? v[0] : 1u) & ((MASK & 2) ? v[1] : 1u) & ((MASK & 4) ? v[2] : 1u) & ((MASK & 8) ? v[3] : 1u);
    if (__builtin_amdgcn_readfirstlane((int)a)) break;
    __builtin_amdgcn_s_sleep(1);
  }
  MEMFENCE();
}

DEVI void glds16(u32 voff, const void* sbase, u32 ldsaddr) {
  asm volatile("s_mov_b32 m0, %2\n\ts_nop 0\n\tglobal_load_lds_dwordx4 %0, %1 sc1" ::"v"(voff), "s"(sbase), "s"(ldsaddr) : "memory");
}
template <int N>
DEVI void wait_vm() { asm volatile("s_waitcnt vmcnt(%0)" ::"n"(N) : "memory"); }
template <int N>
DEVI void wait_vm_reg(u32x4& r) { asm volatile("s_waitcnt vmcnt(%1)" : "+v"(r) : "n"(N)); }
template <class T>
DEVI void pin(T& r) { asm volatile("" : "+v"(r)); }
template <int OFF>
DEVI void gload16(u32x4& r, u32 voff, const void* sbase) {
  asm volatile("global_load_dwordx4 %0, %1, %2 offset:%3 sc1" : "=v"(r) : "v"(voff), "s"(sbase), "n"(OFF) : "memory");
}
DEVI void gstore16(u32x4 v, u32 voff, const void* sbase) {
  asm volatile("global_store_dwordx4 %0, %1, %2 sc1\n\ts_nop 1" ::"v"(voff), "v"(v), "s"(sbase) : "memory");
}
DEVI void barrier() {
  MEMFENCE();
  __builtin_amdgcn_s_barrier();
  MEMFENCE();
}
DEVI void lds_barrier() {
  asm volatile("s_waitcnt lgkmcnt(0)" ::: "memory");
  __builtin_amdgcn_s_barrier();
  MEMFENCE();
}
DEVI bf16x4 tr_read(const char* p) { return __builtin_amdgcn_ds_read_tr16_b64_v4bf16((lds_bf16x4_p)(p)); }
DEVI bf16x8 tr_frag(const char* p0, const char* p1) {
  return __builtin_shufflevector(tr_read(p0), tr_read(p1), 0, 1, 2, 3, 4, 5, 6, 7);
}
DEVI bf16x8 tr_frag_at(u32 a0, u32 a1) {
  return __builtin_shufflevector(__builtin_amdgcn_ds_read_tr16_b64_v4bf16((lds_bf16x4_p)(uintptr_t)a0),
                                 __builtin_amdgcn_ds_read_tr16_b64_v4bf16((lds_bf16x4_p)(uintptr_t)a1), 0, 1, 2, 3, 4, 5, 6, 7);
}
DEVI bf16x8 as_frag(u32x4 v) { return __builtin_bit_cast(bf16x8, v); }
#define MFMA32(a, b, c) __builtin_amdgcn_mfma_f32_32x32x16_bf16(a, b, c, 0, 0, 0)
#define MFMA16(a, b, c) __builtin_amdgcn_mfma_f32_16x16x32_bf16(a, b, c, 0, 0, 0)

DEVI void transpose8(const u32x4 (&r)[8], u32x4 (&o)[8]) {
#pragma unroll
  for (int k = 0; k < 8; ++k)
#pragma unroll
    for (int i = 0; i < 4; ++i)
      o[k][i] = (k & 1) ? perm_hi(r[2 * i][k >> 1], r[2 * i + 1][k >> 1]) : perm_lo(r[2 * i][k >> 1], r[2 * i + 1][k >> 1]);
}
DEVI u32 ln_pack(float s, float q) {
  const float mean = s * (1.f / DM);
  const float var = fmaxf(q * (1.f / DM) - mean * mean, 0.f);
  return pack_bf16(mean, __builtin_amdgcn_sqrtf(rb(rb(var) + 1e-5f)));
}

template <int NP>
DEVI void mod_part(const u32x4 (&wv)[8], const u32 (&sl)[NP], rsrc_t r, u32 dst, u32 tag, int lane) {
  float acc[8] = {};
#pragma unroll
  for (int j = 0; j < NP; ++j)
#pragma unroll
    for (int k = 0; k < 4; ++k) {
      acc[2 * k] = dot2(perm_lo(wv[2 * j][k], wv[2 * j + 1][k]), sl[j], acc[2 * k]);
      acc[2 * k + 1] = dot2(perm_hi(wv[2 * j][k], wv[2 * j + 1][k]), sl[j], acc[2 * k + 1]);
    }
#pragma unroll
  for (int i = 0; i < 4; ++i) acc[i] = rs32(acc[i], acc[i + 4]);
#pragma unroll
  for (int i = 0; i < 2; ++i) {
    acc[i] = rs16(acc[i], acc[i + 2]);
    acc[i] += dpp<0x128>(acc[i]);
  }
  if ((lane & 8) == 0)
    st16c(r, dst + 8 * (8 * (lane & 7) + 2 * (lane >> 4)), u32x4{__float_as_uint(acc[0]), tag, __float_as_uint(acc[1]), tag});
}

struct Params {
  const u16 *x, *la, *ai, *cr, *ci, *ldt, *dsk, *wg, *wu, *wd, *c, *wm;
  u16* out;
  char* ws;
};

__global__ __launch_bounds__(512, 2) void fused_block(Params P) {
  extern __shared__ __attribute__((aligned(16))) char smem[];
  DIAG_DECL
  const int tid = threadIdx.x, lane = tid & 63;
  const int w = __builtin_amdgcn_readfirstlane(tid >> 6);
  const int b = blockIdx.x, xcd = b & 7, bi = b >> 3;
  char* const ws = P.ws;
  const rsrc_t R = __builtin_amdgcn_make_buffer_rsrc(ws, 0, WS_USED, 0x00020000);
  const int mt = 8 * (xcd & 1) + (bi & 7), fb = 4 * (xcd >> 1) + (bi >> 3);
  const int m2 = 2 * xcd + (bi & 1), nt = bi >> 2, te = (bi >> 1) & 1, tl = 3 - te;
  const int ft = 7 * fb + w;
  const u32 l16 = lane * 16;
  const char* wsrc = ws + OFF_WP + (size_t)ft * 32768;
  u32x4 wf[32];
  u32 code_touch = 0;
  {
    const __attribute__((address_space(1))) char* pc;
    asm volatile("s_getpc_b64 %0" : "=s"(pc));
    if (w >= 6)
      code_touch = *(const __attribute__((address_space(1))) u32*)(pc + min((2 * (bi % 3) + (w & 1)) * 8192 + lane * 128, CODE_BYTES));
  }
  if (tid < 48) *(u32*)(smem + L_SYNC + 4 * tid) = 0;
  STAMP(32);
  u32x4 pl[8];
  u32 ps[8];
  if (w < 4) {
    const int d = 64 * xcd + 2 * bi + (w >> 1), n = 64 * (w & 1) + lane;
    ps[0] = P.la[d * NS + n];
    ps[1] = P.ai[d * NS + n];
    ps[2] = P.cr[d * NS + n];
    ps[3] = P.ci[d * NS + n];
    ps[4] = P.cr[d * NS + (n ^ 64)];
    ps[5] = P.ci[d * NS + (n ^ 64)];
    ps[6] = P.ldt[d];
    ps[7] = P.dsk[(d & ~1) + ((lane >> 2) & 1)];
  } else if (w == 4) {
#pragma unroll
    for (int i = 0; i < 8; ++i) pl[i] = *(const u32x4*)(P.x + (size_t)(8 * b + 2 * (lane & 3) + (i & 1)) * DM + 8 * (lane >> 2) + 128 * (i >> 1));
  } else {
    const int ra = 32 * (b & 15) + 4 * (lane >> 3);
    const u32x2 cv = *(const u32x2*)(P.c + ra);
    ps[0] = cv[0];
    ps[1] = cv[1];
#pragma unroll
    for (int i = 0; i < 4; ++i) pl[i] = *(const u32x4*)(P.wm + (size_t)(ra + i) * 3072 + 1024 * (w - 5) + 64 * (b >> 4) + 8 * (lane & 7));
  }
  u32 tag;
  asm volatile("s_load_dword %0, %1, 0x0\n\ts_waitcnt lgkmcnt(0)" : "=s"(tag) : "s"(ws + OFF_E) : "memory");
  tag += 0x9E3779B9u;
  STAMP(34);
  barrier();
  STAMP(35);
  auto load_w = [&](auto J) {
    constexpr int j = decltype(J)::value;
    gload16<(j & 3) * 1024>(wf[j], l16, wsrc + (j >> 2) * 4096);
  };

  const u32 lds0 = (u32)(uintptr_t)(lds_char_p)smem;
  const char* wdg = ws + OFF_WD + ((size_t)(2 * nt) * NFT + 14 * w) * 1024;
  const char* ag = ws + OFF_ACT + ((size_t)(4 * m2) * NFT + 14 * w) * 1024;
  auto stage_lds = [&](int j) { return j < 16 ? (j >> 2) * 32768 + w * 4096 + (j & 3) * 1024 : L_SPARE + (j - 16) * 8192 + w * 1024; };
  auto stage_wd = [&](int j) { glds16(l16, wdg + (size_t)(j & 1) * NFT * 1024 + (j >> 1) * 1024, lds0 + stage_lds(j)); };
  auto stamp_t = [&](int i) { STAMP(i); };
  const int q4 = (lane & 15) >> 2, p4 = lane & 3, hh = lane >> 5, blk = (lane >> 4) & 1;
  u32x4 wr[10], ae[14];
  auto load_wr = [&](auto J) {
    constexpr int j = 18 + decltype(J)::value;
    gload16<0>(wr[j - 18], l16, wdg + (size_t)(j & 1) * NFT * 1024 + (j >> 1) * 1024);
  };
  auto load_ae = [&](auto J) {
    constexpr int j = decltype(J)::value;
    gload16<(j & 3) * 1024>(ae[j], l16, ag + (size_t)te * NFT * 1024 + (j >> 2) * 4096);
  };

  auto down_phase = [&](auto) {
    stamp_t(28);
    bf16x8 wd[28];
#pragma unroll
    for (int j = 0; j < 28; ++j) wd[j] = j < 18 ? *(const bf16x8*)(smem + stage_lds(j) + l16) : as_frag(wr[j - 18]);
    auto put_partial = [&](int qd, const f32x16& a) {
#pragma unroll
      for (int g = 0; g < 4; ++g)
        *(f32x4*)(smem + qd * 32768 + w * 4096 + g * 1024 + l16) = f32x4{a[4 * g], a[4 * g + 1], a[4 * g + 2], a[4 * g + 3]};
    };
    u32x4 al[14];
    auto load_al = [&](auto J) {
      constexpr int j = decltype(J)::value;
      gload16<(j & 3) * 1024>(al[j], l16, ag + (size_t)tl * NFT * 1024 + (j >> 2) * 4096);
    };
    f32x16 a0 = {}, a1 = {};
    static_for<0, 14>([&](auto J) {
      constexpr int j = decltype(J)::value;
      wait_vm_reg<13 - j>(ae[j]);
      a0 = MFMA32(wd[2 * j], as_frag(ae[j]), a0);
      a1 = MFMA32(wd[2 * j + 1], as_frag(ae[j]), a1);
    });
    pin(a0);
    pin(a1);
    stamp_t(13);
    lds_wait<1>(smem, 6);
    stamp_t(29);
    static_for<0, 14>(load_al);
    put_partial(0, a0);
    put_partial(2, a1);
    a0 = f32x16{};
    a1 = f32x16{};
    static_for<0, 14>([&](auto J) {
      constexpr int j = decltype(J)::value;
      wait_vm_reg<13 - j>(al[j]);
      a0 = MFMA32(wd[2 * j], as_frag(al[j]), a0);
      a1 = MFMA32(wd[2 * j + 1], as_frag(al[j]), a1);
    });
    stamp_t(14);
    put_partial(1, a0);
    put_partial(3, a1);
    stamp_t(15);
    lds_barrier();
    stamp_t(30);
    const int qi = w >> 2, qj = (w >> 1) & 1, g0 = 2 * (w & 1);
    u32x2 o[2];
#pragma unroll
    for (int gg = 0; gg < 2; ++gg) {
      f32x4 s = {0.f, 0.f, 0.f, 0.f};
#pragma unroll
      for (int v = 0; v < 8; ++v) s += *(const f32x4*)(smem + (w >> 1) * 32768 + v * 4096 + (g0 + gg) * 1024 + l16);
      const int cl = 32 * qi + 8 * (g0 + gg) + 4 * hh;
      const f32x4 gb = *(const f32x4*)(smem + L_GB + 4 * cl);
      const u32x2 xr = __builtin_bit_cast(u32x2, tr_read(smem + L_RES + qj * 4096 + (cl + q4) * 64 + 32 * (blk ^ hh) + 8 * p4));
#pragma unroll
      for (int e = 0; e < 2; ++e) {
        o[gg][e] = pack_bf16(bflo(xr[e]) + gb[2 * e] * s[2 * e], bfhi(xr[e]) + gb[2 * e + 1] * s[2 * e + 1]);
      }
    }
    auto s0 = __builtin_amdgcn_permlane32_swap(o[0][0], o[1][0], false, false);
    auto s1 = __builtin_amdgcn_permlane32_swap(o[0][1], o[1][1], false, false);
    const int tok = 128 * m2 + 32 * (qj ? tl : te) + (lane & 31);
    *(u32x4*)(P.out + (size_t)tok * DM + 64 * nt + 32 * qi + 8 * g0 + 8 * hh) = u32x4{s0[0], s1[0], s0[1], s1[1]};
  };

  auto x1_arrive = [&]() {
    if (lds_arrive(smem, 4) == 4) {
      if (lane < 8) st4c(R, OFF_FX + 4096 * lane + 16 * b, tag);
      lds_set(smem, 4, 1);
    }
  };

  if (w < 4) {
    const int ch = w >> 1, half = w & 1;
    const int c0 = 64 * xcd + 2 * bi;
    const int n = 64 * half + lane;
    int kidx[9];
#pragma unroll
    for (int e = 0; e < 5; ++e) {
      const int l0 = 1023 - 8 * n - 2 * e, l1 = l0 - 1;
      const u32 pb = pack_bf16((float)l0, (float)l1);
      kidx[2 * e] = L_KC + 4 * (w * 656 + ((l0 < 128) ? max(l0, 0) : (int)(pb & 0xffffu) - 133 * 128));
      if (e < 4) kidx[2 * e + 1] = L_KC + 4 * (w * 656 + ((l1 < 128) ? l1 : (int)(pb >> 16) - 133 * 128));
    }
#pragma unroll
    for (int e = 0; e < 9; ++e) pin(kidx[e]);
    if (lane < 2) *(u32x4*)(smem + CV_K + w * CV_KCOPY + 4096 + lane * 16) = u32x4{0, 0, 0, 0};
#pragma unroll
    for (int i = 0; i < 8; ++i) pin(ps[i]);
    const float v_cr = bflo(ps[2]), v_ci = bflo(ps[3]), o_cr = bflo(ps[4]), o_ci = bflo(ps[5]);
    const float dt = rb(expf(bflo(ps[6])));
    const float zr = -rb(expf(bflo(ps[0]))) * dt, zi = bflo(ps[1]) * dt;
    STAMP(3);
    const float mx = wave_max(fmaxf(fmaxf(fabsf(v_cr), fabsf(v_ci)), fmaxf(fabsf(o_cr), fabsf(o_ci))));
    const int ex = min(max((int)(__float_as_uint(mx) >> 23), 1), 252);
    const float sdn = __uint_as_float((u32)(254 - ex) << 23);
    const float sup = __uint_as_float((u32)(ex + 1) << 23) * dt;
    {
      char* base = smem + w * KG_WAVEB + lane * 4;
      auto put = [&](int row, f32x2 v) { *(u32*)(base + row * KG_ROWB) = pack_f16(v); };
      const f32x2 e8 = cexpk(zr, zi, 8.f), e16 = cexpk(zr, zi, 16.f);
      const f32x2 e1 = cexpk(zr, -zi, 1.f), e2 = cexpk(zr, -zi, 2.f);
      const f32x2 e128 = cexpk(zr, -zi, 128.f), e256 = cexpk(zr, -zi, 256.f);
      f32x2 f0 = e16, f1 = e8;
      put(41, f32x2{1.f, 0.f});
      put(42, e8);
#pragma unroll
      for (int j = 2; j < 16; j += 2) {
        f1 = cmul(f1, e16);
        put(41 + j, f0);
        put(42 + j, f1);
        if (j < 14) f0 = cmul(f0, e16);
      }
      const f32x2 e4 = cmul(e2, e2);
      f32x2 g[2];
      g[0] = f32x2{v_cr * sdn, -v_ci * sdn};
      g[1] = cmul(g[0], e128);
#pragma unroll
      for (int i = 0; i < 17; ++i) {
        const f32x2 gi = g[i & 1];
        if (i < 2) {
          f32x2 a = gi, c = cmul(gi, e1);
#pragma unroll
          for (int e = 0; e < 8; e += 2) {
            put(8 * i + e, a);
            put(8 * i + e + 1, c);
            if (e < 6) { a = cmul(a, e2); c = cmul(c, e2); }
          }
        } else if (i < 4) {
          const f32x2 c = cmul(gi, e2);
          put(16 + 4 * (i - 2), gi);
          put(17 + 4 * (i - 2), c);
          put(18 + 4 * (i - 2), cmul(gi, e4));
          put(19 + 4 * (i - 2), cmul(c, e4));
        } else if (i < 8) {
          put(24 + 2 * (i - 4), gi);
          put(25 + 2 * (i - 4), cmul(gi, e4));
        } else {
          put(32 + (i - 8), gi);
        }
        if (i + 2 < 17) g[i & 1] = cmul(gi, e256);
      }
    }
    lds_set(smem, 0, w);
    STAMP(4);
    const int r = lane & 15, q = lane >> 4;
    {
      f32x4 acc[3] = {};
#pragma unroll
      for (int hf = 0; hf < 2; ++hf) {
        if (hf == 0) {
          if (ch) lds_wait<12>(smem, 0);
          else lds_wait<3>(smem, 0);
        }
        const char* rbase = smem + (2 * ch + hf) * KG_WAVEB + r * KG_ROWB + q * 16;
        f16x8 fr[4][4];
#pragma unroll
        for (int ks = 0; ks < 4; ++ks) {
          fr[ks][3] = *(const f16x8*)(rbase + ks * 64 + 41 * KG_ROWB);
#pragma unroll
          for (int t = 0; t < 3; ++t) fr[ks][t] = *(const f16x8*)(rbase + ks * 64 + 16 * t * KG_ROWB);
        }
        __builtin_amdgcn_sched_barrier(0);
#pragma unroll
        for (int ks = 0; ks < 4; ++ks)
#pragma unroll
          for (int t = 0; t < 3; ++t) acc[t] = __builtin_amdgcn_mfma_f32_16x16x32_f16(fr[ks][t], fr[ks][3], acc[t], 0, 0, 0);
      }
      pin(acc[0]); pin(acc[1]); pin(acc[2]);
      STAMP(12);
      float* kc = (float*)(smem + L_KC) + w * 656;
      *(f32x4*)(kc + 128 * (q >> 1) + 4 * (q & 1) + 8 * r) = acc[0] * sup;
      if (q < 2) {
        *(f32x4*)(kc + 256 + 64 * q + 4 * r) = acc[1] * sup;
      } else {
        *(f32x2*)(kc + 384 + 64 * (q - 2) + 2 * r) = f32x2{acc[1][0], acc[1][1]} * sup;
        *(f32x2*)(kc + 416 + 64 * (q - 2) + 2 * r) = f32x2{acc[1][2], acc[1][3]} * sup;
      }
#pragma unroll
      for (int e = 0; e < 4; ++e)
        if (4 * q + e <= 8) kc[512 + 16 * (4 * q + e) + r] = acc[2][e] * sup;
      MEMFENCE();
      float kv[9];
#pragma unroll
      for (int e = 0; e < 9; ++e) kv[e] = *(const float*)(smem + kidx[e]);
      const float ka = kc[639 - n], kb = kc[640 - n];
      if (n == 127) kv[8] = 0.f;
      const float kt = (n & 1) ? ka : kb;
      const u32 aa = pack_bf16(ka, ka), bb = pack_bf16(kb, kb);
      *(u32x4*)(smem + CV_K + (2 * ch) * CV_KCOPY + 16 * n) = u32x4{bb, pack_bf16(kb, kt), aa, aa};
      *(u32x4*)(smem + CV_K + (2 * ch + 1) * CV_KCOPY + 16 * n) = u32x4{bb, pack_bf16(kt, ka), aa, aa};
      u32x4 o, s;
#pragma unroll
      for (int e = 0; e < 4; ++e) {
        o[e] = pack_bf16(kv[2 * e], kv[2 * e + 1]);
        s[e] = pack_bf16(kv[2 * e + 1], kv[2 * e + 2]);
      }
      *(u32x4*)(smem + CV_K + (2 * ch) * CV_KCOPY + 2048 + 16 * n) = o;
      *(u32x4*)(smem + CV_K + (2 * ch + 1) * CV_KCOPY + 2048 + 16 * n) = s;
    }
    lds_set(smem, 8, w);
    STAMP(5);
    lds_wait<15>(smem, 8);
    lds_wait<3>(smem, 2);
    STAMP(7);
    const char* abase = smem + CV_K + (2 * ch + ((r & 1) ^ 1)) * CV_KCOPY + 2 * (2030 - (r & ~1) + 8 * q) - 64 * (4 * half + 3 + 56);
    const char* bbase = smem + CV_H + ch * CV_HB + 2 * (224 + 16 * r + 8 * q) - 64 * (4 * half + 3);
    f32x4 acc[8] = {};
    bf16x8 af[2][8], bfr[2][8];
    u32 ab[4];
#pragma unroll
    for (int i = 0; i < 4; ++i) {
      ab[i] = (u32)(uintptr_t)(lds_char_p)abase + 1024 * i;
      pin(ab[i]);
    }
    auto ldfr = [&](int rho, int i) {
      const lds_u32_p p = (lds_u32_p)(uintptr_t)(ab[(7 - i) >> 1] + 64 * (3 - rho) + 512 * ((7 - i) & 1));
      af[rho & 1][i] = as_frag(u32x4{p[0], p[1], p[2], p[3]});
      bfr[rho & 1][i] = *(const bf16x8*)(bbase + 64 * (3 - rho) + 512 * i);
    };
#pragma unroll
    for (int i = 0; i < 4; ++i) ldfr(0, i);
    static_for<0, 32>([&](auto S) {
      constexpr int st = decltype(S)::value, rho = st >> 3, m = st & 7;
      if constexpr (st + 4 < 32) ldfr((st + 4) >> 3, (st + 4) & 7);
#pragma unroll
      for (int o = 0; o <= m; ++o) {
        if (m + o < 8) {
          acc[m + o] = MFMA16(af[rho & 1][m], bfr[rho & 1][o], acc[m + o]);
          if (o != m) acc[m + o] = MFMA16(af[rho & 1][o], bfr[rho & 1][m], acc[m + o]);
        }
      }
#pragma unroll
      for (int i = 0; i < 3; ++i) {
        __builtin_amdgcn_sched_group_barrier(0x008, 1, 0);
        __builtin_amdgcn_sched_group_barrier(0x100, 1, 0);
      }
      __builtin_amdgcn_sched_barrier(0);
    });
    STAMP(8);
#pragma unroll
    for (int t = 0; t < 8; ++t) *(f32x4*)(smem + CV_P + w * 8192 + ch * 16 + 1024 * t + 64 * r + 16 * q) = acc[t];
    lds_set(smem, 3, w);
    {
      const int c = (lane >> 2) & 1;
      lds_wait<15>(smem, 2);
      const float ga = *(const float*)(smem + L_EP + 4 * c), cb1 = *(const float*)(smem + L_EP + 8 + 4 * c), dk = bflo(ps[7]);
      lds_wait<15>(smem, 3);
      STAMP(11);
#pragma unroll
      for (int i = 0; i < 2; ++i) {
        const int e = tid + 256 * i;
        const int l0 = 32 * (e >> 3) + 8 * ((e & 3) ^ (2 * ((c0 >> 2) & 1)));
        const char* pp = smem + CV_P + c * (16384 + 16) + 4 * l0;
        const f32x4 y0 = *(const f32x4*)(pp) + *(const f32x4*)(pp + 8192);
        const f32x4 y1 = *(const f32x4*)(pp + 16) + *(const f32x4*)(pp + 8192 + 16);
        const u32x4 hv = *(const u32x4*)(smem + CV_H + c * CV_HB + 480 + 2 * l0);
        const u32x4 xx = *(const u32x4*)(smem + CV_X + c * 4096 + 2 * l0);
        u32x4 o, os;
#pragma unroll
        for (int k = 0; k < 4; ++k) {
          const u32 yb = k < 2 ? pack_bf16(y0[2 * k], y0[2 * k + 1]) : pack_bf16(y1[2 * k - 4], y1[2 * k - 3]);
          const u32 hd = pack_bf16(bflo(hv[k]) * dk, bfhi(hv[k]) * dk);
          const u32 z = pack_bf16(bflo(yb) + bflo(hd), bfhi(yb) + bfhi(hd));
          const u32 gz = pack_bf16(ga * bflo(z), ga * bfhi(z));
          o[k] = pack_bf16(bflo(xx[k]) + bflo(gz), bfhi(xx[k]) + bfhi(gz));
          os[k] = pack_bf16(bflo(o[k]) * cb1, bfhi(o[k]) * cb1);
        }
        const u32 go = (e >> 3) * 32768 + c0 * 64 + (e & 7) * 16;
        st16c(R, OFF_XS + go, os);
        st16c(R, OFF_X1 + go, o);
      }
      STAMP(16);
      drain_vm();
      x1_arrive();
      STAMP(9);
    }
  } else {
    __builtin_amdgcn_s_setprio(2);
    const int un = min(b, 223), cg = un >> 3, type = (un >> 2) & 1, k8 = 2 * (un & 3) + (w & 1);
    const int kg = lane >> 3, cp = lane & 7;
    auto mod_job = [&]() {
      pin(ps[0]);
      pin(ps[1]);
      const u32 sl[2] = {silu2(ps[0]), silu2(ps[1])};
#pragma unroll
      for (int i = 0; i < 4; ++i) pin(pl[i]);
      mod_part<2>(pl, sl, R, OFF_GM + (b & 15) * 24576 + 8192 * (w - 5) + 512 * (b >> 4), tag, lane);
      stamp_t(33);
    };
    if (w < 6) {
      if (w == 4) {
#pragma unroll
        for (int a = 0; a < 4; ++a) {
          pin(pl[2 * a]);
          pin(pl[2 * a + 1]);
#pragma unroll
          for (int k = 0; k < 4; ++k)
            st16c(R, OFF_XG + b * 16384 + (a * 4 + k) * 1024 + l16, u32x4{pl[2 * a][k], tag, pl[2 * a + 1][k], tag});
        }
        STAMP(3);
        float s[2][2] = {}, qq[2][2] = {};
#pragma unroll
        for (int i = 0; i < 8; ++i)
#pragma unroll
          for (int k = 0; k < 4; ++k) {
            s[i & 1][k & 1] = dot2(pl[i][k], ONES2, s[i & 1][k & 1]);
            qq[i & 1][k & 1] = dot2(pl[i][k], pl[i][k], qq[i & 1][k & 1]);
          }
        float v = rs16(rs32(s[0][0] + s[0][1], qq[0][0] + qq[0][1]), rs32(s[1][0] + s[1][1], qq[1][0] + qq[1][1]));
        v += dpp<0x124>(v);
        v += dpp<0x128>(v);
        auto sw = __builtin_amdgcn_permlane32_swap(__float_as_uint(v), __float_as_uint(v), false, false);
        const u32 pk = ln_pack(__uint_as_float(sw[0]), __uint_as_float(sw[1]));
        if (lane < 32 && (lane & 15) < 4) st8c(R, OFF_GST + 64 * b + 16 * (lane & 3) + 8 * (lane >> 4), u32x2{pk, tag});
        STAMP(33);
        drain_vm();
        lds_set(smem, 5, 1);
        RSTAMP(36);
      } else {
        mod_job();
      }
      const int t = tid - 256, c0 = 64 * xcd + 2 * bi, pr = c0 >> 1;
      const u32 xo = OFF_XG + (t >> 2) * 16384 + (((pr >> 6) * 4 + (pr & 3)) * 16 + ((pr >> 2) & 15)) * 64 + (t & 3) * 16;
      const u32 mo = OFF_GM + (lane >> 2) * 24576 + 8 * (((lane >> 1) & 1) * DM + c0 + (lane & 1));
      const u32 m2o = OFF_GM + (lane >> 2) * 24576 + 8 * ((w - 2 + (lane & 2)) * DM + c0 + (lane & 1));
      if (lane < 30) *(u32x4*)(smem + CV_H + (w - 4) * CV_HB + lane * 16) = u32x4{0, 0, 0, 0};
      u32x4 gx[8], gs[8];
      u32x2 gm, gm2;
      u32 bad = 1;
      lds_wait<2>(smem, 5);
      __builtin_amdgcn_s_sleep(GDELAY);
      STAMP(12);
#pragma unroll 1
      for (int spin = 0; spin < SPIN_CAP; ++spin) {
        if (bad) {
          gm = ld8c(R, mo);
          gm2 = ld8c(R, m2o);
#pragma unroll
          for (int i = 0; i < 8; ++i) {
            gx[i] = ld16c(R, xo + i * (32 * 16384));
            gs[i] = ld16c(R, OFF_GST + 16 * t + 2048 * i);
          }
          bad = (gm[1] ^ tag) | (gm2[1] ^ tag);
#pragma unroll
          for (int i = 0; i < 8; ++i) bad |= (gx[i][1] ^ tag) | (gx[i][3] ^ tag) | (gs[i][1] ^ tag) | (gs[i][3] ^ tag);
        }
        if (__all(bad == 0)) break;
        __builtin_amdgcn_s_sleep(8);
        MEMFENCE();
      }
      drain_vm();
      if (w == 5) lds_set(smem, 10, 0);
      STAMP(6);
      RSTAMP(37);
      const float md = rb(sum_hi4(__uint_as_float(gm[0])));
      const f32x2 sa = {rdlane(md, 0), rdlane(md, 1)}, ca1 = {rb(1.f + rdlane(md, 2)), rb(1.f + rdlane(md, 3))};
#pragma unroll
      for (int i = 0; i < 8; ++i) {
        f32x2 hv[2];
#pragma unroll
        for (int e = 0; e < 2; ++e) {
          const u32 st = gs[i][2 * e], xv = gx[i][2 * e];
          hv[e] = __builtin_elementwise_fma((f32x2{bflo(xv), bfhi(xv)} - bflo(st)) * __builtin_amdgcn_rcpf(bfhi(st)), ca1, sa);
        }
        *(u32*)(smem + CV_H + 480 + 512 * i + 4 * t) = pack_bf16(hv[0].x, hv[1].x);
        *(u32*)(smem + CV_H + CV_HB + 480 + 512 * i + 4 * t) = pack_bf16(hv[0].y, hv[1].y);
      }
      lds_set(smem, 2, w - 4);
      STAMP(7);
#pragma unroll
      for (int i = 0; i < 8; ++i) {
        *(u32*)(smem + CV_X + 512 * i + 4 * t) = perm_lo(gx[i][0], gx[i][2]);
        *(u32*)(smem + CV_X + 4096 + 512 * i + 4 * t) = perm_hi(gx[i][0], gx[i][2]);
      }
      {
        float v = rb(sum_hi4(__uint_as_float(gm2[0])));
        if (w == 4 && (lane & 2)) v = rb(1.f + v);
        if (w == 4 && lane < 4) *(float*)(smem + L_EP + 4 * lane) = v;
        if (lane < 4 && (w == 5 || lane >= 2))
          st8c(R, OFF_FM + (w == 4 ? 0 : 1 + (lane >> 1)) * 4096 + 8 * (c0 + (lane & 1)), u32x2{__float_as_uint(v), tag});
      }
      lds_set(smem, 2, w - 2);
    } else {
      u32x4 t[8], p2[8];
      {
        const u16* src = (type ? P.wu : P.wg) + (size_t)(64 * k8 + 8 * kg) * DF + 64 * cg + 8 * cp;
#pragma unroll
        for (int i = 0; i < 8; ++i) p2[i] = *(const u32x4*)(src + i * DF);
      }
      MEMFENCE();
      mod_job();
      transpose8(p2, t);
      pin(code_touch);
      STAMP(3);
      {
        char* tb = smem + L_TR + (1 + (w & 1)) * 8192;
#pragma unroll
        for (int n = 0; n < 8; ++n)
          *(u32x4*)(tb + ((cp >> 1) * 8 + kg) * 256 + (((n & 3) + 8 * (n >> 2) + 4 * (cp & 1)) ^ (cp >> 1)) * 16) = t[n];
        MEMFENCE();
        if (b < 224) {
#pragma unroll
          for (int k = 0; k < 8; ++k) {
            const int row = 4 * k + (lane >> 4), fl = k >> 1;
            const u32x4 o = *(const u32x4*)(tb + row * 256 + ((lane & 15) ^ fl) * 16);
            st16c(R, OFF_WP + (((4 * cg + fl) * 32 + 4 * k8) * 64 + (row & 7) * 32 + 16 * type + (lane & 15)) * 16, o);
          }
        }
      }
      STAMP(4);
      MEMFENCE();
      drain_vm();
      if (lane == 0 && b < 224) st4c(R, OFF_FW + (cg * 16 + type * 8 + k8) * 64, tag);
      STAMP(12);
      lds_wait<1>(smem, 10);
      if (w == 6) {
#pragma unroll 1
        for (int spin = 0; spin < SPIN_CAP; ++spin) {
          const u32 f = ld4c(R, OFF_FW + min(((7 * fb) >> 2) * 16 + lane, ((7 * fb + 6) >> 2) * 16 + 15) * 64);
          if (__all(f == tag)) break;
          __builtin_amdgcn_s_sleep(10);
          MEMFENCE();
        }
        drain_vm();
        lds_set(smem, 5, 0);
        STAMP(5);
      } else {
        const int fg = 28 * xcd + min(bi, 27);
#pragma unroll
        for (int i = 0; i < 8; ++i) p2[i] = *(const u32x4*)(P.wd + (size_t)(8 * fg + i) * DM + 8 * lane);
        transpose8(p2, t);
#pragma unroll
        for (int k = 0; k < 8; ++k) *(u32x4*)(smem + L_TR + lane * 128 + ((k ^ lane) & 7) * 16) = t[k];
        MEMFENCE();
        if (bi < 28) {
#pragma unroll
          for (int k = 0; k < 8; ++k)
            st16c(R, OFF_WD + (2 * k + (lane >> 5)) * (NFT * 1024) + fg * 512 + (lane & 31) * 16,
                  *(const u32x4*)(smem + L_TR + (8 * k + (lane >> 3)) * 128 + ((lane ^ (lane >> 3)) & 7) * 16));
        }
        drain_vm();
        x1_arrive();
        STAMP(6);
        lds_wait<15>(smem, 3);
#pragma unroll 1
        for (int spin = 0; spin < SPIN_CAP; ++spin) {
          u32 bad = 0;
#pragma unroll
          for (int i = 0; i < 8; ++i) t[i] = ld16c(R, OFF_FM + i * 1024 + l16);
#pragma unroll
          for (int i = 0; i < 8; ++i) bad |= (t[i][1] ^ tag) | (t[i][3] ^ tag);
          if (__all(bad == 0)) break;
          __builtin_amdgcn_s_sleep(8);
          MEMFENCE();
        }
#pragma unroll
        for (int i = 0; i < 8; ++i) *(u32*)(smem + L_V + 256 * i + 4 * lane) = perm_hi(t[i][0], t[i][2]);
        lds_set(smem, 5, 2);
        STAMP(5);
      }
    }
    __builtin_amdgcn_s_setprio(0);
    if (w == 7) {
    const char* xsg = ws + OFF_XS + (size_t)(4 * mt) * 32768;
    const char* x1g = ws + OFF_X1 + (size_t)(4 * mt) * 32768;
    auto dma = [&](const char* src, int dst, int n) {
#pragma unroll 4
      for (int i = 0; i < n; ++i) glds16(l16, src + i * 1024, lds0 + dst + i * 1024);
    };
    const u32 fao = OFF_FA + mt * 4096;
    lds_wait<4>(smem, 5);
    lds_wait<15>(smem, 1);
#pragma unroll 1
    for (int spin = 0; spin < SPIN_CAP; ++spin) {
      u32 f[4];
#pragma unroll
      for (int i = 0; i < 4; ++i) f[i] = ld4c(R, OFF_FX + 4096 * xcd + (lane + 64 * i) * 16);
      if (__all(((f[0] ^ tag) | (f[1] ^ tag) | (f[2] ^ tag) | (f[3] ^ tag)) == 0)) break;
      __builtin_amdgcn_s_sleep(1);
      MEMFENCE();
    }
    drain_vm();
    STAMP(10);
    lds_set(smem, 6, 1);
    if (b == 0 && lane == 0) st4c(R, OFF_E, tag);
    dma(x1g + (fb >> 2) * 32768 + (fb & 3) * 8192, L_SPARE, 8);
    barrier();
    STAMP(17);
    dma(xsg + 35840, 35840, 13);
    wait_vm<13>();
    {
      const int g = lane >> 4;
      const char* sb = smem + L_SPARE + (8 * (g >> 1) + 4 * (g & 1) + q4) * 64 + 8 * p4;
      const bf16x8 ones = as_frag(u32x4{ONES2, ONES2, ONES2, ONES2});
      bf16x8 f[2][4];
#pragma unroll
      for (int k2 = 0; k2 < 4; ++k2)
#pragma unroll
        for (int c = 0; c < 2; ++c) {
          const char* p = sb + 32 * (c ^ (g & 1)) + k2 * 2048;
          f[c][k2] = tr_frag(p, p + 1024);
        }
      __builtin_amdgcn_sched_barrier(0);
#pragma unroll
      for (int c = 0; c < 2; ++c) {
        f32x4 aq = {}, as = {};
#pragma unroll
        for (int k2 = 0; k2 < 4; ++k2) {
          aq = MFMA16(f[c][k2], f[c][k2], aq);
          as = MFMA16(ones, f[c][k2], as);
        }
        const float sq = p4 == 0 ? aq[0] : p4 == 1 ? aq[1] : p4 == 2 ? aq[2] : aq[3];
        if (g == q4)
          st16c(R, OFF_GS2 + ((mt * 4 + (fb & 3)) * 128 + 32 * (fb >> 2) + 16 * c + (lane & 15)) * 16,
                u32x4{__float_as_uint(as[0]), tag, __float_as_uint(sq), tag});
      }
    }
    dma(xsg + 49152, 49152, 16);
    lds_barrier();
    dma(xsg + 65536, 65536, 16);
    {
      u32x4 g[2][4];
#pragma unroll 1
      for (int spin = 0; spin < SPIN_CAP; ++spin) {
        u32 bad = 0;
#pragma unroll
        for (int i = 0; i < 8; ++i) g[i & 1][i >> 1] = ld16c(R, OFF_GS2 + mt * 8192 + i * 1024 + l16);
#pragma unroll
        for (int i = 0; i < 8; ++i) bad |= (g[i & 1][i >> 1][1] ^ tag) | (g[i & 1][i >> 1][3] ^ tag);
        drain_vm();
        if (spin == 0) {
          stamp_t(18);
          barrier();
          stamp_t(19);
        }
        if (__all(bad == 0)) break;
        MEMFENCE();
      }
#pragma unroll
      for (int c = 0; c < 2; ++c) {
        const float s1 = (__uint_as_float(g[c][0][0]) + __uint_as_float(g[c][1][0])) + (__uint_as_float(g[c][2][0]) + __uint_as_float(g[c][3][0]));
        const float s2 = (__uint_as_float(g[c][0][2]) + __uint_as_float(g[c][1][2])) + (__uint_as_float(g[c][2][2]) + __uint_as_float(g[c][3][2]));
        const u32 ms = ln_pack(s1, s2) ^ 0x8000u;
        *(u32x4*)(smem + L_SF + (64 * c + lane) * 16) = u32x4{perm_lo(ms, ms), ms, perm_hi(ms, ms), 0};
        const float k0 = -2.f * 0.7978845608028654f * 1.4426950408889634f, k1 = k0 * 0.044715f;
        const float sd = bfhi(ms), rs = __builtin_amdgcn_rcpf(sd);
        *(f32x4*)(smem + L_RS + (64 * c + lane) * 16) = f32x4{k0 * rs, k1 * rs * rs * rs, sd * sd, 0.f};
      }
    }
    lds_set(smem, 7, 0);
    stamp_t(8);
    dma(xsg + 81920, 81920, 48);
    wait_vm<24>();
    stamp_t(20);
    lds_barrier();
    stamp_t(21);
    dma(ws + OFF_X1 + (size_t)(4 * m2 + te) * 32768 + nt * 4096, L_RES, 4);
    dma(ws + OFF_X1 + (size_t)(4 * m2 + tl) * 32768 + nt * 4096, L_RES + 4096, 4);
#pragma unroll
    for (int j = 0; j < 8; ++j) stage_wd(j);
    stage_wd(16);
    stage_wd(17);
    static_for<0, 10>(load_wr);
    *(float*)(smem + L_GB + 4 * lane) = gather_sum<1>(R, OFF_FM + 2 * 4096 + 8 * (64 * nt + lane), 0, tag);
    drain_vm();
#pragma unroll
    for (int j = 0; j < 10; ++j) pin(wr[j]);
    stamp_t(22);
    lds_barrier();
    stamp_t(23);
    if (lane < 2) st4c(R, fao + lane * 1024 + fb * 64, tag);
#pragma unroll
    for (int j = 8; j < 12; ++j) stage_wd(j);
    __builtin_amdgcn_s_sleep(16);
    poll_flag(R, OFF_FA + m2 * 4096 + te * 1024 + (lane & 15) * 64, tag);
    static_for<0, 14>(load_ae);
    stamp_t(24);
    barrier();
    stamp_t(25);
    if (lane == 0) st4c(R, fao + 2 * 1024 + fb * 64, tag);
#pragma unroll
    for (int j = 12; j < 16; ++j) stage_wd(j);
    barrier();
    barrier();
    if (lane == 0) st4c(R, fao + 3 * 1024 + fb * 64, tag);
    __builtin_amdgcn_s_sleep(20);
    poll_flag(R, OFF_FA + m2 * 4096 + tl * 1024 + (lane & 15) * 64, tag);
    lds_set(smem, 6, 0);
    drain_vm();
    down_phase(std::integral_constant<int, 1>{});
  }
  }

  if (w < 7) {
    lds_wait<1>(smem, 5);
    if (w >= 4) {
      lds_wait<3>(smem, 2);
      static_for<0, NPARK>(load_w);
    } else {
      lds_wait<2>(smem, 4);
    }
    static_for<NPARK, 32>(load_w);
    if (w >= 4) {
      lds_wait<15>(smem, 8);
#pragma unroll 4
      for (int i = 0; i < 20; ++i) {
        const int f = 3 * i + (w - 4), tw = f & 3, k = f >> 2;
        glds16(l16, ws + OFF_WP + (size_t)(7 * fb + tw) * 32768 + k * 1024, lds0 + L_PARK + (tw * NPARK + k) * 1024);
      }
      drain_vm();
      lds_set(smem, 9, w - 4);
      STAMP(9);
      lds_wait<15>(smem, 3);
    } else {
      lds_wait<7>(smem, 9);
#pragma unroll
      for (int j = 0; j < NPARK; ++j) wf[j] = *(const u32x4*)(smem + L_PARK + (w * NPARK + j) * 1024 + l16);
    }
    u32x4 aux;
    {
      lds_wait<4>(smem, 5);
      const char* vb = smem + L_V + (lane & 1) * 1024 + 16 * hh;
      f32x16 d = {};
      bf16x8 va[32];
#pragma unroll
      for (int j = 0; j < 12; ++j) va[j] = *(const bf16x8*)(vb + 32 * j);
      static_for<0, 32>([&](auto J) {
        constexpr int j = decltype(J)::value;
        if constexpr (j + 12 < 32) va[j + 12] = *(const bf16x8*)(vb + 32 * (j + 12));
        if constexpr (j >= NPARK) wait_vm_reg<31 - j>(wf[j]);
        d = MFMA32(va[j], as_frag(wf[j]), d);
        pin(d);
        __builtin_amdgcn_sched_barrier(0);
      });
      asm volatile("s_waitcnt lgkmcnt(0)" ::: "memory");
      if (w < 4) lds_set(smem, 1, w);
      const float c = d[0], sf = d[1];
      const float c0 = rb(c), c1 = rb(c - c0), b0 = rb(sf), b1 = rb(sf - b0);
      aux = u32x4{pack_bf16(c0, c1), pack_bf16(c - c0 - c1, b0), pack_bf16(b1, sf - b0 - b1), 0};
      if (hh) aux = u32x4{0, 0, 0, 0};
    }
    STAMP(10);
    u32 xb[2][2];
#pragma unroll
    for (int i = 0; i < 2; ++i) {
      xb[i][0] = lds0 + 65536 * i + (8 * hh + q4) * 64 + 32 * blk + 8 * p4;
      xb[i][1] = lds0 + 65536 * i + (8 * hh + 4 + q4) * 64 + 32 * (blk ^ 1) + 8 * p4;
      pin(xb[i][0]);
      pin(xb[i][1]);
    }
    auto xfrag = [&](int s) { return tr_frag_at(xb[s >> 6][0] + (s & 63) * 1024, xb[s >> 6][1] + (s & 63) * 1024); };
    lds_wait<2>(smem, 6);
#pragma unroll
    for (int i = 0; i < 5; ++i) glds16(l16, ws + OFF_XS + (size_t)(4 * mt) * 32768 + (7 * i + w) * 1024, lds0 + (7 * i + w) * 1024);
    wait_vm<2>();
    barrier();
    STAMP(17);
    constexpr int XD = 3;
    bf16x8 xf[4];
#pragma unroll
    for (int s = 0; s < XD; ++s) xf[s] = xfrag(s);
    const char* actg = ws + OFF_ACT + ((size_t)(4 * mt) * NFT + ft) * 1024;
    f32x16 acc[2];
    f32x4 gc;
    float ev = 0.f, gu = 0.f, prev = 0.f;
    u32x4 o;
    auto epi_a = [&](int t, int i) {
      const float a = acc[t & 1][i];
      ev = __builtin_amdgcn_exp2f(a * __builtin_fmaf(a * a, gc[1], gc[0]));
      gu = a * acc[t & 1][8 + i];
      pin(ev);
      pin(gu);
    };
    auto epi_b = [&](int t, int i) {
      float v = gu * __builtin_amdgcn_rcpf(__builtin_fmaf(ev, gc[2], gc[2]));
      pin(v);
      if (i & 1) o[i >> 1] = pack_bf16(prev, v);
      else prev = v;
      if (i == 7) gstore16(o, l16, actg + (size_t)t * NFT * 1024);
    };
    auto fold = [&](int t) {
      const bf16x8 sf = *(const bf16x8*)(smem + L_SF + t * 512 + (lane & 31) * 16);
      gc = *(const f32x4*)(smem + L_RS + t * 512 + (lane & 31) * 16);
      acc[t & 1] = MFMA32(as_frag(aux), sf, acc[t & 1]);
    };
    static_for<0, 4>([&](auto T) {
      constexpr int t = decltype(T)::value;
      constexpr int e0 = t == 1 ? 16 : 3;
      static_for<0, 32>([&](auto KS) {
        constexpr int ks = decltype(KS)::value, s = 32 * t + ks;
        if constexpr (t == 0 && ks == 21 - XD) {
          wait_vm<0>();
          barrier();
        }
        if constexpr (t == 1 && ks == e0 - 3) lds_wait<1>(smem, 7);
        if constexpr (s + XD < 128) xf[(s + XD) & 3] = xfrag(s + XD);
        if constexpr (ks == 0) acc[t & 1] = MFMA32(as_frag(wf[0]), xf[s & 3], f32x16{});
        else acc[t & 1] = MFMA32(as_frag(wf[ks]), xf[s & 3], acc[t & 1]);
        pin(acc[t & 1]);
        if constexpr (t > 0) {
          if constexpr (ks == e0 - 3) fold(t - 1);
          if constexpr (ks >= e0 && ks < e0 + 16) {
            if constexpr (((ks - e0) & 1) == 0) epi_a(t - 1, (ks - e0) >> 1);
            else epi_b(t - 1, (ks - e0) >> 1);
          }
        }
        if constexpr (t == 1 && ks < 12 && (ks & 1)) stage_wd(ks < 8 ? ks >> 1 : 12 + (ks >> 1));
        if constexpr (t >= 2 && ks >= 20 && ks < 28 && (ks & 1) == 0) stage_wd(4 * (t - 1) + (ks - 20) / 2);
        if constexpr (t >= 2 && ks < 18 && (ks & 3) == 0) load_wr(std::integral_constant<int, 5 * (t - 2) + ks / 4>{});
        __builtin_amdgcn_sched_barrier(0);
      });
      stamp_t(18 + 2 * t);
      if constexpr (t >= 2) wait_vm<4>();
      lds_barrier();
      stamp_t(19 + 2 * t);
    });
    fold(3);
#pragma unroll
    for (int i = 0; i < 8; i += 2) {
      float v[2];
#pragma unroll
      for (int e = 0; e < 2; ++e) {
        const float a = acc[1][i + e];
        const float ex = __builtin_amdgcn_exp2f(a * __builtin_fmaf(a * a, gc[1], gc[0]));
        v[e] = a * acc[1][8 + i + e] * __builtin_amdgcn_rcpf(__builtin_fmaf(ex, gc[2], gc[2]));
      }
      o[i >> 1] = pack_bf16(v[0], v[1]);
    }
    gstore16(o, l16, actg + (size_t)3 * NFT * 1024);
    STAMP(26);
    barrier();
#pragma unroll
    for (int j = 12; j < 16; ++j) stage_wd(j);
    static_for<0, 4>(load_ae);
    wait_vm<8>();
    STAMP(27);
    barrier();
    static_for<4, 14>(load_ae);
    wait_vm<14>();
#pragma unroll
    for (int j = 0; j < 10; ++j) pin(wr[j]);
    down_phase(std::integral_constant<int, 0>{});
  }
  DIAG_END(ws);
}

extern "C" void kernel_launch(void* const* d_in, const int* in_sizes, int n_in, void* d_out, int out_size, void* d_ws,
                              size_t ws_size, hipStream_t stream) {
  Params p;
  p.x = (const u16*)d_in[0];
  p.la = (const u16*)d_in[1];
  p.ai = (const u16*)d_in[2];
  p.cr = (const u16*)d_in[3];
  p.ci = (const u16*)d_in[4];
  p.ldt = (const u16*)d_in[5];
  p.dsk = (const u16*)d_in[6];
  p.wg = (const u16*)d_in[8];
  p.wu = (const u16*)d_in[9];
  p.wd = (const u16*)d_in[10];
  p.c = (const u16*)d_in[11];
  p.wm = (const u16*)d_in[12];
  p.out = (u16*)d_out;
  p.ws = (char*)d_ws;
  (void)hipFuncSetAttribute((const void*)fused_block, hipFuncAttributeMaxDynamicSharedMemorySize, SMEM);
  fused_block<<<256, 512, SMEM, stream>>>(p);
}
```

```cpp
#define TELE 1
#include <hip/hip_runtime.h>
#include <hip/hip_bf16.h>
#include <stdint.h>
#include <type_traits>

typedef unsigned short u16;
typedef unsigned int u32;
typedef unsigned long long u64;
typedef __attribute__((ext_vector_type(8))) __bf16 bf16x8;
typedef __attribute__((ext_vector_type(4))) __bf16 bf16x4;
typedef __attribute__((ext_vector_type(2))) __bf16 bf16x2;
typedef __attribute__((ext_vector_type(2))) _Float16 f16x2;
typedef __attribute__((ext_vector_type(8))) _Float16 f16x8;
typedef __attribute__((ext_vector_type(2))) float f32x2;
typedef __attribute__((ext_vector_type(4))) float f32x4;
typedef __attribute__((ext_vector_type(16))) float f32x16;
typedef __attribute__((ext_vector_type(4))) u32 u32x4;
typedef __attribute__((ext_vector_type(2))) u32 u32x2;
typedef __attribute__((address_space(3))) bf16x4* lds_bf16x4_p;
typedef __attribute__((address_space(3))) char* lds_char_p;
typedef __attribute__((address_space(3))) u32* lds_u32_p;
typedef __amdgpu_buffer_rsrc_t rsrc_t;

#define DEVI __device__ __forceinline__

constexpr int SL = 2048, DM = 512, NS = 128, DF = 1792;
constexpr int NFT = DF / 16;
constexpr int SPIN_CAP = 1 << 15;
#define GDELAY 2
#define WDELAY 6
constexpr int CODE_BYTES = 38 * 1024;

constexpr u32 OFF_E = 0;
constexpr u32 OFF_GST = 4096;
constexpr u32 OFF_FX = 32768;
constexpr u32 OFF_FW = 81920;
constexpr u32 OFF_FA = 131072;
constexpr u32 OFF_FM = 114688;
constexpr u32 OFF_GS2 = 196608;
constexpr u32 OFF_WP = 3u << 20;
constexpr u32 OFF_X1 = OFF_WP + (4u << 20);
constexpr u32 OFF_ACT = OFF_X1 + (2u << 20);
constexpr u32 OFF_XG = OFF_ACT + (8u << 20);
constexpr u32 OFF_XS = OFF_XG + (4u << 20);
constexpr u32 OFF_GM = OFF_XS + (2u << 20);
constexpr u32 WS_USED = OFF_GM + 16 * 24576;

constexpr int KG_ROWB = 272;
constexpr int KG_WAVEB = 57 * KG_ROWB;
constexpr int L_KC = 4 * KG_WAVEB;
constexpr int CV_KCOPY = 4160;
constexpr int L_PARK = 0;
constexpr int NPARK = 21;
constexpr int CV_K = 4 * NPARK * 1024;
constexpr int CV_H = CV_K + 4 * CV_KCOPY;
constexpr int CV_HB = 2288 * 2;
constexpr int CV_X = CV_H + 2 * CV_HB;
constexpr int CV_P = CV_X + 8192;
constexpr int L_TR = CV_P;
static_assert(L_KC + 4 * 656 * 4 <= CV_K, "front LDS map");
constexpr int L_RES = 131072;
constexpr int L_SPARE = L_RES + 8192;
constexpr int L_SF = L_SPARE + 16384;
static_assert(CV_P + 32768 + 16 <= L_SF, "front LDS map");
constexpr int L_RS = L_SF + 2048;
constexpr int L_V = L_RS;
constexpr int L_GB = L_RS + 2048;
constexpr int L_SYNC = L_GB + 256;
constexpr int L_EP = L_SYNC + 192;
constexpr int SMEM = L_EP + 16;

#define DIAG_DECL
#define STAMP(i)
#define RSTAMP(i)
#define DIAG_END(wsp)

template <int I, int N, class F>
DEVI void static_for(F&& f) {
  if constexpr (I < N) {
    f(std::integral_constant<int, I>{});
    static_for<I + 1, N>(f);
  }
}

DEVI float bflo(u32 v) { return __uint_as_float(v << 16); }
DEVI float bfhi(u32 v) { return __uint_as_float(v & 0xffff0000u); }
DEVI float b2f(u16 v) { return __uint_as_float(((u32)v) << 16); }
DEVI u32 pack_bf16(float a, float b) {
  f32x2 v = {a, b};
  return __builtin_bit_cast(u32, __builtin_convertvector(v, bf16x2));
}
DEVI float rb(float a) { return bflo(pack_bf16(a, a)); }
DEVI u32 pack_f16(f32x2 v) { return __builtin_bit_cast(u32, __builtin_convertvector(v, f16x2)); }
DEVI u32 perm_lo(u32 a, u32 b) { return __builtin_amdgcn_perm(b, a, 0x05040100u); }
DEVI u32 perm_hi(u32 a, u32 b) { return __builtin_amdgcn_perm(b, a, 0x07060302u); }
DEVI float dot2(u32 a, u32 b, float c) {
  return __builtin_amdgcn_fdot2_f32_bf16(__builtin_bit_cast(bf16x2, a), __builtin_bit_cast(bf16x2, b), c, false);
}
constexpr u32 ONES2 = 0x3f803f80u;

template <int CTRL>
DEVI float dpp(float v) { return __int_as_float(__builtin_amdgcn_update_dpp(0, __float_as_int(v), CTRL, 0xf, 0xf, true)); }
DEVI float row_sum(float v) {
  v += dpp<0xB1>(v); v += dpp<0x4E>(v); v += dpp<0x141>(v); v += dpp<0x140>(v);
  return v;
}
DEVI float rs32(float a, float b) {
  auto r = __builtin_amdgcn_permlane32_swap(__float_as_uint(a), __float_as_uint(b), false, false);
  return __uint_as_float(r[0]) + __uint_as_float(r[1]);
}
DEVI float rs16(float a, float b) {
  auto r = __builtin_amdgcn_permlane16_swap(__float_as_uint(a), __float_as_uint(b), false, false);
  return __uint_as_float(r[0]) + __uint_as_float(r[1]);
}
DEVI float wave_max(float v) {
  v = fmaxf(v, dpp<0xB1>(v)); v = fmaxf(v, dpp<0x4E>(v)); v = fmaxf(v, dpp<0x141>(v)); v = fmaxf(v, dpp<0x140>(v));
  auto r = __builtin_amdgcn_permlane16_swap(__float_as_uint(v), __float_as_uint(v), false, false);
  v = fmaxf(__uint_as_float(r[0]), __uint_as_float(r[1]));
  auto s = __builtin_amdgcn_permlane32_swap(__float_as_uint(v), __float_as_uint(v), false, false);
  return fmaxf(__uint_as_float(s[0]), __uint_as_float(s[1]));
}

DEVI f32x2 cmul(f32x2 a, f32x2 b) {
  const f32x2 t = a.yy * f32x2{-b.y, b.x};
  return __builtin_elementwise_fma(a.xx, b, t);
}
DEVI f32x2 cexpk(float zr, float zi, float k) {
  const float mag = __builtin_amdgcn_exp2f(zr * (k * 1.4426950408889634f));
  const float rev = __builtin_amdgcn_fractf(zi * (k * 0.15915494309189535f));
  return f32x2{mag * __builtin_amdgcn_cosf(rev), mag * __builtin_amdgcn_sinf(rev)};
}

DEVI u32x4 ld16c(rsrc_t r, u32 off) { return __builtin_amdgcn_raw_buffer_load_b128(r, off, 0, 16); }
DEVI u32x2 ld8c(rsrc_t r, u32 off) { return __builtin_amdgcn_raw_buffer_load_b64(r, off, 0, 16); }
DEVI u32 ld4c(rsrc_t r, u32 off) { return __builtin_amdgcn_raw_buffer_load_b32(r, off, 0, 16); }
DEVI void st16c(rsrc_t r, u32 off, u32x4 v) { __builtin_amdgcn_raw_buffer_store_b128(v, r, off, 0, 16); }
DEVI void st8c(rsrc_t r, u32 off, u32x2 v) { __builtin_amdgcn_raw_buffer_store_b64(v, r, off, 0, 16); }
DEVI void st4c(rsrc_t r, u32 off, u32 v) { __builtin_amdgcn_raw_buffer_store_b32(v, r, off, 0, 16); }
#define MEMFENCE() asm volatile("" ::: "memory")
DEVI void drain_vm() {
  __builtin_amdgcn_s_waitcnt(0x0F70);
  asm volatile("s_waitcnt vmcnt(0)" ::: "memory");
}

template <int N>
DEVI float gather_sum(rsrc_t r, u32 off, u32 stride, u32 tag) {
  float v = 0.f;
#pragma unroll 1
  for (int spin = 0; spin < SPIN_CAP; ++spin) {
    u32x2 g[N];
#pragma unroll
    for (int i = 0; i < N; ++i) g[i] = ld8c(r, off + i * stride);
    u32 bad = 0;
    v = 0.f;
#pragma unroll
    for (int i = 0; i < N; ++i) {
      v += __uint_as_float(g[i][0]);
      bad |= g[i][1] ^ tag;
    }
    if (__all(bad == 0)) break;
    __builtin_amdgcn_s_sleep(2);
    MEMFENCE();
  }
  MEMFENCE();
  return v;
}
DEVI float sum_hi4(float v) {
  v += dpp<0x124>(v);
  v += dpp<0x128>(v);
  v = rs16(v, v);
  return rs32(v, v);
}
DEVI u32 silu2(u32 c) {
  const float a = bflo(c), b = bfhi(c);
  return pack_bf16(a * __builtin_amdgcn_rcpf(1.f + __expf(-a)), b * __builtin_amdgcn_rcpf(1.f + __expf(-b)));
}
DEVI float rdlane(float v, int l) { return __int_as_float(__builtin_amdgcn_readlane(__float_as_int(v), l)); }
DEVI void poll_flag(rsrc_t r, u32 off, u32 tag) {
#pragma unroll 1
  for (int spin = 0; spin < SPIN_CAP; ++spin) {
    const u32 f = ld4c(r, off);
    if (__all(f == tag)) break;
    __builtin_amdgcn_s_sleep(1);
    MEMFENCE();
  }
  MEMFENCE();
}

DEVI void lds_set(char* smem, int grp, int word) {
  MEMFENCE();
  *(__attribute__((address_space(3))) volatile u32*)(smem + L_SYNC + 16 * grp + 4 * word) = 1u;
  MEMFENCE();
}
DEVI u32 lds_arrive(char* smem, int grp) {
  u32 old;
  const u32 addr = (u32)(uintptr_t)(lds_char_p)(smem + L_SYNC + 16 * grp);
  asm volatile("s_mov_b64 exec, 1\n\tds_add_rtn_u32 %0, %1, %2\n\ts_mov_b64 exec, -1\n\ts_waitcnt lgkmcnt(0)"
               : "=&v"(old) : "v"(addr), "v"(1u) : "memory");
  return (u32)__builtin_amdgcn_readfirstlane((int)old);
}
template <int MASK>
DEVI void lds_wait(char* smem, int grp) {
  typedef __attribute__((address_space(3))) volatile u32x4* lds_u32x4_p;
#pragma unroll 1
  for (int spin = 0; spin < (1 << 22); ++spin) {
    MEMFENCE();
    const u32x4 v = *(lds_u32x4_p)(smem + L_SYNC + 16 * grp);
    const u32 a = ((MASK & 1) ? v[0] : 1u) & ((MASK & 2) ? v[1] : 1u) & ((MASK & 4) ? v[2] : 1u) & ((MASK & 8) ? v[3] : 1u);
    if (__builtin_amdgcn_readfirstlane((int)a)) break;
    __builtin_amdgcn_s_sleep(1);
  }
  MEMFENCE();
}

template <int MA, int MB>
DEVI void lds_wait2(char* smem, int ga, int gb) {
  typedef __attribute__((address_space(3))) volatile u32x4* lds_u32x4_p;
#pragma unroll 1
  for (int spin = 0; spin < (1 << 22); ++spin) {
    MEMFENCE();
    const u32x4 v = *(lds_u32x4_p)(smem + L_SYNC + 16 * ga), u = *(lds_u32x4_p)(smem + L_SYNC + 16 * gb);
    const u32 a = ((MA & 1) ? v[0] : 1u) & ((MA & 2) ? v[1] : 1u) & ((MA & 4) ? v[2] : 1u) & ((MA & 8) ? v[3] : 1u) &
                  ((MB & 1) ? u[0] : 1u) & ((MB & 2) ? u[1] : 1u) & ((MB & 4) ? u[2] : 1u) & ((MB & 8) ? u[3] : 1u);
    if (__builtin_amdgcn_readfirstlane((int)a)) break;
  }
  MEMFENCE();
}

DEVI void glds16(u32 voff, const void* sbase, u32 ldsaddr) {
  asm volatile("s_mov_b32 m0, %2\n\ts_nop 0\n\tglobal_load_lds_dwordx4 %0, %1 sc1" ::"v"(voff), "s"(sbase), "s"(ldsaddr) : "memory");
}
DEVI void glds16p(u32 voff, const void* sbase, u32 ldsaddr) {
  asm volatile("s_mov_b32 m0, %2\n\ts_nop 0\n\tglobal_load_lds_dwordx4 %0, %1" ::"v"(voff), "s"(sbase), "s"(ldsaddr) : "memory");
}
template <int N>
DEVI void wait_vm() { asm volatile("s_waitcnt vmcnt(%0)" ::"n"(N) : "memory"); }
template <int N>
DEVI void wait_vm_reg(u32x4& r) { asm volatile("s_waitcnt vmcnt(%1)" : "+v"(r) : "n"(N)); }
template <class T>
DEVI void pin(T& r) { asm volatile("" : "+v"(r)); }
template <int OFF>
DEVI void gload16(u32x4& r, u32 voff, const void* sbase) {
  asm volatile("global_load_dwordx4 %0, %1, %2 offset:%3 sc1" : "=v"(r) : "v"(voff), "s"(sbase), "n"(OFF) : "memory");
}
DEVI void gstore16(u32x4 v, u32 voff, const void* sbase) {
  asm volatile("global_store_dwordx4 %0, %1, %2 sc1\n\ts_nop 1" ::"v"(voff), "v"(v), "s"(sbase) : "memory");
}
DEVI void barrier() {
  MEMFENCE();
  __builtin_amdgcn_s_barrier();
  MEMFENCE();
}
DEVI void lds_barrier() {
  asm volatile("s_waitcnt lgkmcnt(0)" ::: "memory");
  __builtin_amdgcn_s_barrier();
  MEMFENCE();
}
DEVI bf16x4 tr_read(const char* p) { return __builtin_amdgcn_ds_read_tr16_b64_v4bf16((lds_bf16x4_p)(p)); }
DEVI bf16x8 tr_frag(const char* p0, const char* p1) {
  return __builtin_shufflevector(tr_read(p0), tr_read(p1), 0, 1, 2, 3, 4, 5, 6, 7);
}
DEVI bf16x8 tr_frag_at(u32 a0, u32 a1) {
  return __builtin_shufflevector(__builtin_amdgcn_ds_read_tr16_b64_v4bf16((lds_bf16x4_p)(uintptr_t)a0),
                                 __builtin_amdgcn_ds_read_tr16_b64_v4bf16((lds_bf16x4_p)(uintptr_t)a1), 0, 1, 2, 3, 4, 5, 6, 7);
}
DEVI bf16x8 as_frag(u32x4 v) { return __builtin_bit_cast(bf16x8, v); }
#define MFMA32(a, b, c) __builtin_amdgcn_mfma_f32_32x32x16_bf16(a, b, c, 0, 0, 0)
#define MFMA16(a, b, c) __builtin_amdgcn_mfma_f32_16x16x32_bf16(a, b, c, 0, 0, 0)

DEVI void transpose8(const u32x4 (&r)[8], u32x4 (&o)[8]) {
#pragma unroll
  for (int k = 0; k < 8; ++k)
#pragma unroll
    for (int i = 0; i < 4; ++i)
      o[k][i] = (k & 1) ? perm_hi(r[2 * i][k >> 1], r[2 * i + 1][k >> 1]) : perm_lo(r[2 * i][k >> 1], r[2 * i + 1][k >> 1]);
}
DEVI u32 ln_pack(float s, float q) {
  const float mean = s * (1.f / DM);
  const float var = fmaxf(q * (1.f / DM) - mean * mean, 0.f);
  return pack_bf16(mean, __builtin_amdgcn_sqrtf(rb(rb(var) + 1e-5f)));
}

template <int NP>
DEVI void mod_part(const u32x4 (&wv)[8], const u32 (&sl)[NP], rsrc_t r, u32 dst, u32 tag, int lane) {
  float acc[8] = {};
#pragma unroll
  for (int j = 0; j < NP; ++j)
#pragma unroll
    for (int k = 0; k < 4; ++k) {
      acc[2 * k] = dot2(perm_lo(wv[2 * j][k], wv[2 * j + 1][k]), sl[j], acc[2 * k]);
      acc[2 * k + 1] = dot2(perm_hi(wv[2 * j][k], wv[2 * j + 1][k]), sl[j], acc[2 * k + 1]);
    }
#pragma unroll
  for (int i = 0; i < 4; ++i) acc[i] = rs32(acc[i], acc[i + 4]);
#pragma unroll
  for (int i = 0; i < 2; ++i) {
    acc[i] = rs16(acc[i], acc[i + 2]);
    acc[i] += dpp<0x128>(acc[i]);
  }
  if ((lane & 8) == 0)
    st16c(r, dst + 8 * (8 * (lane & 7) + 2 * (lane >> 4)), u32x4{__float_as_uint(acc[0]), tag, __float_as_uint(acc[1]), tag});
}

struct Params {
  const u16 *x, *la, *ai, *cr, *ci, *ldt, *dsk, *wg, *wu, *wd, *c, *wm;
  u16* out;
  char* ws;
};

__global__ __launch_bounds__(512, 2) void fused_block(Params P) {
  extern __shared__ __attribute__((aligned(16))) char smem[];
  DIAG_DECL
  u64 tl0 = __builtin_amdgcn_s_memrealtime(), tln = 0;
#define TP(n) do { if (TELE == n) tln = __builtin_amdgcn_s_memrealtime(); } while (0)
  const int tid = threadIdx.x, lane = tid & 63;
  const int w = __builtin_amdgcn_readfirstlane(tid >> 6);
  const int b = blockIdx.x, xcd = b & 7, bi = b >> 3;
  char* const ws = P.ws;
  const rsrc_t R = __builtin_amdgcn_make_buffer_rsrc(ws, 0, WS_USED, 0x00020000);
  const int mt = 8 * (xcd & 1) + (bi & 7), fb = 4 * (xcd >> 1) + (bi >> 3);
  const int m2 = 2 * xcd + (bi & 1), nt = bi >> 2, te = (bi >> 1) & 1, tl = 3 - te;
  const int ft = 7 * fb + w;
  const u32 l16 = lane * 16;
  const char* wsrc = ws + OFF_WP + (size_t)ft * 32768;
  u32x4 wf[32];
  u32 code_touch = 0;
  {
    const __attribute__((address_space(1))) char* pc;
    asm volatile("s_getpc_b64 %0" : "=s"(pc));
    if (w >= 6)
      code_touch = *(const __attribute__((address_space(1))) u32*)(pc + min((2 * (bi % 3) + (w & 1)) * 8192 + lane * 128, CODE_BYTES));
  }
  if (tid < 48) *(u32*)(smem + L_SYNC + 4 * tid) = 0;
  STAMP(32);
  u32x4 pl[8];
  u32 ps[8];
  if (w < 4) {
    const int d = 64 * xcd + 2 * bi + (w >> 1), n = 64 * (w & 1) + lane;
    ps[0] = P.la[d * NS + n];
    ps[1] = P.ai[d * NS + n];
    ps[2] = P.cr[d * NS + n];
    ps[3] = P.ci[d * NS + n];
    ps[4] = P.cr[d * NS + (n ^ 64)];
    ps[5] = P.ci[d * NS + (n ^ 64)];
    ps[6] = P.ldt[d];
    ps[7] = P.dsk[(d & ~1) + ((lane >> 2) & 1)];
  } else if (w == 4) {
#pragma unroll
    for (int i = 0; i < 8; ++i) pl[i] = *(const u32x4*)(P.x + (size_t)(8 * b + 2 * (lane & 3) + (i & 1)) * DM + 8 * (lane >> 2) + 128 * (i >> 1));
  } else if (w == 5) {
    const int ra = 32 * (b & 15) + 4 * (lane >> 3);
    const u32x2 cv = *(const u32x2*)(P.c + ra);
    ps[0] = cv[0];
    ps[1] = cv[1];
#pragma unroll
    for (int i = 0; i < 4; ++i) pl[i] = *(const u32x4*)(P.wm + (size_t)(ra + i) * 3072 + 64 * (b >> 4) + 8 * (lane & 7));
  } else {
    const int un = min(b, 223);
    const u16* src = (((un >> 2) & 1) ? P.wu : P.wg) + (size_t)(64 * (2 * (un & 3) + (w & 1)) + 8 * (lane >> 3)) * DF + 64 * (un >> 3) + 8 * (lane & 7);
    __builtin_amdgcn_s_sleep(WDELAY);
#pragma unroll
    for (int i = 0; i < 8; ++i) pl[i] = *(const u32x4*)(src + i * DF);
  }
  u32 tag;
  asm volatile("s_load_dword %0, %1, 0x0\n\ts_waitcnt lgkmcnt(0)" : "=s"(tag) : "s"(ws + OFF_E) : "memory");
  tag += 0x9E3779B9u;
  STAMP(34);
  barrier();
  TP(1);
  STAMP(35);
  auto load_w = [&](auto J) {
    constexpr int j = decltype(J)::value;
    gload16<(j & 3) * 1024>(wf[j], l16, wsrc + (j >> 2) * 4096);
  };

  const u32 lds0 = (u32)(uintptr_t)(lds_char_p)smem;
  const char* ag = ws + OFF_ACT + ((size_t)(4 * m2) * NFT + 14 * w) * 1024;
  const char* wdg = (const char*)(P.wd + (size_t)(224 * w) * DM + 64 * nt);
  const u32 wdo = (lane >> 3) * 1024 + 16 * ((lane & 7) ^ ((lane >> 2) & 4));
  auto slot_lds = [&](int k) {
    const int sl = k < 5 ? k : k < 9 ? k : k - 9;
    return sl == 4 ? L_SPARE + w * 2048 : ((sl < 4 ? sl : sl - 1) >> 1) * 32768 + w * 4096 + ((sl < 4 ? sl : sl - 1) & 1) * 2048;
  };
  auto stage_wd = [&](int j) { glds16p(wdo, wdg + j * 8192, lds0 + slot_lds(j >> 1) + (j & 1) * 1024); };
  auto stamp_t = [&](int i) { STAMP(i); };
  const int q4 = (lane & 15) >> 2, p4 = lane & 3, hh = lane >> 5, blk = (lane >> 4) & 1;
  auto wd_frag = [&](int j) {
    const char* p = smem + slot_lds(j >> 1) + (8 * hh + q4) * 128 + 64 * ((j & 1) ^ (q4 >> 1)) + 32 * blk + 8 * p4;
    return tr_frag(p, p + 512);
  };
  bf16x8 wr[10];
  u32x4 ae[14];
  auto load_ae = [&](auto J) {
    constexpr int j = decltype(J)::value;
    gload16<(j & 3) * 1024>(ae[j], l16, ag + (size_t)te * NFT * 1024 + (j >> 2) * 4096);
  };

  auto down_phase = [&](auto) {
    stamp_t(28);
    bf16x8 wd[28];
#pragma unroll
    for (int j = 0; j < 28; ++j) wd[j] = j < 10 ? wr[j] : wd_frag(j);
    auto put_partial = [&](int qd, const f32x16& a) {
#pragma unroll
      for (int g = 0; g < 4; ++g)
        *(f32x4*)(smem + qd * 32768 + w * 4096 + g * 1024 + l16) = f32x4{a[4 * g], a[4 * g + 1], a[4 * g + 2], a[4 * g + 3]};
    };
    u32x4 al[14];
    auto load_al = [&](auto J) {
      constexpr int j = decltype(J)::value;
      gload16<(j & 3) * 1024>(al[j], l16, ag + (size_t)tl * NFT * 1024 + (j >> 2) * 4096);
    };
    f32x16 a0 = {}, a1 = {};
    static_for<0, 14>([&](auto J) {
      constexpr int j = decltype(J)::value;
      wait_vm_reg<13 - j>(ae[j]);
      a0 = MFMA32(wd[2 * j], as_frag(ae[j]), a0);
      a1 = MFMA32(wd[2 * j + 1], as_frag(ae[j]), a1);
    });
    pin(a0);
    pin(a1);
    stamp_t(13);
    lds_wait<1>(smem, 6);
    stamp_t(29);
    static_for<0, 14>(load_al);
    put_partial(0, a0);
    put_partial(2, a1);
    a0 = f32x16{};
    a1 = f32x16{};
    static_for<0, 14>([&](auto J) {
      constexpr int j = decltype(J)::value;
      wait_vm_reg<13 - j>(al[j]);
      a0 = MFMA32(wd[2 * j], as_frag(al[j]), a0);
      a1 = MFMA32(wd[2 * j + 1], as_frag(al[j]), a1);
    });
    stamp_t(14);
    put_partial(1, a0);
    put_partial(3, a1);
    stamp_t(15);
    lds_barrier();
    stamp_t(30);
    const int qi = w >> 2, qj = (w >> 1) & 1, g0 = 2 * (w & 1);
    u32x2 o[2];
#pragma unroll
    for (int gg = 0; gg < 2; ++gg) {
      f32x4 s = {0.f, 0.f, 0.f, 0.f};
#pragma unroll
      for (int v = 0; v < 8; ++v) s += *(const f32x4*)(smem + (w >> 1) * 32768 + v * 4096 + (g0 + gg) * 1024 + l16);
      const int cl = 32 * qi + 8 * (g0 + gg) + 4 * hh;
      const f32x4 gb = *(const f32x4*)(smem + L_GB + 4 * cl);
      const u32x2 xr = __builtin_bit_cast(u32x2, tr_read(smem + L_RES + qj * 4096 + (cl + q4) * 64 + 32 * (blk ^ hh) + 8 * p4));
#pragma unroll
      for (int e = 0; e < 2; ++e) {
        o[gg][e] = pack_bf16(bflo(xr[e]) + gb[2 * e] * s[2 * e], bfhi(xr[e]) + gb[2 * e + 1] * s[2 * e + 1]);
      }
    }
    auto s0 = __builtin_amdgcn_permlane32_swap(o[0][0], o[1][0], false, false);
    auto s1 = __builtin_amdgcn_permlane32_swap(o[0][1], o[1][1], false, false);
    const int tok = 128 * m2 + 32 * (qj ? tl : te) + (lane & 31);
    *(u32x4*)(P.out + (size_t)tok * DM + 64 * nt + 32 * qi + 8 * g0 + 8 * hh) = u32x4{s0[0], s1[0], s0[1], s1[1]};
  };

  auto x1_arrive = [&]() {
    if (lds_arrive(smem, 4) == 3) {
      if (lane < 8) st4c(R, OFF_FX + 4096 * lane + 16 * b, tag);
      lds_set(smem, 4, 1);
    }
  };

  u32x2 cv;
  auto mod_load = [&](int part) {
    const int ra = 32 * (b & 15) + 4 * (lane >> 3);
    cv = *(const u32x2*)(P.c + ra);
#pragma unroll
    for (int i = 0; i < 4; ++i) pl[i] = *(const u32x4*)(P.wm + (size_t)(ra + i) * 3072 + 1024 * part + 64 * (b >> 4) + 8 * (lane & 7));
  };
  auto mod_job = [&](int part) {
    pin(cv);
    const u32 sl[2] = {silu2(cv[0]), silu2(cv[1])};
#pragma unroll
    for (int i = 0; i < 4; ++i) pin(pl[i]);
    mod_part<2>(pl, sl, R, OFF_GM + (b & 15) * 24576 + 8192 * part + 512 * (b >> 4), tag, lane);
    stamp_t(33);
  };

  if (w < 4) {
    const int ch = w >> 1, half = w & 1;
    const int c0 = 64 * xcd + 2 * bi;
    const int n = 64 * half + lane;
    int kidx[9];
#pragma unroll
    for (int e = 0; e < 5; ++e) {
      const int l0 = 1023 - 8 * n - 2 * e, l1 = l0 - 1;
      const u32 pb = pack_bf16((float)l0, (float)l1);
      kidx[2 * e] = L_KC + 4 * (w * 656 + ((l0 < 128) ? max(l0, 0) : (int)(pb & 0xffffu) - 133 * 128));
      if (e < 4) kidx[2 * e + 1] = L_KC + 4 * (w * 656 + ((l1 < 128) ? l1 : (int)(pb >> 16) - 133 * 128));
    }
#pragma unroll
    for (int e = 0; e < 9; ++e) pin(kidx[e]);
    if (lane < 2) *(u32x4*)(smem + CV_K + w * CV_KCOPY + 4096 + lane * 16) = u32x4{0, 0, 0, 0};
#pragma unroll
    for (int i = 0; i < 8; ++i) pin(ps[i]);
    MEMFENCE();
    if (half == 0) mod_load(1 + ch);
    const float v_cr = bflo(ps[2]), v_ci = bflo(ps[3]), o_cr = bflo(ps[4]), o_ci = bflo(ps[5]);
    const float dt = rb(expf(bflo(ps[6])));
    const float zr = -rb(expf(bflo(ps[0]))) * dt, zi = bflo(ps[1]) * dt;
    STAMP(3);
    const float mx = wave_max(fmaxf(fmaxf(fabsf(v_cr), fabsf(v_ci)), fmaxf(fabsf(o_cr), fabsf(o_ci))));
    const int ex = min(max((int)(__float_as_uint(mx) >> 23), 1), 252);
    const float sdn = __uint_as_float((u32)(254 - ex) << 23);
    const float sup = __uint_as_float((u32)(ex + 1) << 23) * dt;
    {
      char* base = smem + w * KG_WAVEB + lane * 4;
      auto put = [&](int row, f32x2 v) { *(u32*)(base + row * KG_ROWB) = pack_f16(v); };
      const f32x2 e8 = cexpk(zr, zi, 8.f), e16 = cexpk(zr, zi, 16.f);
      const f32x2 e1 = cexpk(zr, -zi, 1.f), e2 = cexpk(zr, -zi, 2.f);
      const f32x2 e128 = cexpk(zr, -zi, 128.f), e256 = cexpk(zr, -zi, 256.f);
      f32x2 f0 = e16, f1 = e8;
      put(41, f32x2{1.f, 0.f});
      put(42, e8);
#pragma unroll
      for (int j = 2; j < 16; j += 2) {
        f1 = cmul(f1, e16);
        put(41 + j, f0);
        put(42 + j, f1);
        if (j < 14) f0 = cmul(f0, e16);
      }
      const f32x2 e4 = cmul(e2, e2);
      f32x2 g[2];
      g[0] = f32x2{v_cr * sdn, -v_ci * sdn};
      g[1] = cmul(g[0], e128);
#pragma unroll
      for (int i = 0; i < 17; ++i) {
        const f32x2 gi = g[i & 1];
        if (i < 2) {
          f32x2 a = gi, c = cmul(gi, e1);
#pragma unroll
          for (int e = 0; e < 8; e += 2) {
            put(8 * i + e, a);
            put(8 * i + e + 1, c);
            if (e < 6) { a = cmul(a, e2); c = cmul(c, e2); }
          }
        } else if (i < 4) {
          const f32x2 c = cmul(gi, e2);
          put(16 + 4 * (i - 2), gi);
          put(17 + 4 * (i - 2), c);
          put(18 + 4 * (i - 2), cmul(gi, e4));
          put(19 + 4 * (i - 2), cmul(c, e4));
        } else if (i < 8) {
          put(24 + 2 * (i - 4), gi);
          put(25 + 2 * (i - 4), cmul(gi, e4));
        } else {
          put(32 + (i - 8), gi);
        }
        if (i + 2 < 17) g[i & 1] = cmul(gi, e256);
      }
    }
    lds_set(smem, 0, w);
    STAMP(4);
    pin(cv);
#pragma unroll
    for (int i = 0; i < 4; ++i) pin(pl[i]);
    if (half == 0) mod_job(1 + ch);
    const int r = lane & 15, q = lane >> 4;
    {
      f32x4 acc[3] = {};
#pragma unroll
      for (int hf = 0; hf < 2; ++hf) {
        if (hf == 0) {
          if (ch) lds_wait<12>(smem, 0);
          else lds_wait<3>(smem, 0);
        }
        const char* rbase = smem + (2 * ch + hf) * KG_WAVEB + r * KG_ROWB + q * 16;
        f16x8 fr[4][4];
#pragma unroll
        for (int ks = 0; ks < 4; ++ks) {
          fr[ks][3] = *(const f16x8*)(rbase + ks * 64 + 41 * KG_ROWB);
#pragma unroll
          for (int t = 0; t < 3; ++t) fr[ks][t] = *(const f16x8*)(rbase + ks * 64 + 16 * t * KG_ROWB);
        }
        __builtin_amdgcn_sched_barrier(0);
#pragma unroll
        for (int ks = 0; ks < 4; ++ks)
#pragma unroll
          for (int t = 0; t < 3; ++t) acc[t] = __builtin_amdgcn_mfma_f32_16x16x32_f16(fr[ks][t], fr[ks][3], acc[t], 0, 0, 0);
      }
      pin(acc[0]); pin(acc[1]); pin(acc[2]);
      STAMP(12);
      float* kc = (float*)(smem + L_KC) + w * 656;
      *(f32x4*)(kc + 128 * (q >> 1) + 4 * (q & 1) + 8 * r) = acc[0] * sup;
      if (q < 2) {
        *(f32x4*)(kc + 256 + 64 * q + 4 * r) = acc[1] * sup;
      } else {
        *(f32x2*)(kc + 384 + 64 * (q - 2) + 2 * r) = f32x2{acc[1][0], acc[1][1]} * sup;
        *(f32x2*)(kc + 416 + 64 * (q - 2) + 2 * r) = f32x2{acc[1][2], acc[1][3]} * sup;
      }
#pragma unroll
      for (int e = 0; e < 4; ++e)
        if (4 * q + e <= 8) kc[512 + 16 * (4 * q + e) + r] = acc[2][e] * sup;
      MEMFENCE();
      float kv[9];
#pragma unroll
      for (int e = 0; e < 9; ++e) kv[e] = *(const float*)(smem + kidx[e]);
      const float ka = kc[639 - n], kb = kc[640 - n];
      if (n == 127) kv[8] = 0.f;
      const float kt = (n & 1) ? ka : kb;
      const u32 aa = pack_bf16(ka, ka), bb = pack_bf16(kb, kb);
      *(u32x4*)(smem + CV_K + (2 * ch) * CV_KCOPY + 16 * n) = u32x4{bb, pack_bf16(kb, kt), aa, aa};
      *(u32x4*)(smem + CV_K + (2 * ch + 1) * CV_KCOPY + 16 * n) = u32x4{bb, pack_bf16(kt, ka), aa, aa};
      u32x4 o, s;
#pragma unroll
      for (int e = 0; e < 4; ++e) {
        o[e] = pack_bf16(kv[2 * e], kv[2 * e + 1]);
        s[e] = pack_bf16(kv[2 * e + 1], kv[2 * e + 2]);
      }
      *(u32x4*)(smem + CV_K + (2 * ch) * CV_KCOPY + 2048 + 16 * n) = o;
      *(u32x4*)(smem + CV_K + (2 * ch + 1) * CV_KCOPY + 2048 + 16 * n) = s;
    }
    lds_set(smem, 8, w);
    STAMP(5);
    lds_wait2<15, 15>(smem, 8, 2);
    STAMP(7);
    const char* abase = smem + CV_K + (2 * ch + ((r & 1) ^ 1)) * CV_KCOPY + 2 * (2030 - (r & ~1) + 8 * q) - 64 * (4 * half + 3 + 56);
    const char* bbase = smem + CV_H + ch * CV_HB + 2 * (224 + 16 * r + 8 * q) - 64 * (4 * half + 3);
    f32x4 acc[8] = {};
    bf16x8 af[2][8], bfr[2][8];
    u32 ab[4];
#pragma unroll
    for (int i = 0; i < 4; ++i) {
      ab[i] = (u32)(uintptr_t)(lds_char_p)abase + 1024 * i;
      pin(ab[i]);
    }
    auto ldfr = [&](int rho, int i) {
      const lds_u32_p p = (lds_u32_p)(uintptr_t)(ab[(7 - i) >> 1] + 64 * (3 - rho) + 512 * ((7 - i) & 1));
      af[rho & 1][i] = as_frag(u32x4{p[0], p[1], p[2], p[3]});
      bfr[rho & 1][i] = *(const bf16x8*)(bbase + 64 * (3 - rho) + 512 * i);
    };
#pragma unroll
    for (int i = 0; i < 4; ++i) ldfr(0, i);
    static_for<0, 32>([&](auto S) {
      constexpr int st = decltype(S)::value, rho = st >> 3, m = st & 7;
      if constexpr (st + 4 < 32) ldfr((st + 4) >> 3, (st + 4) & 7);
#pragma unroll
      for (int o = 0; o <= m; ++o) {
        if (m + o < 8) {
          acc[m + o] = MFMA16(af[rho & 1][m], bfr[rho & 1][o], acc[m + o]);
          if (o != m) acc[m + o] = MFMA16(af[rho & 1][o], bfr[rho & 1][m], acc[m + o]);
        }
      }
#pragma unroll
      for (int i = 0; i < 3; ++i) {
        __builtin_amdgcn_sched_group_barrier(0x008, 1, 0);
        __builtin_amdgcn_sched_group_barrier(0x100, 1, 0);
      }
      __builtin_amdgcn_sched_barrier(0);
    });
    STAMP(8);
#pragma unroll
    for (int t = 0; t < 8; ++t) *(f32x4*)(smem + CV_P + w * 8192 + ch * 16 + 1024 * t + 64 * r + 16 * (q ^ ((r >> 1) & 3))) = acc[t];
    lds_set(smem, 3, w);
    {
      const int c = (lane >> 2) & 1;
      lds_wait2<15, 14>(smem, 3, 10);
      const float ga = *(const float*)(smem + L_EP + 4 * c), cb1 = *(const float*)(smem + L_EP + 8 + 4 * c), dk = bflo(ps[7]);
      STAMP(11);
#pragma unroll
      for (int i = 0; i < 2; ++i) {
        const int e = tid + 256 * i;
        const int l0 = 32 * (e >> 3) + 8 * ((e & 3) ^ (2 * ((c0 >> 2) & 1)));
        const char* pp = smem + CV_P + c * (16384 + 16);
        const int po = (4 * l0) ^ (16 * ((l0 >> 5) & 3));
        const f32x4 y0 = *(const f32x4*)(pp + po) + *(const f32x4*)(pp + 8192 + po);
        const f32x4 y1 = *(const f32x4*)(pp + (po ^ 16)) + *(const f32x4*)(pp + 8192 + (po ^ 16));
        const u32x4 hv = *(const u32x4*)(smem + CV_H + c * CV_HB + 480 + 2 * l0);
        const u32x4 xx = *(const u32x4*)(smem + CV_X + c * 4096 + 2 * l0);
        u32x4 o, os;
#pragma unroll
        for (int k = 0; k < 4; ++k) {
          const u32 yb = k < 2 ? pack_bf16(y0[2 * k], y0[2 * k + 1]) : pack_bf16(y1[2 * k - 4], y1[2 * k - 3]);
          const u32 hd = pack_bf16(bflo(hv[k]) * dk, bfhi(hv[k]) * dk);
          const u32 z = pack_bf16(bflo(yb) + bflo(hd), bfhi(yb) + bfhi(hd));
          const u32 gz = pack_bf16(ga * bflo(z), ga * bfhi(z));
          o[k] = pack_bf16(bflo(xx[k]) + bflo(gz), bfhi(xx[k]) + bfhi(gz));
          os[k] = pack_bf16(bflo(o[k]) * cb1, bfhi(o[k]) * cb1);
        }
        const u32 go = (e >> 3) * 32768 + c0 * 64 + (e & 7) * 16;
        st16c(R, OFF_XS + go, os);
        st16c(R, OFF_X1 + go, o);
      }
      STAMP(16);
      drain_vm();
      x1_arrive();
      STAMP(9);
    }
  } else {
    __builtin_amdgcn_s_setprio(2);
    const int un = min(b, 223), cg = un >> 3, type = (un >> 2) & 1, k8 = 2 * (un & 3) + (w & 1);
    const int kg = lane >> 3, cp = lane & 7;
    if (w < 6) {
      if (w == 4) {
#pragma unroll
        for (int a = 0; a < 4; ++a) {
          pin(pl[2 * a]);
          pin(pl[2 * a + 1]);
#pragma unroll
          for (int k = 0; k < 4; ++k)
            st16c(R, OFF_XG + b * 16384 + (a * 4 + k) * 1024 + l16, u32x4{pl[2 * a][k], tag, pl[2 * a + 1][k], tag});
        }
        STAMP(3);
        float s[2][2] = {}, qq[2][2] = {};
#pragma unroll
        for (int i = 0; i < 8; ++i)
#pragma unroll
          for (int k = 0; k < 4; ++k) {
            s[i & 1][k & 1] = dot2(pl[i][k], ONES2, s[i & 1][k & 1]);
            qq[i & 1][k & 1] = dot2(pl[i][k], pl[i][k], qq[i & 1][k & 1]);
          }
        float v = rs16(rs32(s[0][0] + s[0][1], qq[0][0] + qq[0][1]), rs32(s[1][0] + s[1][1], qq[1][0] + qq[1][1]));
        v += dpp<0x124>(v);
        v += dpp<0x128>(v);
        auto sw = __builtin_amdgcn_permlane32_swap(__float_as_uint(v), __float_as_uint(v), false, false);
        const u32 pk = ln_pack(__uint_as_float(sw[0]), __uint_as_float(sw[1]));
        if (lane < 32 && (lane & 15) < 4) st8c(R, OFF_GST + 64 * b + 16 * (lane & 3) + 8 * (lane >> 4), u32x2{pk, tag});
        STAMP(33);
        drain_vm();
        lds_set(smem, 5, 1);
        RSTAMP(36);
      } else {
        cv = u32x2{ps[0], ps[1]};
        mod_job(0);
      }
      const int t = tid - 256, c0 = 64 * xcd + 2 * bi, pr = c0 >> 1;
      const u32 xo = OFF_XG + (t >> 2) * 16384 + (((pr >> 6) * 4 + (pr & 3)) * 16 + ((pr >> 2) & 15)) * 64 + (t & 3) * 16;
      const u32 mo = OFF_GM + (lane >> 2) * 24576 + 8 * (((lane >> 1) & 1) * DM + c0 + (lane & 1));
      if (lane < 30) *(u32x4*)(smem + CV_H + (w - 4) * CV_HB + lane * 16) = u32x4{0, 0, 0, 0};
      u32x4 gx[8], gs[8];
      u32x2 gm;
      u32 bad = 1;
      lds_wait<2>(smem, 5);
      __builtin_amdgcn_s_sleep(GDELAY);
      STAMP(12);
#pragma unroll 1
      for (int spin = 0; spin < SPIN_CAP; ++spin) {
        if (bad) {
          gm = ld8c(R, mo);
#pragma unroll
          for (int i = 0; i < 8; ++i) {
            gx[i] = ld16c(R, xo + i * (32 * 16384));
            gs[i] = ld16c(R, OFF_GST + 16 * t + 2048 * i);
          }
          bad = gm[1] ^ tag;
#pragma unroll
          for (int i = 0; i < 8; ++i) bad |= (gx[i][1] ^ tag) | (gx[i][3] ^ tag) | (gs[i][1] ^ tag) | (gs[i][3] ^ tag);
        }
        if (__all(bad == 0)) break;
        __builtin_amdgcn_s_sleep(8);
        MEMFENCE();
      }
      drain_vm();
      if (w == 5) lds_set(smem, 10, 0);
      STAMP(6);
      RSTAMP(37);
      const float md = rb(sum_hi4(__uint_as_float(gm[0])));
      const f32x2 sa = {rdlane(md, 0), rdlane(md, 1)}, ca1 = {rb(1.f + rdlane(md, 2)), rb(1.f + rdlane(md, 3))};
#pragma unroll
      for (int i = 0; i < 8; ++i) {
        f32x2 hv[2];
#pragma unroll
        for (int e = 0; e < 2; ++e) {
          const u32 st = gs[i][2 * e], xv = gx[i][2 * e];
          hv[e] = __builtin_elementwise_fma((f32x2{bflo(xv), bfhi(xv)} - bflo(st)) * __builtin_amdgcn_rcpf(bfhi(st)), ca1, sa);
        }
        *(u32*)(smem + CV_H + 480 + 512 * i + 4 * t) = pack_bf16(hv[0].x, hv[1].x);
        *(u32*)(smem + CV_H + CV_HB + 480 + 512 * i + 4 * t) = pack_bf16(hv[0].y, hv[1].y);
      }
      lds_set(smem, 2, w - 4);
      STAMP(7);
#pragma unroll
      for (int i = 0; i < 8; ++i) {
        *(u32*)(smem + CV_X + 512 * i + 4 * t) = perm_lo(gx[i][0], gx[i][2]);
        *(u32*)(smem + CV_X + 4096 + 512 * i + 4 * t) = perm_hi(gx[i][0], gx[i][2]);
      }
      lds_set(smem, 10, w - 2);
    } else {
      u32x4 t[8];
#pragma unroll
      for (int i = 0; i < 8; ++i) pin(pl[i]);
      transpose8(pl, t);
      pin(code_touch);
      STAMP(3);
      {
        char* tb = smem + L_TR + (w & 1) * 8192;
#pragma unroll
        for (int n = 0; n < 8; ++n)
          *(u32x4*)(tb + ((cp >> 1) * 8 + kg) * 256 + (((n & 3) + 8 * (n >> 2) + 4 * (cp & 1)) ^ (cp >> 1)) * 16) = t[n];
        MEMFENCE();
        if (b < 224) {
#pragma unroll
          for (int k = 0; k < 8; ++k) {
            const int row = 4 * k + (lane >> 4), fl = k >> 1;
            const u32x4 o = *(const u32x4*)(tb + row * 256 + ((lane & 15) ^ fl) * 16);
            st16c(R, OFF_WP + (((4 * cg + fl) * 32 + 4 * k8) * 64 + (row & 7) * 32 + 16 * type + (lane & 15)) * 16, o);
          }
        }
      }
      STAMP(4);
      asm volatile("s_waitcnt lgkmcnt(0)" ::: "memory");
      lds_set(smem, 2, w - 4);
      drain_vm();
      if (lane == 0 && b < 224) st4c(R, OFF_FW + (cg * 16 + type * 8 + k8) * 64, tag);
      STAMP(12);
      lds_wait<1>(smem, 10);
      TP(2);
      if (w == 6) {
#pragma unroll 1
        for (int spin = 0; spin < SPIN_CAP; ++spin) {
          const u32 f = ld4c(R, OFF_FW + min(((7 * fb) >> 2) * 16 + lane, ((7 * fb + 6) >> 2) * 16 + 15) * 64);
          if (__all(f == tag)) break;
          __builtin_amdgcn_s_sleep(10);
          MEMFENCE();
        }
        drain_vm();
        lds_wait<2>(smem, 10);
        lds_set(smem, 5, 0);
        STAMP(5);
      } else {
        {
          const int c0 = 64 * xcd + 2 * bi;
          float v = gather_sum<2>(R, OFF_GM + (lane >> 3) * 49152 + 8 * ((2 + ((lane >> 1) & 3)) * DM + c0 + (lane & 1)), 24576, tag);
          v += dpp<0x128>(v);
          v = rs16(v, v);
          v = rb(rs32(v, v));
          if ((lane & 6) == 4) v = rb(1.f + v);
          if (lane < 8) {
            if ((lane & 2) == 0) *(float*)(smem + L_EP + 2 * (lane & 4) + 4 * (lane & 1)) = v;
            if (lane >= 2) st8c(R, OFF_FM + ((lane >> 1) == 2 ? 0 : (lane >> 2) + 1) * 4096 + 8 * (c0 + (lane & 1)), u32x2{__float_as_uint(v), tag});
          }
          lds_set(smem, 10, 1);
          STAMP(7);
        }
#pragma unroll
        for (int i = 0; i < 7; ++i) t[i] = *(const u32x4*)(P.wd + (size_t)(7 * b + i) * DM + 8 * lane);
#pragma unroll
        for (int i = 0; i < 7; ++i) pin(t[i]);
        lds_wait<15>(smem, 3);
        TP(3);
#pragma unroll 1
        for (int spin = 0; spin < SPIN_CAP; ++spin) {
          u32 bad = 0;
#pragma unroll
          for (int i = 0; i < 8; ++i) t[i] = ld16c(R, OFF_FM + i * 1024 + l16);
#pragma unroll
          for (int i = 0; i < 8; ++i) bad |= (t[i][1] ^ tag) | (t[i][3] ^ tag);
          if (__all(bad == 0)) break;
          __builtin_amdgcn_s_sleep(8);
          MEMFENCE();
        }
#pragma unroll
        for (int i = 0; i < 8; ++i) *(u32*)(smem + L_V + 256 * i + 4 * lane) = perm_hi(t[i][0], t[i][2]);
        lds_set(smem, 5, 2);
        STAMP(5);
      }
    }
    __builtin_amdgcn_s_setprio(0);
    if (w == 7) {
    const char* xsg = ws + OFF_XS + (size_t)(4 * mt) * 32768;
    const char* x1g = ws + OFF_X1 + (size_t)(4 * mt) * 32768;
    auto dma = [&](const char* src, int dst, int n) {
#pragma unroll 4
      for (int i = 0; i < n; ++i) glds16(l16, src + i * 1024, lds0 + dst + i * 1024);
    };
    const u32 fao = OFF_FA + mt * 4096;
    lds_wait<4>(smem, 5);
    lds_wait<15>(smem, 1);
#pragma unroll 1
    for (int spin = 0; spin < SPIN_CAP; ++spin) {
      u32 f[4];
#pragma unroll
      for (int i = 0; i < 4; ++i) f[i] = ld4c(R, OFF_FX + 4096 * xcd + (lane + 64 * i) * 16);
      if (__all(((f[0] ^ tag) | (f[1] ^ tag) | (f[2] ^ tag) | (f[3] ^ tag)) == 0)) break;
      __builtin_amdgcn_s_sleep(1);
      MEMFENCE();
    }
    drain_vm();
    TP(4);
    STAMP(10);
    lds_set(smem, 6, 1);
    if (b == 0 && lane == 0) st4c(R, OFF_E, tag);
    dma(x1g + (fb >> 2) * 32768 + (fb & 3) * 8192, L_SPARE, 8);
    barrier();
    STAMP(17);
    dma(xsg + 35840, 35840, 13);
    wait_vm<13>();
    {
      const int g = lane >> 4;
      const char* sb = smem + L_SPARE + (8 * (g >> 1) + 4 * (g & 1) + q4) * 64 + 8 * p4;
      const bf16x8 ones = as_frag(u32x4{ONES2, ONES2, ONES2, ONES2});
      bf16x8 f[2][4];
#pragma unroll
      for (int k2 = 0; k2 < 4; ++k2)
#pragma unroll
        for (int c = 0; c < 2; ++c) {
          const char* p = sb + 32 * (c ^ (g & 1)) + k2 * 2048;
          f[c][k2] = tr_frag(p, p + 1024);
        }
      __builtin_amdgcn_sched_barrier(0);
#pragma unroll
      for (int c = 0; c < 2; ++c) {
        f32x4 aq = {}, as = {};
#pragma unroll
        for (int k2 = 0; k2 < 4; ++k2) {
          aq = MFMA16(f[c][k2], f[c][k2], aq);
          as = MFMA16(ones, f[c][k2], as);
        }
        const float sq = p4 == 0 ? aq[0] : p4 == 1 ? aq[1] : p4 == 2 ? aq[2] : aq[3];
        if (g == q4)
          st16c(R, OFF_GS2 + ((mt * 4 + (fb & 3)) * 128 + 32 * (fb >> 2) + 16 * c + (lane & 15)) * 16,
                u32x4{__float_as_uint(as[0]), tag, __float_as_uint(sq), tag});
      }
    }
    dma(xsg + 49152, 49152, 16);
    lds_barrier();
    dma(xsg + 65536, 65536, 36);
    wait_vm<28>();
    stamp_t(18);
    barrier();
    stamp_t(19);
    {
      u32x4 g[2][4];
#pragma unroll 1
      for (int spin = 0; spin < SPIN_CAP; ++spin) {
        u32 bad = 0;
#pragma unroll
        for (int i = 0; i < 8; ++i) g[i & 1][i >> 1] = ld16c(R, OFF_GS2 + mt * 8192 + i * 1024 + l16);
#pragma unroll
        for (int i = 0; i < 8; ++i) bad |= (g[i & 1][i >> 1][1] ^ tag) | (g[i & 1][i >> 1][3] ^ tag);
        if (__all(bad == 0)) break;
        __builtin_amdgcn_s_sleep(4);
        MEMFENCE();
      }
      drain_vm();
#pragma unroll
      for (int c = 0; c < 2; ++c) {
        const float s1 = (__uint_as_float(g[c][0][0]) + __uint_as_float(g[c][1][0])) + (__uint_as_float(g[c][2][0]) + __uint_as_float(g[c][3][0]));
        const float s2 = (__uint_as_float(g[c][0][2]) + __uint_as_float(g[c][1][2])) + (__uint_as_float(g[c][2][2]) + __uint_as_float(g[c][3][2]));
        const u32 ms = ln_pack(s1, s2) ^ 0x8000u;
        *(u32x4*)(smem + L_SF + (64 * c + lane) * 16) = u32x4{perm_lo(ms, ms), ms, perm_hi(ms, ms), 0};
        const float k0 = -2.f * 0.7978845608028654f * 1.4426950408889634f, k1 = k0 * 0.044715f;
        const float sd = bfhi(ms), rs = __builtin_amdgcn_rcpf(sd);
        *(f32x4*)(smem + L_RS + (64 * c + lane) * 16) = f32x4{k0 * rs, k1 * rs * rs * rs, sd * sd, 0.f};
      }
    }
    lds_set(smem, 7, 0);
    stamp_t(8);
    dma(xsg + 102400, 102400, 28);
    stamp_t(20);
    lds_barrier();
    stamp_t(21);
    dma(ws + OFF_X1 + (size_t)(4 * m2 + te) * 32768 + nt * 4096, L_RES, 4);
    dma(ws + OFF_X1 + (size_t)(4 * m2 + tl) * 32768 + nt * 4096, L_RES + 4096, 4);
#pragma unroll
    for (int j = 0; j < 10; ++j) stage_wd(j);
    *(float*)(smem + L_GB + 4 * lane) = gather_sum<1>(R, OFF_FM + 2 * 4096 + 8 * (64 * nt + lane), 0, tag);
    drain_vm();
#pragma unroll
    for (int j = 0; j < 10; ++j) wr[j] = wd_frag(j);
#pragma unroll
    for (int j = 0; j < 10; ++j) pin(wr[j]);
#pragma unroll
    for (int j = 18; j < 28; ++j) stage_wd(j);
    stamp_t(22);
    lds_barrier();
    stamp_t(23);
    if (lane < 2) st4c(R, fao + lane * 1024 + fb * 64, tag);
#pragma unroll
    for (int j = 10; j < 14; ++j) stage_wd(j);
    __builtin_amdgcn_s_sleep(16);
    poll_flag(R, OFF_FA + m2 * 4096 + te * 1024 + (lane & 15) * 64, tag);
    static_for<0, 14>(load_ae);
    stamp_t(24);
    barrier();
    stamp_t(25);
    TP(5);
    if (lane == 0) st4c(R, fao + 2 * 1024 + fb * 64, tag);
#pragma unroll
    for (int j = 14; j < 18; ++j) stage_wd(j);
    barrier();
    barrier();
    if (lane == 0) st4c(R, fao + 3 * 1024 + fb * 64, tag);
    __builtin_amdgcn_s_sleep(20);
    poll_flag(R, OFF_FA + m2 * 4096 + tl * 1024 + (lane & 15) * 64, tag);
    lds_set(smem, 6, 0);
    drain_vm();
    down_phase(std::integral_constant<int, 1>{});
    TP(6);
    { const u64 te_ = __builtin_amdgcn_s_memrealtime() + 2 * (tln - tl0); while (__builtin_amdgcn_s_memrealtime() < te_) __builtin_amdgcn_s_sleep(2); }
  }
  }

  if (w < 7) {
    lds_wait<1>(smem, 5);
    if (w >= 4) {
      lds_wait<3>(smem, 2);
      static_for<0, NPARK>(load_w);
    } else {
      lds_wait<2>(smem, 4);
    }
    static_for<NPARK, 32>(load_w);
    if (w >= 4) {
      lds_wait<15>(smem, 8);
#pragma unroll 4
      for (int i = 0; i < 4 * NPARK / 3; ++i) {
        const int f = 3 * i + (w - 4), tw = f & 3, k = f >> 2;
        glds16(l16, ws + OFF_WP + (size_t)(7 * fb + tw) * 32768 + k * 1024, lds0 + L_PARK + (tw * NPARK + k) * 1024);
      }
      drain_vm();
      lds_set(smem, 9, w - 4);
      STAMP(9);
      lds_wait<15>(smem, 3);
    } else {
      lds_wait<7>(smem, 9);
#pragma unroll
      for (int j = 0; j < NPARK; ++j) wf[j] = *(const u32x4*)(smem + L_PARK + (w * NPARK + j) * 1024 + l16);
    }
    u32x4 aux;
    {
      lds_wait<4>(smem, 5);
      const char* vb = smem + L_V + (lane & 1) * 1024 + 16 * hh;
      f32x16 d = {};
      bf16x8 va[32];
#pragma unroll
      for (int j = 0; j < 12; ++j) va[j] = *(const bf16x8*)(vb + 32 * j);
      static_for<0, 32>([&](auto J) {
        constexpr int j = decltype(J)::value;
        if constexpr (j + 12 < 32) va[j + 12] = *(const bf16x8*)(vb + 32 * (j + 12));
        if constexpr (j >= NPARK) wait_vm_reg<31 - j>(wf[j]);
        d = MFMA32(va[j], as_frag(wf[j]), d);
        pin(d);
        __builtin_amdgcn_sched_barrier(0);
      });
      asm volatile("s_waitcnt lgkmcnt(0)" ::: "memory");
      if (w < 4) lds_set(smem, 1, w);
      const float c = d[0], sf = d[1];
      const float c0 = rb(c), c1 = rb(c - c0), b0 = rb(sf), b1 = rb(sf - b0);
      aux = u32x4{pack_bf16(c0, c1), pack_bf16(c - c0 - c1, b0), pack_bf16(b1, sf - b0 - b1), 0};
      if (hh) aux = u32x4{0, 0, 0, 0};
    }
    STAMP(10);
    u32 xb[2][2];
#pragma unroll
    for (int i = 0; i < 2; ++i) {
      xb[i][0] = lds0 + 65536 * i + (8 * hh + q4) * 64 + 32 * blk + 8 * p4;
      xb[i][1] = lds0 + 65536 * i + (8 * hh + 4 + q4) * 64 + 32 * (blk ^ 1) + 8 * p4;
      pin(xb[i][0]);
      pin(xb[i][1]);
    }
    auto xfrag = [&](int s) { return tr_frag_at(xb[s >> 6][0] + (s & 63) * 1024, xb[s >> 6][1] + (s & 63) * 1024); };
    lds_wait<2>(smem, 6);
#pragma unroll
    for (int i = 0; i < 5; ++i) glds16(l16, ws + OFF_XS + (size_t)(4 * mt) * 32768 + (7 * i + w) * 1024, lds0 + (7 * i + w) * 1024);
    wait_vm<2>();
    barrier();
    STAMP(17);
    constexpr int XD = 3;
    bf16x8 xf[4];
#pragma unroll
    for (int s = 0; s < XD; ++s) xf[s] = xfrag(s);
    const char* actg = ws + OFF_ACT + ((size_t)(4 * mt) * NFT + ft) * 1024;
    f32x16 acc[3];
    f32x4 gcs[2];
    float ev = 0.f, gu = 0.f, prev = 0.f;
    u32x4 o;
    auto epi_a = [&](int t, int i) {
      const f32x4& gc = gcs[t & 1];
      const float a = acc[t % 3][i];
      ev = __builtin_amdgcn_exp2f(a * __builtin_fmaf(a * a, gc[1], gc[0]));
      gu = a * acc[t % 3][8 + i];
      pin(ev);
      pin(gu);
    };
    auto epi_b = [&](int t, int i) {
      float v = gu * __builtin_amdgcn_rcpf(__builtin_fmaf(ev, gcs[t & 1][2], gcs[t & 1][2]));
      pin(v);
      if (i & 1) o[i >> 1] = pack_bf16(prev, v);
      else prev = v;
      if (i == 7) gstore16(o, l16, actg + (size_t)t * NFT * 1024);
    };
    auto fold = [&](int t) {
      const bf16x8 sf = *(const bf16x8*)(smem + L_SF + t * 512 + (lane & 31) * 16);
      gcs[t & 1] = *(const f32x4*)(smem + L_RS + t * 512 + (lane & 31) * 16);
      acc[t % 3] = MFMA32(as_frag(aux), sf, acc[t % 3]);
    };
    constexpr int ES0 = 56;
    static_for<0, 4>([&](auto T) {
      constexpr int t = decltype(T)::value;
      static_for<0, 32>([&](auto KS) {
        constexpr int ks = decltype(KS)::value, s = 32 * t + ks;
        if constexpr (t == 0 && ks == 21 - XD) {
          wait_vm<0>();
          barrier();
        }
        if constexpr (s == ES0 - 3) lds_wait<1>(smem, 7);
        if constexpr (s + XD < 128) xf[(s + XD) & 3] = xfrag(s + XD);
        if constexpr (ks == 0) acc[t % 3] = MFMA32(as_frag(wf[0]), xf[s & 3], f32x16{});
        else acc[t % 3] = MFMA32(as_frag(wf[ks]), xf[s & 3], acc[t % 3]);
        pin(acc[t % 3]);
        if constexpr (s == ES0 - 3) fold(0);
        if constexpr (s == 64) fold(1);
        if constexpr (s == 96) fold(2);
        if constexpr (s >= ES0 && s < ES0 + 32) {
          if constexpr (((s - ES0) & 1) == 0) epi_a((s - ES0) >> 4, ((s - ES0) >> 1) & 7);
          else epi_b((s - ES0) >> 4, ((s - ES0) >> 1) & 7);
        }
        if constexpr (s >= 99 && s < 115) {
          if constexpr (((s - 99) & 1) == 0) epi_a(2, (s - 99) >> 1);
          else epi_b(2, (s - 99) >> 1);
        }
        if constexpr (t == 1 && ks >= 8 && ks < 20 && (ks & 1) == 0) stage_wd(ks < 16 ? (ks - 8) / 2 : ks / 2);
        if constexpr (t == 2 && ks < 16 && (ks & 3) == 1) stage_wd(4 + ks / 4);
        if constexpr (t == 3 && ks < 8 && (ks & 1) == 0) stage_wd(10 + ks / 2);
        if constexpr (t == 3 && ks >= 8 && ks < 18) wr[ks - 8] = wd_frag(ks - 8);
        if constexpr (t == 3 && ks == 21) {
#pragma unroll
          for (int j = 0; j < 10; ++j) pin(wr[j]);
        }
        if constexpr (t == 3 && ks >= 22) stage_wd(18 + ks - 22);
        __builtin_amdgcn_sched_barrier(0);
      });
      stamp_t(18 + 2 * t);
      if constexpr (t == 2) wait_vm<0>();
      if constexpr (t == 3) wait_vm<10>();
      lds_barrier();
      stamp_t(19 + 2 * t);
    });
    fold(3);
#pragma unroll
    for (int i = 0; i < 8; i += 2) {
      float v[2];
#pragma unroll
      for (int e = 0; e < 2; ++e) {
        const f32x4& gc = gcs[1];
        const float a = acc[0][i + e];
        const float ex = __builtin_amdgcn_exp2f(a * __builtin_fmaf(a * a, gc[1], gc[0]));
        v[e] = a * acc[0][8 + i + e] * __builtin_amdgcn_rcpf(__builtin_fmaf(ex, gc[2], gc[2]));
      }
      o[i >> 1] = pack_bf16(v[0], v[1]);
    }
    gstore16(o, l16, actg + (size_t)3 * NFT * 1024);
    STAMP(26);
    barrier();
#pragma unroll
    for (int j = 14; j < 18; ++j) stage_wd(j);
    static_for<0, 4>(load_ae);
    wait_vm<8>();
    STAMP(27);
    barrier();
    static_for<4, 14>(load_ae);
    wait_vm<14>();
    down_phase(std::integral_constant<int, 0>{});
  }
  DIAG_END(ws);
}

extern "C" void kernel_launch(void* const* d_in, const int* in_sizes, int n_in, void* d_out, int out_size, void* d_ws,
                              size_t ws_size, hipStream_t stream) {
  Params p;
  p.x = (const u16*)d_in[0];
  p.la = (const u16*)d_in[1];
  p.ai = (const u16*)d_in[2];
  p.cr = (const u16*)d_in[3];
  p.ci = (const u16*)d_in[4];
  p.ldt = (const u16*)d_in[5];
  p.dsk = (const u16*)d_in[6];
  p.wg = (const u16*)d_in[8];
  p.wu = (const u16*)d_in[9];
  p.wd = (const u16*)d_in[10];
  p.c = (const u16*)d_in[11];
  p.wm = (const u16*)d_in[12];
  p.out = (u16*)d_out;
  p.ws = (char*)d_ws;
  (void)hipFuncSetAttribute((const void*)fused_block, hipFuncAttributeMaxDynamicSharedMemorySize, SMEM);
  fused_block<<<256, 512, SMEM, stream>>>(p);
}
```

```cpp
#include <hip/hip_runtime.h>
#include <hip/hip_bf16.h>
#include <stdint.h>
#include <type_traits>

typedef unsigned short u16;
typedef unsigned int u32;
typedef unsigned long long u64;
typedef __attribute__((ext_vector_type(8))) __bf16 bf16x8;
typedef __attribute__((ext_vector_type(4))) __bf16 bf16x4;
typedef __attribute__((ext_vector_type(2))) __bf16 bf16x2;
typedef __attribute__((ext_vector_type(2))) _Float16 f16x2;
typedef __attribute__((ext_vector_type(8))) _Float16 f16x8;
typedef __attribute__((ext_vector_type(2))) float f32x2;
typedef __attribute__((ext_vector_type(4))) float f32x4;
typedef __attribute__((ext_vector_type(16))) float f32x16;
typedef __attribute__((ext_vector_type(4))) u32 u32x4;
typedef __attribute__((ext_vector_type(2))) u32 u32x2;
typedef __attribute__((address_space(3))) bf16x4* lds_bf16x4_p;
typedef __attribute__((address_space(3))) char* lds_char_p;
typedef __attribute__((address_space(3))) u32* lds_u32_p;
typedef __amdgpu_buffer_rsrc_t rsrc_t;

#define DEVI __device__ __forceinline__

constexpr int SL = 2048, DM = 512, NS = 128, DF = 1792;
constexpr int NFT = DF / 16;
constexpr int SPIN_CAP = 1 << 15;
#define GDELAY 2
#define WDELAY 6
constexpr int CODE_BYTES = 38 * 1024;

constexpr u32 OFF_E = 0;
constexpr u32 OFF_GST = 4096;
constexpr u32 OFF_FX = 32768;
constexpr u32 OFF_FW = 81920;
constexpr u32 OFF_FA = 131072;
constexpr u32 OFF_FM = 114688;
constexpr u32 OFF_GS2 = 196608;
constexpr u32 OFF_WP = 3u << 20;
constexpr u32 OFF_X1 = OFF_WP + (4u << 20);
constexpr u32 OFF_ACT = OFF_X1 + (2u << 20);
constexpr u32 OFF_XG = OFF_ACT + (8u << 20);
constexpr u32 OFF_XS = OFF_XG + (4u << 20);
constexpr u32 OFF_GM = OFF_XS + (2u << 20);
constexpr u32 WS_USED = OFF_GM + 16 * 24576;

constexpr int KG_ROWB = 272;
constexpr int KG_WAVEB = 57 * KG_ROWB;
constexpr int L_KC = 4 * KG_WAVEB;
constexpr int CV_KCOPY = 4160;
constexpr int L_PARK = 0;
constexpr int NPARK = 21;
constexpr int CV_K = 4 * NPARK * 1024;
constexpr int CV_H = CV_K + 4 * CV_KCOPY;
constexpr int CV_HB = 2288 * 2;
constexpr int CV_X = CV_H + 2 * CV_HB;
constexpr int CV_P = CV_X + 8192;
constexpr int L_TR = CV_P;
static_assert(L_KC + 4 * 656 * 4 <= CV_K, "front LDS map");
constexpr int L_RES = 131072;
constexpr int L_SPARE = L_RES + 8192;
constexpr int L_SF = L_SPARE + 16384;
static_assert(CV_P + 32768 + 16 <= L_SF, "front LDS map");
constexpr int L_RS = L_SF + 2048;
constexpr int L_V = L_RS;
constexpr int L_GB = L_RS + 2048;
constexpr int L_SYNC = L_GB + 256;
constexpr int L_EP = L_SYNC + 192;
constexpr int SMEM = L_EP + 16;

#define DIAG_DECL
#define STAMP(i)
#define RSTAMP(i)
#define DIAG_END(wsp)

template <int I, int N, class F>
DEVI void static_for(F&& f) {
  if constexpr (I < N) {
    f(std::integral_constant<int, I>{});
    static_for<I + 1, N>(f);
  }
}

DEVI float bflo(u32 v) { return __uint_as_float(v << 16); }
DEVI float bfhi(u32 v) { return __uint_as_float(v & 0xffff0000u); }
DEVI float b2f(u16 v) { return __uint_as_float(((u32)v) << 16); }
DEVI u32 pack_bf16(float a, float b) {
  f32x2 v = {a, b};
  return __builtin_bit_cast(u32, __builtin_convertvector(v, bf16x2));
}
DEVI float rb(float a) { return bflo(pack_bf16(a, a)); }
DEVI u32 pack_f16(f32x2 v) { return __builtin_bit_cast(u32, __builtin_convertvector(v, f16x2)); }
DEVI u32 perm_lo(u32 a, u32 b) { return __builtin_amdgcn_perm(b, a, 0x05040100u); }
DEVI u32 perm_hi(u32 a, u32 b) { return __builtin_amdgcn_perm(b, a, 0x07060302u); }
DEVI float dot2(u32 a, u32 b, float c) {
  return __builtin_amdgcn_fdot2_f32_bf16(__builtin_bit_cast(bf16x2, a), __builtin_bit_cast(bf16x2, b), c, false);
}
constexpr u32 ONES2 = 0x3f803f80u;

template <int CTRL>
DEVI float dpp(float v) { return __int_as_float(__builtin_amdgcn_update_dpp(0, __float_as_int(v), CTRL, 0xf, 0xf, true)); }
DEVI float row_sum(float v) {
  v += dpp<0xB1>(v); v += dpp<0x4E>(v); v += dpp<0x141>(v); v += dpp<0x140>(v);
  return v;
}
DEVI float rs32(float a, float b) {
  auto r = __builtin_amdgcn_permlane32_swap(__float_as_uint(a), __float_as_uint(b), false, false);
  return __uint_as_float(r[0]) + __uint_as_float(r[1]);
}
DEVI float rs16(float a, float b) {
  auto r = __builtin_amdgcn_permlane16_swap(__float_as_uint(a), __float_as_uint(b), false, false);
  return __uint_as_float(r[0]) + __uint_as_float(r[1]);
}
DEVI float wave_max(float v) {
  v = fmaxf(v, dpp<0xB1>(v)); v = fmaxf(v, dpp<0x4E>(v)); v = fmaxf(v, dpp<0x141>(v)); v = fmaxf(v, dpp<0x140>(v));
  auto r = __builtin_amdgcn_permlane16_swap(__float_as_uint(v), __float_as_uint(v), false, false);
  v = fmaxf(__uint_as_float(r[0]), __uint_as_float(r[1]));
  auto s = __builtin_amdgcn_permlane32_swap(__float_as_uint(v), __float_as_uint(v), false, false);
  return fmaxf(__uint_as_float(s[0]), __uint_as_float(s[1]));
}

DEVI f32x2 cmul(f32x2 a, f32x2 b) {
  const f32x2 t = a.yy * f32x2{-b.y, b.x};
  return __builtin_elementwise_fma(a.xx, b, t);
}
DEVI f32x2 cexpk(float zr, float zi, float k) {
  const float mag = __builtin_amdgcn_exp2f(zr * (k * 1.4426950408889634f));
  const float rev = __builtin_amdgcn_fractf(zi * (k * 0.15915494309189535f));
  return f32x2{mag * __builtin_amdgcn_cosf(rev), mag * __builtin_amdgcn_sinf(rev)};
}

DEVI u32x4 ld16c(rsrc_t r, u32 off) { return __builtin_amdgcn_raw_buffer_load_b128(r, off, 0, 16); }
DEVI u32x2 ld8c(rsrc_t r, u32 off) { return __builtin_amdgcn_raw_buffer_load_b64(r, off, 0, 16); }
DEVI u32 ld4c(rsrc_t r, u32 off) { return __builtin_amdgcn_raw_buffer_load_b32(r, off, 0, 16); }
DEVI void st16c(rsrc_t r, u32 off, u32x4 v) { __builtin_amdgcn_raw_buffer_store_b128(v, r, off, 0, 16); }
DEVI void st8c(rsrc_t r, u32 off, u32x2 v) { __builtin_amdgcn_raw_buffer_store_b64(v, r, off, 0, 16); }
DEVI void st4c(rsrc_t r, u32 off, u32 v) { __builtin_amdgcn_raw_buffer_store_b32(v, r, off, 0, 16); }
#define MEMFENCE() asm volatile("" ::: "memory")
DEVI void drain_vm() {
  __builtin_amdgcn_s_waitcnt(0x0F70);
  asm volatile("s_waitcnt vmcnt(0)" ::: "memory");
}

template <int N>
DEVI float gather_sum(rsrc_t r, u32 off, u32 stride, u32 tag) {
  float v = 0.f;
#pragma unroll 1
  for (int spin = 0; spin < SPIN_CAP; ++spin) {
    u32x2 g[N];
#pragma unroll
    for (int i = 0; i < N; ++i) g[i] = ld8c(r, off + i * stride);
    u32 bad = 0;
    v = 0.f;
#pragma unroll
    for (int i = 0; i < N; ++i) {
      v += __uint_as_float(g[i][0]);
      bad |= g[i][1] ^ tag;
    }
    if (__all(bad == 0)) break;
    __builtin_amdgcn_s_sleep(2);
    MEMFENCE();
  }
  MEMFENCE();
  return v;
}
DEVI float sum_hi4(float v) {
  v += dpp<0x124>(v);
  v += dpp<0x128>(v);
  v = rs16(v, v);
  return rs32(v, v);
}
DEVI u32 silu2(u32 c) {
  const float a = bflo(c), b = bfhi(c);
  return pack_bf16(a * __builtin_amdgcn_rcpf(1.f + __expf(-a)), b * __builtin_amdgcn_rcpf(1.f + __expf(-b)));
}
DEVI float rdlane(float v, int l) { return __int_as_float(__builtin_amdgcn_readlane(__float_as_int(v), l)); }
DEVI void poll_flag(rsrc_t r, u32 off, u32 tag) {
#pragma unroll 1
  for (int spin = 0; spin < SPIN_CAP; ++spin) {
    const u32 f = ld4c(r, off);
    if (__all(f == tag)) break;
    __builtin_amdgcn_s_sleep(1);
    MEMFENCE();
  }
  MEMFENCE();
}

DEVI void lds_set(char* smem, int grp, int word) {
  MEMFENCE();
  *(__attribute__((address_space(3))) volatile u32*)(smem + L_SYNC + 16 * grp + 4 * word) = 1u;
  MEMFENCE();
}
DEVI u32 lds_arrive(char* smem, int grp) {
  u32 old;
  const u32 addr = (u32)(uintptr_t)(lds_char_p)(smem + L_SYNC + 16 * grp);
  asm volatile("s_mov_b64 exec, 1\n\tds_add_rtn_u32 %0, %1, %2\n\ts_mov_b64 exec, -1\n\ts_waitcnt lgkmcnt(0)"
               : "=&v"(old) : "v"(addr), "v"(1u) : "memory");
  return (u32)__builtin_amdgcn_readfirstlane((int)old);
}
template <int MASK>
DEVI void lds_wait(char* smem, int grp) {
  typedef __attribute__((address_space(3))) volatile u32x4* lds_u32x4_p;
#pragma unroll 1
  for (int spin = 0; spin < (1 << 22); ++spin) {
    MEMFENCE();
    const u32x4 v = *(lds_u32x4_p)(smem + L_SYNC + 16 * grp);
    const u32 a = ((MASK & 1) ? v[0] : 1u) & ((MASK & 2) ? v[1] : 1u) & ((MASK & 4) ? v[2] : 1u) & ((MASK & 8) ? v[3] : 1u);
    if (__builtin_amdgcn_readfirstlane((int)a)) break;
    __builtin_amdgcn_s_sleep(1);
  }
  MEMFENCE();
}

template <int MA, int MB>
DEVI void lds_wait2(char* smem, int ga, int gb) {
  typedef __attribute__((address_space(3))) volatile u32x4* lds_u32x4_p;
#pragma unroll 1
  for (int spin = 0; spin < (1 << 22); ++spin) {
    MEMFENCE();
    const u32x4 v = *(lds_u32x4_p)(smem + L_SYNC + 16 * ga), u = *(lds_u32x4_p)(smem + L_SYNC + 16 * gb);
    const u32 a = ((MA & 1) ? v[0] : 1u) & ((MA & 2) ? v[1] : 1u) & ((MA & 4) ? v[2] : 1u) & ((MA & 8) ? v[3] : 1u) &
                  ((MB & 1) ? u[0] : 1u) & ((MB & 2) ? u[1] : 1u) & ((MB & 4) ? u[2] : 1u) & ((MB & 8) ? u[3] : 1u);
    if (__builtin_amdgcn_readfirstlane((int)a)) break;
  }
  MEMFENCE();
}

DEVI void glds16(u32 voff, const void* sbase, u32 ldsaddr) {
  asm volatile("s_mov_b32 m0, %2\n\ts_nop 0\n\tglobal_load_lds_dwordx4 %0, %1 sc1" ::"v"(voff), "s"(sbase), "s"(ldsaddr) : "memory");
}
DEVI void glds16p(u32 voff, const void* sbase, u32 ldsaddr) {
  asm volatile("s_mov_b32 m0, %2\n\ts_nop 0\n\tglobal_load_lds_dwordx4 %0, %1" ::"v"(voff), "s"(sbase), "s"(ldsaddr) : "memory");
}
template <int N>
DEVI void wait_vm() { asm volatile("s_waitcnt vmcnt(%0)" ::"n"(N) : "memory"); }
template <int N>
DEVI void wait_vm_reg(u32x4& r) { asm volatile("s_waitcnt vmcnt(%1)" : "+v"(r) : "n"(N)); }
template <class T>
DEVI void pin(T& r) { asm volatile("" : "+v"(r)); }
template <int OFF>
DEVI void gload16(u32x4& r, u32 voff, const void* sbase) {
  asm volatile("global_load_dwordx4 %0, %1, %2 offset:%3 sc1" : "=v"(r) : "v"(voff), "s"(sbase), "n"(OFF) : "memory");
}
DEVI void gstore16(u32x4 v, u32 voff, const void* sbase) {
  asm volatile("global_store_dwordx4 %0, %1, %2 sc1\n\ts_nop 1" ::"v"(voff), "v"(v), "s"(sbase) : "memory");
}
DEVI void barrier() {
  MEMFENCE();
  __builtin_amdgcn_s_barrier();
  MEMFENCE();
}
DEVI void lds_barrier() {
  asm volatile("s_waitcnt lgkmcnt(0)" ::: "memory");
  __builtin_amdgcn_s_barrier();
  MEMFENCE();
}
DEVI bf16x4 tr_read(const char* p) { return __builtin_amdgcn_ds_read_tr16_b64_v4bf16((lds_bf16x4_p)(p)); }
DEVI bf16x8 tr_frag(const char* p0, const char* p1) {
  return __builtin_shufflevector(tr_read(p0), tr_read(p1), 0, 1, 2, 3, 4, 5, 6, 7);
}
DEVI bf16x8 tr_frag_at(u32 a0, u32 a1) {
  return __builtin_shufflevector(__builtin_amdgcn_ds_read_tr16_b64_v4bf16((lds_bf16x4_p)(uintptr_t)a0),
                                 __builtin_amdgcn_ds_read_tr16_b64_v4bf16((lds_bf16x4_p)(uintptr_t)a1), 0, 1, 2, 3, 4, 5, 6, 7);
}
DEVI bf16x8 as_frag(u32x4 v) { return __builtin_bit_cast(bf16x8, v); }
#define MFMA32(a, b, c) __builtin_amdgcn_mfma_f32_32x32x16_bf16(a, b, c, 0, 0, 0)
#define MFMA16(a, b, c) __builtin_amdgcn_mfma_f32_16x16x32_bf16(a, b, c, 0, 0, 0)

DEVI void transpose8(const u32x4 (&r)[8], u32x4 (&o)[8]) {
#pragma unroll
  for (int k = 0; k < 8; ++k)
#pragma unroll
    for (int i = 0; i < 4; ++i)
      o[k][i] = (k & 1) ? perm_hi(r[2 * i][k >> 1], r[2 * i + 1][k >> 1]) : perm_lo(r[2 * i][k >> 1], r[2 * i + 1][k >> 1]);
}
DEVI u32 ln_pack(float s, float q) {
  const float mean = s * (1.f / DM);
  const float var = fmaxf(q * (1.f / DM) - mean * mean, 0.f);
  return pack_bf16(mean, __builtin_amdgcn_sqrtf(rb(rb(var) + 1e-5f)));
}

template <int NP>
DEVI void mod_part(const u32x4 (&wv)[8], const u32 (&sl)[NP], rsrc_t r, u32 dst, u32 tag, int lane) {
  float acc[8] = {};
#pragma unroll
  for (int j = 0; j < NP; ++j)
#pragma unroll
    for (int k = 0; k < 4; ++k) {
      acc[2 * k] = dot2(perm_lo(wv[2 * j][k], wv[2 * j + 1][k]), sl[j], acc[2 * k]);
      acc[2 * k + 1] = dot2(perm_hi(wv[2 * j][k], wv[2 * j + 1][k]), sl[j], acc[2 * k + 1]);
    }
#pragma unroll
  for (int i = 0; i < 4; ++i) acc[i] = rs32(acc[i], acc[i + 4]);
#pragma unroll
  for (int i = 0; i < 2; ++i) {
    acc[i] = rs16(acc[i], acc[i + 2]);
    acc[i] += dpp<0x128>(acc[i]);
  }
  if ((lane & 8) == 0)
    st16c(r, dst + 8 * (8 * (lane & 7) + 2 * (lane >> 4)), u32x4{__float_as_uint(acc[0]), tag, __float_as_uint(acc[1]), tag});
}

struct Params {
  const u16 *x, *la, *ai, *cr, *ci, *ldt, *dsk, *wg, *wu, *wd, *c, *wm;
  u16* out;
  char* ws;
};

__global__ __launch_bounds__(512, 2) void fused_block(Params P) {
  extern __shared__ __attribute__((aligned(16))) char smem[];
  DIAG_DECL
  const int tid = threadIdx.x, lane = tid & 63;
  const int w = __builtin_amdgcn_readfirstlane(tid >> 6);
  const int b = blockIdx.x, xcd = b & 7, bi = b >> 3;
  char* const ws = P.ws;
  const rsrc_t R = __builtin_amdgcn_make_buffer_rsrc(ws, 0, WS_USED, 0x00020000);
  const int mt = 8 * (xcd & 1) + (bi & 7), fb = 4 * (xcd >> 1) + (bi >> 3);
  const int m2 = 2 * xcd + (bi & 1), nt = bi >> 2, te = (bi >> 1) & 1, tl = 3 - te;
  const int ft = 7 * fb + w;
  const u32 l16 = lane * 16;
  const char* wsrc = ws + OFF_WP + (size_t)ft * 32768;
  u32x4 wf[32];
  u32 code_touch = 0;
  {
    const __attribute__((address_space(1))) char* pc;
    asm volatile("s_getpc_b64 %0" : "=s"(pc));
    if (w >= 6)
      code_touch = *(const __attribute__((address_space(1))) u32*)(pc + min((2 * (bi % 3) + (w & 1)) * 8192 + lane * 128, CODE_BYTES));
  }
  if (tid < 48) *(u32*)(smem + L_SYNC + 4 * tid) = 0;
  STAMP(32);
  u32x4 pl[8];
  u32 ps[8];
  if (w < 4) {
    const int d = 64 * xcd + 2 * bi + (w >> 1), n = 64 * (w & 1) + lane;
    ps[0] = P.la[d * NS + n];
    ps[1] = P.ai[d * NS + n];
    ps[2] = P.cr[d * NS + n];
    ps[3] = P.ci[d * NS + n];
    ps[4] = P.cr[d * NS + (n ^ 64)];
    ps[5] = P.ci[d * NS + (n ^ 64)];
    ps[6] = P.ldt[d];
    ps[7] = P.dsk[(d & ~1) + ((lane >> 2) & 1)];
  } else if (w == 4) {
#pragma unroll
    for (int i = 0; i < 8; ++i) pl[i] = *(const u32x4*)(P.x + (size_t)(8 * b + 2 * (lane & 3) + (i & 1)) * DM + 8 * (lane >> 2) + 128 * (i >> 1));
  } else if (w == 5) {
    const int ra = 32 * (b & 15) + 4 * (lane >> 3);
    const u32x2 cv = *(const u32x2*)(P.c + ra);
    ps[0] = cv[0];
    ps[1] = cv[1];
#pragma unroll
    for (int i = 0; i < 4; ++i) pl[i] = *(const u32x4*)(P.wm + (size_t)(ra + i) * 3072 + 64 * (b >> 4) + 8 * (lane & 7));
  } else {
    const int un = min(b, 223);
    const u16* src = (((un >> 2) & 1) ? P.wu : P.wg) + (size_t)(64 * (2 * (un & 3) + (w & 1)) + 8 * (lane >> 3)) * DF + 64 * (un >> 3) + 8 * (lane & 7);
    __builtin_amdgcn_s_sleep(WDELAY);
#pragma unroll
    for (int i = 0; i < 8; ++i) pl[i] = *(const u32x4*)(src + i * DF);
  }
  u32 tag;
  asm volatile("s_load_dword %0, %1, 0x0\n\ts_waitcnt lgkmcnt(0)" : "=s"(tag) : "s"(ws + OFF_E) : "memory");
  tag += 0x9E3779B9u;
  STAMP(34);
  barrier();
  STAMP(35);
  auto load_w = [&](auto J) {
    constexpr int j = decltype(J)::value;
    gload16<(j & 3) * 1024>(wf[j], l16, wsrc + (j >> 2) * 4096);
  };

  const u32 lds0 = (u32)(uintptr_t)(lds_char_p)smem;
  const char* ag = ws + OFF_ACT + ((size_t)(4 * m2) * NFT + 14 * w) * 1024;
  const char* wdg = (const char*)(P.wd + (size_t)(224 * w) * DM + 64 * nt);
  const u32 wdo = (lane >> 3) * 1024 + 16 * ((lane & 7) ^ ((lane >> 2) & 4));
  auto slot_lds = [&](int k) {
    const int sl = k < 5 ? k : k < 9 ? k : k - 9;
    return sl == 4 ? L_SPARE + w * 2048 : ((sl < 4 ? sl : sl - 1) >> 1) * 32768 + w * 4096 + ((sl < 4 ? sl : sl - 1) & 1) * 2048;
  };
  auto stage_wd = [&](int j) { glds16p(wdo, wdg + j * 8192, lds0 + slot_lds(j >> 1) + (j & 1) * 1024); };
  auto stamp_t = [&](int i) { STAMP(i); };
  const int q4 = (lane & 15) >> 2, p4 = lane & 3, hh = lane >> 5, blk = (lane >> 4) & 1;
  auto wd_frag = [&](int j) {
    const char* p = smem + slot_lds(j >> 1) + (8 * hh + q4) * 128 + 64 * ((j & 1) ^ (q4 >> 1)) + 32 * blk + 8 * p4;
    return tr_frag(p, p + 512);
  };
  bf16x8 wr[10];
  u32x4 ae[14];
  auto load_ae = [&](auto J) {
    constexpr int j = decltype(J)::value;
    gload16<(j & 3) * 1024>(ae[j], l16, ag + (size_t)te * NFT * 1024 + (j >> 2) * 4096);
  };

  auto down_phase = [&](auto) {
    stamp_t(28);
    bf16x8 wd[28];
#pragma unroll
    for (int j = 0; j < 28; ++j) wd[j] = j < 10 ? wr[j] : wd_frag(j);
    auto put_partial = [&](int qd, const f32x16& a) {
#pragma unroll
      for (int g = 0; g < 4; ++g)
        *(f32x4*)(smem + qd * 32768 + w * 4096 + g * 1024 + l16) = f32x4{a[4 * g], a[4 * g + 1], a[4 * g + 2], a[4 * g + 3]};
    };
    u32x4 al[14];
    auto load_al = [&](auto J) {
      constexpr int j = decltype(J)::value;
      gload16<(j & 3) * 1024>(al[j], l16, ag + (size_t)tl * NFT * 1024 + (j >> 2) * 4096);
    };
    f32x16 a0 = {}, a1 = {};
    static_for<0, 14>([&](auto J) {
      constexpr int j = decltype(J)::value;
      wait_vm_reg<13 - j>(ae[j]);
      a0 = MFMA32(wd[2 * j], as_frag(ae[j]), a0);
      a1 = MFMA32(wd[2 * j + 1], as_frag(ae[j]), a1);
    });
    pin(a0);
    pin(a1);
    stamp_t(13);
    lds_wait<1>(smem, 6);
    stamp_t(29);
    static_for<0, 14>(load_al);
    put_partial(0, a0);
    put_partial(2, a1);
    a0 = f32x16{};
    a1 = f32x16{};
    static_for<0, 14>([&](auto J) {
      constexpr int j = decltype(J)::value;
      wait_vm_reg<13 - j>(al[j]);
      a0 = MFMA32(wd[2 * j], as_frag(al[j]), a0);
      a1 = MFMA32(wd[2 * j + 1], as_frag(al[j]), a1);
    });
    stamp_t(14);
    put_partial(1, a0);
    put_partial(3, a1);
    stamp_t(15);
    lds_barrier();
    stamp_t(30);
    const int qi = w >> 2, qj = (w >> 1) & 1, g0 = 2 * (w & 1);
    u32x2 o[2];
#pragma unroll
    for (int gg = 0; gg < 2; ++gg) {
      f32x4 s = {0.f, 0.f, 0.f, 0.f};
#pragma unroll
      for (int v = 0; v < 8; ++v) s += *(const f32x4*)(smem + (w >> 1) * 32768 + v * 4096 + (g0 + gg) * 1024 + l16);
      const int cl = 32 * qi + 8 * (g0 + gg) + 4 * hh;
      const f32x4 gb = *(const f32x4*)(smem + L_GB + 4 * cl);
      const u32x2 xr = __builtin_bit_cast(u32x2, tr_read(smem + L_RES + qj * 4096 + (cl + q4) * 64 + 32 * (blk ^ hh) + 8 * p4));
#pragma unroll
      for (int e = 0; e < 2; ++e) {
        o[gg][e] = pack_bf16(bflo(xr[e]) + gb[2 * e] * s[2 * e], bfhi(xr[e]) + gb[2 * e + 1] * s[2 * e + 1]);
      }
    }
    auto s0 = __builtin_amdgcn_permlane32_swap(o[0][0], o[1][0], false, false);
    auto s1 = __builtin_amdgcn_permlane32_swap(o[0][1], o[1][1], false, false);
    const int tok = 128 * m2 + 32 * (qj ? tl : te) + (lane & 31);
    __builtin_amdgcn_raw_buffer_store_b128(u32x4{s0[0], s1[0], s0[1], s1[1]}, __builtin_amdgcn_make_buffer_rsrc(P.out, 0, SL * DM * 2, 0x00020000), 2 * (tok * DM + 64 * nt + 32 * qi + 8 * g0 + 8 * hh), 0, 2);
  };

  auto x1_arrive = [&]() {
    if (lds_arrive(smem, 4) == 3) {
      if (lane < 8) st4c(R, OFF_FX + 4096 * lane + 16 * b, tag);
      lds_set(smem, 4, 1);
    }
  };

  u32x2 cv;
  auto mod_load = [&](int part) {
    const int ra = 32 * (b & 15) + 4 * (lane >> 3);
    cv = *(const u32x2*)(P.c + ra);
#pragma unroll
    for (int i = 0; i < 4; ++i) pl[i] = *(const u32x4*)(P.wm + (size_t)(ra + i) * 3072 + 1024 * part + 64 * (b >> 4) + 8 * (lane & 7));
  };
  auto mod_job = [&](int part) {
    pin(cv);
    const u32 sl[2] = {silu2(cv[0]), silu2(cv[1])};
#pragma unroll
    for (int i = 0; i < 4; ++i) pin(pl[i]);
    mod_part<2>(pl, sl, R, OFF_GM + (b & 15) * 24576 + 8192 * part + 512 * (b >> 4), tag, lane);
    stamp_t(33);
  };

  if (w < 4) {
    const int ch = w >> 1, half = w & 1;
    const int c0 = 64 * xcd + 2 * bi;
    const int n = 64 * half + lane;
    int kidx[9];
#pragma unroll
    for (int e = 0; e < 5; ++e) {
      const int l0 = 1023 - 8 * n - 2 * e, l1 = l0 - 1;
      const u32 pb = pack_bf16((float)l0, (float)l1);
      kidx[2 * e] = L_KC + 4 * (w * 656 + ((l0 < 128) ? max(l0, 0) : (int)(pb & 0xffffu) - 133 * 128));
      if (e < 4) kidx[2 * e + 1] = L_KC + 4 * (w * 656 + ((l1 < 128) ? l1 : (int)(pb >> 16) - 133 * 128));
    }
#pragma unroll
    for (int e = 0; e < 9; ++e) pin(kidx[e]);
    if (lane < 2) *(u32x4*)(smem + CV_K + w * CV_KCOPY + 4096 + lane * 16) = u32x4{0, 0, 0, 0};
#pragma unroll
    for (int i = 0; i < 8; ++i) pin(ps[i]);
    MEMFENCE();
    if (half == 0) mod_load(1 + ch);
    const float v_cr = bflo(ps[2]), v_ci = bflo(ps[3]), o_cr = bflo(ps[4]), o_ci = bflo(ps[5]);
    const float dt = rb(expf(bflo(ps[6])));
    const float zr = -rb(expf(bflo(ps[0]))) * dt, zi = bflo(ps[1]) * dt;
    STAMP(3);
    const float mx = wave_max(fmaxf(fmaxf(fabsf(v_cr), fabsf(v_ci)), fmaxf(fabsf(o_cr), fabsf(o_ci))));
    const int ex = min(max((int)(__float_as_uint(mx) >> 23), 1), 252);
    const float sdn = __uint_as_float((u32)(254 - ex) << 23);
    const float sup = __uint_as_float((u32)(ex + 1) << 23) * dt;
    {
      char* base = smem + w * KG_WAVEB + lane * 4;
      auto put = [&](int row, f32x2 v) { *(u32*)(base + row * KG_ROWB) = pack_f16(v); };
      const f32x2 e8 = cexpk(zr, zi, 8.f), e16 = cexpk(zr, zi, 16.f);
      const f32x2 e1 = cexpk(zr, -zi, 1.f), e2 = cexpk(zr, -zi, 2.f);
      const f32x2 e128 = cexpk(zr, -zi, 128.f), e256 = cexpk(zr, -zi, 256.f);
      f32x2 f0 = e16, f1 = e8;
      put(41, f32x2{1.f, 0.f});
      put(42, e8);
#pragma unroll
      for (int j = 2; j < 16; j += 2) {
        f1 = cmul(f1, e16);
        put(41 + j, f0);
        put(42 + j, f1);
        if (j < 14) f0 = cmul(f0, e16);
      }
      const f32x2 e4 = cmul(e2, e2);
      f32x2 g[2];
      g[0] = f32x2{v_cr * sdn, -v_ci * sdn};
      g[1] = cmul(g[0], e128);
#pragma unroll
      for (int i = 0; i < 17; ++i) {
        const f32x2 gi = g[i & 1];
        if (i < 2) {
          f32x2 a = gi, c = cmul(gi, e1);
#pragma unroll
          for (int e = 0; e < 8; e += 2) {
            put(8 * i + e, a);
            put(8 * i + e + 1, c);
            if (e < 6) { a = cmul(a, e2); c = cmul(c, e2); }
          }
        } else if (i < 4) {
          const f32x2 c = cmul(gi, e2);
          put(16 + 4 * (i - 2), gi);
          put(17 + 4 * (i - 2), c);
          put(18 + 4 * (i - 2), cmul(gi, e4));
          put(19 + 4 * (i - 2), cmul(c, e4));
        } else if (i < 8) {
          put(24 + 2 * (i - 4), gi);
          put(25 + 2 * (i - 4), cmul(gi, e4));
        } else {
          put(32 + (i - 8), gi);
        }
        if (i + 2 < 17) g[i & 1] = cmul(gi, e256);
      }
    }
    lds_set(smem, 0, w);
    STAMP(4);
    pin(cv);
#pragma unroll
    for (int i = 0; i < 4; ++i) pin(pl[i]);
    if (half == 0) mod_job(1 + ch);
    const int r = lane & 15, q = lane >> 4;
    {
      f32x4 acc[3] = {};
#pragma unroll
      for (int hf = 0; hf < 2; ++hf) {
        if (hf == 0) {
          if (ch) lds_wait<12>(smem, 0);
          else lds_wait<3>(smem, 0);
        }
        const char* rbase = smem + (2 * ch + hf) * KG_WAVEB + r * KG_ROWB + q * 16;
        f16x8 fr[4][4];
#pragma unroll
        for (int ks = 0; ks < 4; ++ks) {
          fr[ks][3] = *(const f16x8*)(rbase + ks * 64 + 41 * KG_ROWB);
#pragma unroll
          for (int t = 0; t < 3; ++t) fr[ks][t] = *(const f16x8*)(rbase + ks * 64 + 16 * t * KG_ROWB);
        }
        __builtin_amdgcn_sched_barrier(0);
#pragma unroll
        for (int ks = 0; ks < 4; ++ks)
#pragma unroll
          for (int t = 0; t < 3; ++t) acc[t] = __builtin_amdgcn_mfma_f32_16x16x32_f16(fr[ks][t], fr[ks][3], acc[t], 0, 0, 0);
      }
      pin(acc[0]); pin(acc[1]); pin(acc[2]);
      STAMP(12);
      float* kc = (float*)(smem + L_KC) + w * 656;
      *(f32x4*)(kc + 128 * (q >> 1) + 4 * (q & 1) + 8 * r) = acc[0] * sup;
      if (q < 2) {
        *(f32x4*)(kc + 256 + 64 * q + 4 * r) = acc[1] * sup;
      } else {
        *(f32x2*)(kc + 384 + 64 * (q - 2) + 2 * r) = f32x2{acc[1][0], acc[1][1]} * sup;
        *(f32x2*)(kc + 416 + 64 * (q - 2) + 2 * r) = f32x2{acc[1][2], acc[1][3]} * sup;
      }
#pragma unroll
      for (int e = 0; e < 4; ++e)
        if (4 * q + e <= 8) kc[512 + 16 * (4 * q + e) + r] = acc[2][e] * sup;
      MEMFENCE();
      float kv[9];
#pragma unroll
      for (int e = 0; e < 9; ++e) kv[e] = *(const float*)(smem + kidx[e]);
      const float ka = kc[639 - n], kb = kc[640 - n];
      if (n == 127) kv[8] = 0.f;
      const float kt = (n & 1) ? ka : kb;
      const u32 aa = pack_bf16(ka, ka), bb = pack_bf16(kb, kb);
      *(u32x4*)(smem + CV_K + (2 * ch) * CV_KCOPY + 16 * n) = u32x4{bb, pack_bf16(kb, kt), aa, aa};
      *(u32x4*)(smem + CV_K + (2 * ch + 1) * CV_KCOPY + 16 * n) = u32x4{bb, pack_bf16(kt, ka), aa, aa};
      u32x4 o, s;
#pragma unroll
      for (int e = 0; e < 4; ++e) {
        o[e] = pack_bf16(kv[2 * e], kv[2 * e + 1]);
        s[e] = pack_bf16(kv[2 * e + 1], kv[2 * e + 2]);
      }
      *(u32x4*)(smem + CV_K + (2 * ch) * CV_KCOPY + 2048 + 16 * n) = o;
      *(u32x4*)(smem + CV_K + (2 * ch + 1) * CV_KCOPY + 2048 + 16 * n) = s;
    }
    lds_set(smem, 8, w);
    STAMP(5);
    lds_wait2<15, 15>(smem, 8, 2);
    STAMP(7);
    const char* abase = smem + CV_K + (2 * ch + ((r & 1) ^ 1)) * CV_KCOPY + 2 * (2030 - (r & ~1) + 8 * q) - 64 * (4 * half + 3 + 56);
    const char* bbase = smem + CV_H + ch * CV_HB + 2 * (224 + 16 * r + 8 * q) - 64 * (4 * half + 3);
    f32x4 acc[8] = {};
    bf16x8 af[2][8], bfr[2][8];
    u32 ab[4];
#pragma unroll
    for (int i = 0; i < 4; ++i) {
      ab[i] = (u32)(uintptr_t)(lds_char_p)abase + 1024 * i;
      pin(ab[i]);
    }
    auto ldfr = [&](int rho, int i) {
      const lds_u32_p p = (lds_u32_p)(uintptr_t)(ab[(7 - i) >> 1] + 64 * (3 - rho) + 512 * ((7 - i) & 1));
      af[rho & 1][i] = as_frag(u32x4{p[0], p[1], p[2], p[3]});
      bfr[rho & 1][i] = *(const bf16x8*)(bbase + 64 * (3 - rho) + 512 * i);
    };
#pragma unroll
    for (int i = 0; i < 4; ++i) ldfr(0, i);
    static_for<0, 32>([&](auto S) {
      constexpr int st = decltype(S)::value, rho = st >> 3, m = st & 7;
      if constexpr (st + 4 < 32) ldfr((st + 4) >> 3, (st + 4) & 7);
#pragma unroll
      for (int o = 0; o <= m; ++o) {
        if (m + o < 8) {
          acc[m + o] = MFMA16(af[rho & 1][m], bfr[rho & 1][o], acc[m + o]);
          if (o != m) acc[m + o] = MFMA16(af[rho & 1][o], bfr[rho & 1][m], acc[m + o]);
        }
      }
#pragma unroll
      for (int i = 0; i < 3; ++i) {
        __builtin_amdgcn_sched_group_barrier(0x008, 1, 0);
        __builtin_amdgcn_sched_group_barrier(0x100, 1, 0);
      }
      __builtin_amdgcn_sched_barrier(0);
    });
    STAMP(8);
#pragma unroll
    for (int t = 0; t < 8; ++t) *(f32x4*)(smem + CV_P + w * 8192 + ch * 16 + 1024 * t + 64 * r + 16 * (q ^ ((r >> 1) & 3))) = acc[t];
    lds_set(smem, 3, w);
    {
      const int c = (lane >> 2) & 1;
      lds_wait2<15, 14>(smem, 3, 10);
      const float ga = *(const float*)(smem + L_EP + 4 * c), cb1 = *(const float*)(smem + L_EP + 8 + 4 * c), dk = bflo(ps[7]);
      STAMP(11);
#pragma unroll
      for (int i = 0; i < 2; ++i) {
        const int e = tid + 256 * i;
        const int l0 = 32 * (e >> 3) + 8 * ((e & 3) ^ (2 * ((c0 >> 2) & 1)));
        const char* pp = smem + CV_P + c * (16384 + 16);
        const int po = (4 * l0) ^ (16 * ((l0 >> 5) & 3));
        const f32x4 y0 = *(const f32x4*)(pp + po) + *(const f32x4*)(pp + 8192 + po);
        const f32x4 y1 = *(const f32x4*)(pp + (po ^ 16)) + *(const f32x4*)(pp + 8192 + (po ^ 16));
        const u32x4 hv = *(const u32x4*)(smem + CV_H + c * CV_HB + 480 + 2 * l0);
        const u32x4 xx = *(const u32x4*)(smem + CV_X + c * 4096 + 2 * l0);
        u32x4 o, os;
#pragma unroll
        for (int k = 0; k < 4; ++k) {
          const u32 yb = k < 2 ? pack_bf16(y0[2 * k], y0[2 * k + 1]) : pack_bf16(y1[2 * k - 4], y1[2 * k - 3]);
          const u32 hd = pack_bf16(bflo(hv[k]) * dk, bfhi(hv[k]) * dk);
          const u32 z = pack_bf16(bflo(yb) + bflo(hd), bfhi(yb) + bfhi(hd));
          const u32 gz = pack_bf16(ga * bflo(z), ga * bfhi(z));
          o[k] = pack_bf16(bflo(xx[k]) + bflo(gz), bfhi(xx[k]) + bfhi(gz));
          os[k] = pack_bf16(bflo(o[k]) * cb1, bfhi(o[k]) * cb1);
        }
        const u32 go = (e >> 3) * 32768 + c0 * 64 + (e & 7) * 16;
        st16c(R, OFF_XS + go, os);
        st16c(R, OFF_X1 + go, o);
      }
      STAMP(16);
      drain_vm();
      x1_arrive();
      STAMP(9);
    }
  } else {
    __builtin_amdgcn_s_setprio(2);
    const int un = min(b, 223), cg = un >> 3, type = (un >> 2) & 1, k8 = 2 * (un & 3) + (w & 1);
    const int kg = lane >> 3, cp = lane & 7;
    if (w < 6) {
      if (w == 4) {
#pragma unroll
        for (int a = 0; a < 4; ++a) {
          pin(pl[2 * a]);
          pin(pl[2 * a + 1]);
#pragma unroll
          for (int k = 0; k < 4; ++k)
            st16c(R, OFF_XG + b * 16384 + (a * 4 + k) * 1024 + l16, u32x4{pl[2 * a][k], tag, pl[2 * a + 1][k], tag});
        }
        STAMP(3);
        float s[2][2] = {}, qq[2][2] = {};
#pragma unroll
        for (int i = 0; i < 8; ++i)
#pragma unroll
          for (int k = 0; k < 4; ++k) {
            s[i & 1][k & 1] = dot2(pl[i][k], ONES2, s[i & 1][k & 1]);
            qq[i & 1][k & 1] = dot2(pl[i][k], pl[i][k], qq[i & 1][k & 1]);
          }
        float v = rs16(rs32(s[0][0] + s[0][1], qq[0][0] + qq[0][1]), rs32(s[1][0] + s[1][1], qq[1][0] + qq[1][1]));
        v += dpp<0x124>(v);
        v += dpp<0x128>(v);
        auto sw = __builtin_amdgcn_permlane32_swap(__float_as_uint(v), __float_as_uint(v), false, false);
        const u32 pk = ln_pack(__uint_as_float(sw[0]), __uint_as_float(sw[1]));
        if (lane < 32 && (lane & 15) < 4) st8c(R, OFF_GST + 64 * b + 16 * (lane & 3) + 8 * (lane >> 4), u32x2{pk, tag});
        STAMP(33);
        drain_vm();
        lds_set(smem, 5, 1);
        RSTAMP(36);
      } else {
        cv = u32x2{ps[0], ps[1]};
        mod_job(0);
      }
      const int t = tid - 256, c0 = 64 * xcd + 2 * bi, pr = c0 >> 1;
      const u32 xo = OFF_XG + (t >> 2) * 16384 + (((pr >> 6) * 4 + (pr & 3)) * 16 + ((pr >> 2) & 15)) * 64 + (t & 3) * 16;
      const u32 mo = OFF_GM + (lane >> 2) * 24576 + 8 * (((lane >> 1) & 1) * DM + c0 + (lane & 1));
      if (lane < 30) *(u32x4*)(smem + CV_H + (w - 4) * CV_HB + lane * 16) = u32x4{0, 0, 0, 0};
      u32x4 gx[8], gs[8];
      u32x2 gm;
      u32 bad = 1;
      lds_wait<2>(smem, 5);
      __builtin_amdgcn_s_sleep(GDELAY);
      STAMP(12);
#pragma unroll 1
      for (int spin = 0; spin < SPIN_CAP; ++spin) {
        if (bad) {
          gm = ld8c(R, mo);
#pragma unroll
          for (int i = 0; i < 8; ++i) {
            gx[i] = ld16c(R, xo + i * (32 * 16384));
            gs[i] = ld16c(R, OFF_GST + 16 * t + 2048 * i);
          }
          bad = gm[1] ^ tag;
#pragma unroll
          for (int i = 0; i < 8; ++i) bad |= (gx[i][1] ^ tag) | (gx[i][3] ^ tag) | (gs[i][1] ^ tag) | (gs[i][3] ^ tag);
        }
        if (__all(bad == 0)) break;
        __builtin_amdgcn_s_sleep(8);
        MEMFENCE();
      }
      drain_vm();
      if (w == 5) lds_set(smem, 10, 0);
      STAMP(6);
      RSTAMP(37);
      const float md = rb(sum_hi4(__uint_as_float(gm[0])));
      const f32x2 sa = {rdlane(md, 0), rdlane(md, 1)}, ca1 = {rb(1.f + rdlane(md, 2)), rb(1.f + rdlane(md, 3))};
#pragma unroll
      for (int i = 0; i < 8; ++i) {
        f32x2 hv[2];
#pragma unroll
        for (int e = 0; e < 2; ++e) {
          const u32 st = gs[i][2 * e], xv = gx[i][2 * e];
          hv[e] = __builtin_elementwise_fma((f32x2{bflo(xv), bfhi(xv)} - bflo(st)) * __builtin_amdgcn_rcpf(bfhi(st)), ca1, sa);
        }
        *(u32*)(smem + CV_H + 480 + 512 * i + 4 * t) = pack_bf16(hv[0].x, hv[1].x);
        *(u32*)(smem + CV_H + CV_HB + 480 + 512 * i + 4 * t) = pack_bf16(hv[0].y, hv[1].y);
      }
      lds_set(smem, 2, w - 4);
      STAMP(7);
#pragma unroll
      for (int i = 0; i < 8; ++i) {
        *(u32*)(smem + CV_X + 512 * i + 4 * t) = perm_lo(gx[i][0], gx[i][2]);
        *(u32*)(smem + CV_X + 4096 + 512 * i + 4 * t) = perm_hi(gx[i][0], gx[i][2]);
      }
      lds_set(smem, 10, w - 2);
    } else {
      u32x4 t[8];
#pragma unroll
      for (int i = 0; i < 8; ++i) pin(pl[i]);
      transpose8(pl, t);
      pin(code_touch);
      STAMP(3);
      {
        char* tb = smem + L_TR + (w & 1) * 8192;
#pragma unroll
        for (int n = 0; n < 8; ++n)
          *(u32x4*)(tb + ((cp >> 1) * 8 + kg) * 256 + (((n & 3) + 8 * (n >> 2) + 4 * (cp & 1)) ^ (cp >> 1)) * 16) = t[n];
        MEMFENCE();
        if (b < 224) {
#pragma unroll
          for (int k = 0; k < 8; ++k) {
            const int row = 4 * k + (lane >> 4), fl = k >> 1;
            const u32x4 o = *(const u32x4*)(tb + row * 256 + ((lane & 15) ^ fl) * 16);
            st16c(R, OFF_WP + (((4 * cg + fl) * 32 + 4 * k8) * 64 + (row & 7) * 32 + 16 * type + (lane & 15)) * 16, o);
          }
        }
      }
      STAMP(4);
      asm volatile("s_waitcnt lgkmcnt(0)" ::: "memory");
      lds_set(smem, 2, w - 4);
      drain_vm();
      if (lane == 0 && b < 224) st4c(R, OFF_FW + (cg * 16 + type * 8 + k8) * 64, tag);
      STAMP(12);
      lds_wait<1>(smem, 10);
      if (w == 6) {
#pragma unroll 1
        for (int spin = 0; spin < SPIN_CAP; ++spin) {
          const u32 f = ld4c(R, OFF_FW + min(((7 * fb) >> 2) * 16 + lane, ((7 * fb + 6) >> 2) * 16 + 15) * 64);
          if (__all(f == tag)) break;
          __builtin_amdgcn_s_sleep(10);
          MEMFENCE();
        }
        drain_vm();
        lds_wait<2>(smem, 10);
        lds_set(smem, 5, 0);
        STAMP(5);
      } else {
        {
          const int c0 = 64 * xcd + 2 * bi;
          float v = gather_sum<2>(R, OFF_GM + (lane >> 3) * 49152 + 8 * ((2 + ((lane >> 1) & 3)) * DM + c0 + (lane & 1)), 24576, tag);
          v += dpp<0x128>(v);
          v = rs16(v, v);
          v = rb(rs32(v, v));
          if ((lane & 6) == 4) v = rb(1.f + v);
          if (lane < 8) {
            if ((lane & 2) == 0) *(float*)(smem + L_EP + 2 * (lane & 4) + 4 * (lane & 1)) = v;
            if (lane >= 2) st8c(R, OFF_FM + ((lane >> 1) == 2 ? 0 : (lane >> 2) + 1) * 4096 + 8 * (c0 + (lane & 1)), u32x2{__float_as_uint(v), tag});
          }
          lds_set(smem, 10, 1);
          STAMP(7);
        }
#pragma unroll
        for (int i = 0; i < 7; ++i) t[i] = *(const u32x4*)(P.wd + (size_t)(7 * b + i) * DM + 8 * lane);
#pragma unroll
        for (int i = 0; i < 7; ++i) pin(t[i]);
        lds_wait<15>(smem, 3);
#pragma unroll 1
        for (int spin = 0; spin < SPIN_CAP; ++spin) {
          u32 bad = 0;
#pragma unroll
          for (int i = 0; i < 8; ++i) t[i] = ld16c(R, OFF_FM + i * 1024 + l16);
#pragma unroll
          for (int i = 0; i < 8; ++i) bad |= (t[i][1] ^ tag) | (t[i][3] ^ tag);
          if (__all(bad == 0)) break;
          __builtin_amdgcn_s_sleep(8);
          MEMFENCE();
        }
#pragma unroll
        for (int i = 0; i < 8; ++i) *(u32*)(smem + L_V + 256 * i + 4 * lane) = perm_hi(t[i][0], t[i][2]);
        lds_set(smem, 5, 2);
        STAMP(5);
      }
    }
    __builtin_amdgcn_s_setprio(0);
    if (w == 7) {
    const char* xsg = ws + OFF_XS + (size_t)(4 * mt) * 32768;
    const char* x1g = ws + OFF_X1 + (size_t)(4 * mt) * 32768;
    auto dma = [&](const char* src, int dst, int n) {
#pragma unroll 4
      for (int i = 0; i < n; ++i) glds16(l16, src + i * 1024, lds0 + dst + i * 1024);
    };
    const u32 fao = OFF_FA + mt * 4096;
    lds_wait<4>(smem, 5);
    lds_wait<15>(smem, 1);
#pragma unroll 1
    for (int spin = 0; spin < SPIN_CAP; ++spin) {
      u32 f[4];
#pragma unroll
      for (int i = 0; i < 4; ++i) f[i] = ld4c(R, OFF_FX + 4096 * xcd + (lane + 64 * i) * 16);
      if (__all(((f[0] ^ tag) | (f[1] ^ tag) | (f[2] ^ tag) | (f[3] ^ tag)) == 0)) break;
      __builtin_amdgcn_s_sleep(1);
      MEMFENCE();
    }
    drain_vm();
    STAMP(10);
    lds_set(smem, 6, 1);
    if (b == 0 && lane == 0) st4c(R, OFF_E, tag);
    dma(x1g + (fb >> 2) * 32768 + (fb & 3) * 8192, L_SPARE, 8);
    barrier();
    STAMP(17);
    dma(xsg + 35840, 35840, 13);
    wait_vm<13>();
    {
      const int g = lane >> 4;
      const char* sb = smem + L_SPARE + (8 * (g >> 1) + 4 * (g & 1) + q4) * 64 + 8 * p4;
      const bf16x8 ones = as_frag(u32x4{ONES2, ONES2, ONES2, ONES2});
      bf16x8 f[2][4];
#pragma unroll
      for (int k2 = 0; k2 < 4; ++k2)
#pragma unroll
        for (int c = 0; c < 2; ++c) {
          const char* p = sb + 32 * (c ^ (g & 1)) + k2 * 2048;
          f[c][k2] = tr_frag(p, p + 1024);
        }
      __builtin_amdgcn_sched_barrier(0);
#pragma unroll
      for (int c = 0; c < 2; ++c) {
        f32x4 aq = {}, as = {};
#pragma unroll
        for (int k2 = 0; k2 < 4; ++k2) {
          aq = MFMA16(f[c][k2], f[c][k2], aq);
          as = MFMA16(ones, f[c][k2], as);
        }
        const float sq = p4 == 0 ? aq[0] : p4 == 1 ? aq[1] : p4 == 2 ? aq[2] : aq[3];
        if (g == q4)
          st16c(R, OFF_GS2 + ((mt * 4 + (fb & 3)) * 128 + 32 * (fb >> 2) + 16 * c + (lane & 15)) * 16,
                u32x4{__float_as_uint(as[0]), tag, __float_as_uint(sq), tag});
      }
    }
    dma(xsg + 49152, 49152, 16);
    lds_barrier();
    dma(xsg + 65536, 65536, 36);
    wait_vm<28>();
    stamp_t(18);
    barrier();
    stamp_t(19);
    {
      u32x4 g[2][4];
#pragma unroll 1
      for (int spin = 0; spin < SPIN_CAP; ++spin) {
        u32 bad = 0;
#pragma unroll
        for (int i = 0; i < 8; ++i) g[i & 1][i >> 1] = ld16c(R, OFF_GS2 + mt * 8192 + i * 1024 + l16);
#pragma unroll
        for (int i = 0; i < 8; ++i) bad |= (g[i & 1][i >> 1][1] ^ tag) | (g[i & 1][i >> 1][3] ^ tag);
        if (__all(bad == 0)) break;
        __builtin_amdgcn_s_sleep(4);
        MEMFENCE();
      }
      drain_vm();
#pragma unroll
      for (int c = 0; c < 2; ++c) {
        const float s1 = (__uint_as_float(g[c][0][0]) + __uint_as_float(g[c][1][0])) + (__uint_as_float(g[c][2][0]) + __uint_as_float(g[c][3][0]));
        const float s2 = (__uint_as_float(g[c][0][2]) + __uint_as_float(g[c][1][2])) + (__uint_as_float(g[c][2][2]) + __uint_as_float(g[c][3][2]));
        const u32 ms = ln_pack(s1, s2) ^ 0x8000u;
        *(u32x4*)(smem + L_SF + (64 * c + lane) * 16) = u32x4{perm_lo(ms, ms), ms, perm_hi(ms, ms), 0};
        const float k0 = -2.f * 0.7978845608028654f * 1.4426950408889634f, k1 = k0 * 0.044715f;
        const float sd = bfhi(ms), rs = __builtin_amdgcn_rcpf(sd);
        *(f32x4*)(smem + L_RS + (64 * c + lane) * 16) = f32x4{k0 * rs, k1 * rs * rs * rs, sd * sd, 0.f};
      }
    }
    lds_set(smem, 7, 0);
    stamp_t(8);
    dma(xsg + 102400, 102400, 28);
    stamp_t(20);
    lds_barrier();
    stamp_t(21);
    dma(ws + OFF_X1 + (size_t)(4 * m2 + te) * 32768 + nt * 4096, L_RES, 4);
    dma(ws + OFF_X1 + (size_t)(4 * m2 + tl) * 32768 + nt * 4096, L_RES + 4096, 4);
#pragma unroll
    for (int j = 0; j < 10; ++j) stage_wd(j);
    *(float*)(smem + L_GB + 4 * lane) = gather_sum<1>(R, OFF_FM + 2 * 4096 + 8 * (64 * nt + lane), 0, tag);
    drain_vm();
#pragma unroll
    for (int j = 0; j < 10; ++j) wr[j] = wd_frag(j);
#pragma unroll
    for (int j = 0; j < 10; ++j) pin(wr[j]);
#pragma unroll
    for (int j = 18; j < 28; ++j) stage_wd(j);
    stamp_t(22);
    lds_barrier();
    stamp_t(23);
    if (lane < 2) st4c(R, fao + lane * 1024 + fb * 64, tag);
#pragma unroll
    for (int j = 10; j < 14; ++j) stage_wd(j);
    __builtin_amdgcn_s_sleep(16);
    poll_flag(R, OFF_FA + m2 * 4096 + te * 1024 + (lane & 15) * 64, tag);
    static_for<0, 14>(load_ae);
    stamp_t(24);
    barrier();
    stamp_t(25);
    if (lane == 0) st4c(R, fao + 2 * 1024 + fb * 64, tag);
#pragma unroll
    for (int j = 14; j < 18; ++j) stage_wd(j);
    barrier();
    barrier();
    if (lane == 0) st4c(R, fao + 3 * 1024 + fb * 64, tag);
    __builtin_amdgcn_s_sleep(20);
    poll_flag(R, OFF_FA + m2 * 4096 + tl * 1024 + (lane & 15) * 64, tag);
    lds_set(smem, 6, 0);
    drain_vm();
    down_phase(std::integral_constant<int, 1>{});
  }
  }

  if (w < 7) {
    lds_wait<1>(smem, 5);
    if (w >= 4) {
      lds_wait<3>(smem, 2);
      static_for<0, NPARK>(load_w);
    } else {
      lds_wait<2>(smem, 4);
    }
    static_for<NPARK, 32>(load_w);
    if (w >= 4) {
      lds_wait<15>(smem, 8);
#pragma unroll 4
      for (int i = 0; i < 4 * NPARK / 3; ++i) {
        const int f = 3 * i + (w - 4), tw = f & 3, k = f >> 2;
        glds16(l16, ws + OFF_WP + (size_t)(7 * fb + tw) * 32768 + k * 1024, lds0 + L_PARK + (tw * NPARK + k) * 1024);
      }
      drain_vm();
      lds_set(smem, 9, w - 4);
      STAMP(9);
      lds_wait<15>(smem, 3);
    } else {
      lds_wait<7>(smem, 9);
#pragma unroll
      for (int j = 0; j < NPARK; ++j) wf[j] = *(const u32x4*)(smem + L_PARK + (w * NPARK + j) * 1024 + l16);
    }
    u32x4 aux;
    {
      lds_wait<4>(smem, 5);
      const char* vb = smem + L_V + (lane & 1) * 1024 + 16 * hh;
      f32x16 d = {};
      bf16x8 va[32];
#pragma unroll
      for (int j = 0; j < 12; ++j) va[j] = *(const bf16x8*)(vb + 32 * j);
      static_for<0, 32>([&](auto J) {
        constexpr int j = decltype(J)::value;
        if constexpr (j + 12 < 32) va[j + 12] = *(const bf16x8*)(vb + 32 * (j + 12));
        if constexpr (j >= NPARK) wait_vm_reg<31 - j>(wf[j]);
        d = MFMA32(va[j], as_frag(wf[j]), d);
        pin(d);
        __builtin_amdgcn_sched_barrier(0);
      });
      asm volatile("s_waitcnt lgkmcnt(0)" ::: "memory");
      if (w < 4) lds_set(smem, 1, w);
      const float c = d[0], sf = d[1];
      const float c0 = rb(c), c1 = rb(c - c0), b0 = rb(sf), b1 = rb(sf - b0);
      aux = u32x4{pack_bf16(c0, c1), pack_bf16(c - c0 - c1, b0), pack_bf16(b1, sf - b0 - b1), 0};
      if (hh) aux = u32x4{0, 0, 0, 0};
    }
    STAMP(10);
    u32 xb[2][2];
#pragma unroll
    for (int i = 0; i < 2; ++i) {
      xb[i][0] = lds0 + 65536 * i + (8 * hh + q4) * 64 + 32 * blk + 8 * p4;
      xb[i][1] = lds0 + 65536 * i + (8 * hh + 4 + q4) * 64 + 32 * (blk ^ 1) + 8 * p4;
      pin(xb[i][0]);
      pin(xb[i][1]);
    }
    auto xfrag = [&](int s) { return tr_frag_at(xb[s >> 6][0] + (s & 63) * 1024, xb[s >> 6][1] + (s & 63) * 1024); };
    lds_wait<2>(smem, 6);
#pragma unroll
    for (int i = 0; i < 5; ++i) glds16(l16, ws + OFF_XS + (size_t)(4 * mt) * 32768 + (7 * i + w) * 1024, lds0 + (7 * i + w) * 1024);
    wait_vm<2>();
    barrier();
    STAMP(17);
    constexpr int XD = 3;
    bf16x8 xf[4];
#pragma unroll
    for (int s = 0; s < XD; ++s) xf[s] = xfrag(s);
    const char* actg = ws + OFF_ACT + ((size_t)(4 * mt) * NFT + ft) * 1024;
    f32x16 acc[3];
    f32x4 gcs[2];
    float ev = 0.f, gu = 0.f, prev = 0.f;
    u32x4 o;
    auto epi_a = [&](int t, int i) {
      const f32x4& gc = gcs[t & 1];
      const float a = acc[t % 3][i];
      ev = __builtin_amdgcn_exp2f(a * __builtin_fmaf(a * a, gc[1], gc[0]));
      gu = a * acc[t % 3][8 + i];
      pin(ev);
      pin(gu);
    };
    auto epi_b = [&](int t, int i) {
      float v = gu * __builtin_amdgcn_rcpf(__builtin_fmaf(ev, gcs[t & 1][2], gcs[t & 1][2]));
      pin(v);
      if (i & 1) o[i >> 1] = pack_bf16(prev, v);
      else prev = v;
      if (i == 7) gstore16(o, l16, actg + (size_t)t * NFT * 1024);
    };
    auto fold = [&](int t) {
      const bf16x8 sf = *(const bf16x8*)(smem + L_SF + t * 512 + (lane & 31) * 16);
      gcs[t & 1] = *(const f32x4*)(smem + L_RS + t * 512 + (lane & 31) * 16);
      acc[t % 3] = MFMA32(as_frag(aux), sf, acc[t % 3]);
    };
    constexpr int ES0 = 56;
    static_for<0, 4>([&](auto T) {
      constexpr int t = decltype(T)::value;
      static_for<0, 32>([&](auto KS) {
        constexpr int ks = decltype(KS)::value, s = 32 * t + ks;
        if constexpr (t == 0 && ks == 21 - XD) {
          wait_vm<0>();
          barrier();
        }
        if constexpr (s == ES0 - 3) lds_wait<1>(smem, 7);
        if constexpr (s + XD < 128) xf[(s + XD) & 3] = xfrag(s + XD);
        if constexpr (ks == 0) acc[t % 3] = MFMA32(as_frag(wf[0]), xf[s & 3], f32x16{});
        else acc[t % 3] = MFMA32(as_frag(wf[ks]), xf[s & 3], acc[t % 3]);
        pin(acc[t % 3]);
        if constexpr (s == ES0 - 3) fold(0);
        if constexpr (s == 64) fold(1);
        if constexpr (s == 96) fold(2);
        if constexpr (s >= ES0 && s < ES0 + 32) {
          if constexpr (((s - ES0) & 1) == 0) epi_a((s - ES0) >> 4, ((s - ES0) >> 1) & 7);
          else epi_b((s - ES0) >> 4, ((s - ES0) >> 1) & 7);
        }
        if constexpr (s >= 99 && s < 115) {
          if constexpr (((s - 99) & 1) == 0) epi_a(2, (s - 99) >> 1);
          else epi_b(2, (s - 99) >> 1);
        }
        if constexpr (t == 1 && ks >= 8 && ks < 20 && (ks & 1) == 0) stage_wd(ks < 16 ? (ks - 8) / 2 : ks / 2);
        if constexpr (t == 2 && ks < 16 && (ks & 3) == 1) stage_wd(4 + ks / 4);
        if constexpr (t == 3 && ks < 8 && (ks & 1) == 0) stage_wd(10 + ks / 2);
        if constexpr (t == 3 && ks >= 8 && ks < 18) wr[ks - 8] = wd_frag(ks - 8);
        if constexpr (t == 3 && ks == 21) {
#pragma unroll
          for (int j = 0; j < 10; ++j) pin(wr[j]);
        }
        if constexpr (t == 3 && ks >= 22) stage_wd(18 + ks - 22);
        __builtin_amdgcn_sched_barrier(0);
      });
      stamp_t(18 + 2 * t);
      if constexpr (t == 2) wait_vm<0>();
      if constexpr (t == 3) wait_vm<10>();
      lds_barrier();
      stamp_t(19 + 2 * t);
    });
    fold(3);
#pragma unroll
    for (int i = 0; i < 8; i += 2) {
      float v[2];
#pragma unroll
      for (int e = 0; e < 2; ++e) {
        const f32x4& gc = gcs[1];
        const float a = acc[0][i + e];
        const float ex = __builtin_amdgcn_exp2f(a * __builtin_fmaf(a * a, gc[1], gc[0]));
        v[e] = a * acc[0][8 + i + e] * __builtin_amdgcn_rcpf(__builtin_fmaf(ex, gc[2], gc[2]));
      }
      o[i >> 1] = pack_bf16(v[0], v[1]);
    }
    gstore16(o, l16, actg + (size_t)3 * NFT * 1024);
    STAMP(26);
    barrier();
#pragma unroll
    for (int j = 14; j < 18; ++j) stage_wd(j);
    static_for<0, 4>(load_ae);
    wait_vm<8>();
    STAMP(27);
    barrier();
    static_for<4, 14>(load_ae);
    wait_vm<14>();
    down_phase(std::integral_constant<int, 0>{});
  }
  DIAG_END(ws);
}

extern "C" void kernel_launch(void* const* d_in, const int* in_sizes, int n_in, void* d_out, int out_size, void* d_ws,
                              size_t ws_size, hipStream_t stream) {
  Params p;
  p.x = (const u16*)d_in[0];
  p.la = (const u16*)d_in[1];
  p.ai = (const u16*)d_in[2];
  p.cr = (const u16*)d_in[3];
  p.ci = (const u16*)d_in[4];
  p.ldt = (const u16*)d_in[5];
  p.dsk = (const u16*)d_in[6];
  p.wg = (const u16*)d_in[8];
  p.wu = (const u16*)d_in[9];
  p.wd = (const u16*)d_in[10];
  p.c = (const u16*)d_in[11];
  p.wm = (const u16*)d_in[12];
  p.out = (u16*)d_out;
  p.ws = (char*)d_ws;
  (void)hipFuncSetAttribute((const void*)fused_block, hipFuncAttributeMaxDynamicSharedMemorySize, SMEM);
  fused_block<<<256, 512, SMEM, stream>>>(p);
}
```

```cpp
#include <hip/hip_runtime.h>
#include <hip/hip_bf16.h>
#include <stdint.h>
#include <type_traits>

typedef unsigned short u16;
typedef unsigned int u32;
typedef unsigned long long u64;
typedef __attribute__((ext_vector_type(8))) __bf16 bf16x8;
typedef __attribute__((ext_vector_type(4))) __bf16 bf16x4;
typedef __attribute__((ext_vector_type(2))) __bf16 bf16x2;
typedef __attribute__((ext_vector_type(2))) _Float16 f16x2;
typedef __attribute__((ext_vector_type(8))) _Float16 f16x8;
typedef __attribute__((ext_vector_type(2))) float f32x2;
typedef __attribute__((ext_vector_type(4))) float f32x4;
typedef __attribute__((ext_vector_type(16))) float f32x16;
typedef __attribute__((ext_vector_type(4))) u32 u32x4;
typedef __attribute__((ext_vector_type(2))) u32 u32x2;
typedef __attribute__((address_space(3))) bf16x4* lds_bf16x4_p;
typedef __attribute__((address_space(3))) char* lds_char_p;
typedef __attribute__((address_space(3))) u32* lds_u32_p;
typedef __amdgpu_buffer_rsrc_t rsrc_t;

#define DEVI __device__ __forceinline__

constexpr int SL = 2048, DM = 512, NS = 128, DF = 1792;
constexpr int NFT = DF / 16;
constexpr int SPIN_CAP = 1 << 15;
#define GDELAY 2
#define ESTART 60
#define SDELAY 12
#define SPRIO 0
#define AWIN 7
#define LO2 7
#define LO3 14
#define WDELAY 6
constexpr int CODE_BYTES = 38 * 1024;

constexpr u32 OFF_E = 0;
constexpr u32 OFF_GST = 4096;
constexpr u32 OFF_FX = 32768;
constexpr u32 OFF_FW = 81920;
constexpr u32 OFF_FA = 131072;
constexpr u32 OFF_FM = 114688;
constexpr u32 OFF_GS2 = 196608;
constexpr u32 OFF_WP = 3u << 20;
constexpr u32 OFF_X1 = OFF_WP + (4u << 20);
constexpr u32 OFF_ACT = OFF_X1 + (2u << 20);
constexpr u32 OFF_XG = OFF_ACT + (8u << 20);
constexpr u32 OFF_XS = OFF_XG + (4u << 20);
constexpr u32 OFF_GM = OFF_XS + (2u << 20);
constexpr u32 WS_USED = OFF_GM + 16 * 24576;

constexpr int KG_ROWB = 272;
constexpr int KG_WAVEB = 57 * KG_ROWB;
constexpr int L_KC = 4 * KG_WAVEB;
constexpr int CV_KCOPY = 4160;
constexpr int L_PARK = 0;
constexpr int NPARK = 21;
constexpr int CV_K = 4 * NPARK * 1024;
constexpr int CV_H = CV_K + 4 * CV_KCOPY;
constexpr int CV_HB = 2288 * 2;
constexpr int CV_X = CV_H + 2 * CV_HB;
constexpr int CV_P = CV_X + 8192;
constexpr int L_TR = CV_P;
static_assert(L_KC + 4 * 656 * 4 <= CV_K, "front LDS map");
constexpr int L_RES = 131072;
constexpr int L_SPARE = L_RES + 8192;
constexpr int L_SF = L_SPARE + 16384;
static_assert(CV_P + 32768 + 16 <= L_SF, "front LDS map");
constexpr int L_RS = L_SF + 2048;
constexpr int L_V = L_RS;
constexpr int L_GB = L_RS + 2048;
constexpr int L_SYNC = L_GB + 256;
constexpr int L_EP = L_SYNC + 256;
constexpr int SMEM = L_EP + 16;

#define DIAG_DECL
#define STAMP(i)
#define RSTAMP(i)
#define DIAG_END(wsp)

template <int I, int N, class F>
DEVI void static_for(F&& f) {
  if constexpr (I < N) {
    f(std::integral_constant<int, I>{});
    static_for<I + 1, N>(f);
  }
}

DEVI float bflo(u32 v) { return __uint_as_float(v << 16); }
DEVI float bfhi(u32 v) { return __uint_as_float(v & 0xffff0000u); }
DEVI f32x2 unpack2(u32 v) { return f32x2{bflo(v), bfhi(v)}; }
DEVI float b2f(u16 v) { return __uint_as_float(((u32)v) << 16); }
DEVI u32 pack_bf16(float a, float b) {
  f32x2 v = {a, b};
  return __builtin_bit_cast(u32, __builtin_convertvector(v, bf16x2));
}
DEVI float rb(float a) { return bflo(pack_bf16(a, a)); }
DEVI u32 pack_f16(f32x2 v) { return __builtin_bit_cast(u32, __builtin_convertvector(v, f16x2)); }
DEVI u32 perm_lo(u32 a, u32 b) { return __builtin_amdgcn_perm(b, a, 0x05040100u); }
DEVI u32 perm_hi(u32 a, u32 b) { return __builtin_amdgcn_perm(b, a, 0x07060302u); }
DEVI float dot2(u32 a, u32 b, float c) {
  return __builtin_amdgcn_fdot2_f32_bf16(__builtin_bit_cast(bf16x2, a), __builtin_bit_cast(bf16x2, b), c, false);
}
constexpr u32 ONES2 = 0x3f803f80u;

template <int CTRL>
DEVI float dpp(float v) { return __int_as_float(__builtin_amdgcn_update_dpp(0, __float_as_int(v), CTRL, 0xf, 0xf, true)); }
DEVI float row_sum(float v) {
  v += dpp<0xB1>(v); v += dpp<0x4E>(v); v += dpp<0x141>(v); v += dpp<0x140>(v);
  return v;
}
DEVI float rs32(float a, float b) {
  auto r = __builtin_amdgcn_permlane32_swap(__float_as_uint(a), __float_as_uint(b), false, false);
  return __uint_as_float(r[0]) + __uint_as_float(r[1]);
}
DEVI float rs16(float a, float b) {
  auto r = __builtin_amdgcn_permlane16_swap(__float_as_uint(a), __float_as_uint(b), false, false);
  return __uint_as_float(r[0]) + __uint_as_float(r[1]);
}
DEVI float wave_max(float v) {
  v = fmaxf(v, dpp<0xB1>(v)); v = fmaxf(v, dpp<0x4E>(v)); v = fmaxf(v, dpp<0x141>(v)); v = fmaxf(v, dpp<0x140>(v));
  auto r = __builtin_amdgcn_permlane16_swap(__float_as_uint(v), __float_as_uint(v), false, false);
  v = fmaxf(__uint_as_float(r[0]), __uint_as_float(r[1]));
  auto s = __builtin_amdgcn_permlane32_swap(__float_as_uint(v), __float_as_uint(v), false, false);
  return fmaxf(__uint_as_float(s[0]), __uint_as_float(s[1]));
}

DEVI f32x2 cmul(f32x2 a, f32x2 b) {
  const f32x2 t = a.yy * f32x2{-b.y, b.x};
  return __builtin_elementwise_fma(a.xx, b, t);
}
DEVI f32x2 cexpk(float zr, float zi, float k) {
  const float mag = __builtin_amdgcn_exp2f(zr * (k * 1.4426950408889634f));
  const float rev = __builtin_amdgcn_fractf(zi * (k * 0.15915494309189535f));
  return f32x2{mag * __builtin_amdgcn_cosf(rev), mag * __builtin_amdgcn_sinf(rev)};
}

DEVI u32x4 ld16c(rsrc_t r, u32 off) { return __builtin_amdgcn_raw_buffer_load_b128(r, off, 0, 16); }
DEVI u32x2 ld8c(rsrc_t r, u32 off) { return __builtin_amdgcn_raw_buffer_load_b64(r, off, 0, 16); }
DEVI u32 ld4c(rsrc_t r, u32 off) { return __builtin_amdgcn_raw_buffer_load_b32(r, off, 0, 16); }
DEVI void st16c(rsrc_t r, u32 off, u32x4 v) { __builtin_amdgcn_raw_buffer_store_b128(v, r, off, 0, 16); }
DEVI void st8c(rsrc_t r, u32 off, u32x2 v) { __builtin_amdgcn_raw_buffer_store_b64(v, r, off, 0, 16); }
DEVI void st4c(rsrc_t r, u32 off, u32 v) { __builtin_amdgcn_raw_buffer_store_b32(v, r, off, 0, 16); }
DEVI u32 only(bool c, u32 off) { return c ? off : 0x80000000u; }
#define MEMFENCE() asm volatile("" ::: "memory")
DEVI void drain_vm() {
  __builtin_amdgcn_s_waitcnt(0x0F70);
  asm volatile("s_waitcnt vmcnt(0)" ::: "memory");
}

template <int N>
DEVI float gather_sum(rsrc_t r, u32 off, u32 stride, u32 tag) {
  float v = 0.f;
#pragma unroll 1
  for (int spin = 0; spin < SPIN_CAP; ++spin) {
    u32x2 g[N];
#pragma unroll
    for (int i = 0; i < N; ++i) g[i] = ld8c(r, off + i * stride);
    u32 bad = 0;
    v = 0.f;
#pragma unroll
    for (int i = 0; i < N; ++i) {
      v += __uint_as_float(g[i][0]);
      bad |= g[i][1] ^ tag;
    }
    if (__all(bad == 0)) break;
    __builtin_amdgcn_s_sleep(2);
    MEMFENCE();
  }
  MEMFENCE();
  return v;
}
DEVI float sum_hi4(float v) {
  v += dpp<0x124>(v);
  v += dpp<0x128>(v);
  v = rs16(v, v);
  return rs32(v, v);
}
DEVI u32 silu2(u32 c) {
  const float a = bflo(c), b = bfhi(c);
  return pack_bf16(a * __builtin_amdgcn_rcpf(1.f + __expf(-a)), b * __builtin_amdgcn_rcpf(1.f + __expf(-b)));
}
DEVI float rdlane(float v, int l) { return __int_as_float(__builtin_amdgcn_readlane(__float_as_int(v), l)); }
DEVI void poll_flag(rsrc_t r, u32 off, u32 tag) {
#pragma unroll 1
  for (int spin = 0; spin < SPIN_CAP; ++spin) {
    const u32 f = ld4c(r, off);
    if (__all(f == tag)) break;
    __builtin_amdgcn_s_sleep(1);
    MEMFENCE();
  }
  MEMFENCE();
}

DEVI void lds_set(char* smem, int grp, int word) {
  MEMFENCE();
  *(__attribute__((address_space(3))) volatile u32*)(smem + L_SYNC + 16 * grp + 4 * word) = 1u;
  MEMFENCE();
}
DEVI u32 lds_peek(char* smem, int grp, int word) {
  return *(__attribute__((address_space(3))) volatile u32*)(smem + L_SYNC + 16 * grp + 4 * word);
}
DEVI u32 lds_arrive(char* smem, int grp) {
  u32 old;
  const u32 addr = (u32)(uintptr_t)(lds_char_p)(smem + L_SYNC + 16 * grp);
  asm volatile("s_mov_b64 exec, 1\n\tds_add_rtn_u32 %0, %1, %2\n\ts_mov_b64 exec, -1\n\ts_waitcnt lgkmcnt(0)"
               : "=&v"(old) : "v"(addr), "v"(1u) : "memory");
  return (u32)__builtin_amdgcn_readfirstlane((int)old);
}
template <int MASK>
DEVI void lds_wait(char* smem, int grp) {
  typedef __attribute__((address_space(3))) volatile u32x4* lds_u32x4_p;
#pragma unroll 1
  for (int spin = 0; spin < (1 << 22); ++spin) {
    MEMFENCE();
    const u32x4 v = *(lds_u32x4_p)(smem + L_SYNC + 16 * grp);
    const u32 a = ((MASK & 1) ? v[0] : 1u) & ((MASK & 2) ? v[1] : 1u) & ((MASK & 4) ? v[2] : 1u) & ((MASK & 8) ? v[3] : 1u);
    if (__builtin_amdgcn_readfirstlane((int)a)) break;
    __builtin_amdgcn_s_sleep(1);
  }
  MEMFENCE();
}

template <int MA, int MB>
DEVI void lds_wait2(char* smem, int ga, int gb) {
  typedef __attribute__((address_space(3))) volatile u32x4* lds_u32x4_p;
#pragma unroll 1
  for (int spin = 0; spin < (1 << 22); ++spin) {
    MEMFENCE();
    const u32x4 v = *(lds_u32x4_p)(smem + L_SYNC + 16 * ga), u = *(lds_u32x4_p)(smem + L_SYNC + 16 * gb);
    const u32 a = ((MA & 1) ? v[0] : 1u) & ((MA & 2) ? v[1] : 1u) & ((MA & 4) ? v[2] : 1u) & ((MA & 8) ? v[3] : 1u) &
                  ((MB & 1) ? u[0] : 1u) & ((MB & 2) ? u[1] : 1u) & ((MB & 4) ? u[2] : 1u) & ((MB & 8) ? u[3] : 1u);
    if (__builtin_amdgcn_readfirstlane((int)a)) break;
  }
  MEMFENCE();
}

DEVI void glds16(u32 voff, const void* sbase, u32 ldsaddr) {
  asm volatile("s_mov_b32 m0, %2\n\ts_nop 0\n\tglobal_load_lds_dwordx4 %0, %1 sc1" ::"v"(voff), "s"(sbase), "s"(ldsaddr) : "memory");
}
DEVI void glds16p(u32 voff, const void* sbase, u32 ldsaddr) {
  asm volatile("s_mov_b32 m0, %2\n\ts_nop 0\n\tglobal_load_lds_dwordx4 %0, %1" ::"v"(voff), "s"(sbase), "s"(ldsaddr) : "memory");
}
template <int N>
DEVI void wait_vm() { asm volatile("s_waitcnt vmcnt(%0)" ::"n"(N) : "memory"); }
template <int N, class T>
DEVI void wait_vm_reg(T& r) { asm volatile("s_waitcnt vmcnt(%1)" : "+v"(r) : "n"(N)); }
template <class T>
DEVI void pin(T& r) { asm volatile("" : "+v"(r)); }
template <int OFF>
DEVI void gload16(u32x4& r, u32 voff, const void* sbase) {
  asm volatile("global_load_dwordx4 %0, %1, %2 offset:%3 sc1" : "=v"(r) : "v"(voff), "s"(sbase), "n"(OFF) : "memory");
}
template <int OFF>
DEVI void gload4(u32& r, u32 voff, const void* sbase) {
  asm volatile("global_load_dword %0, %1, %2 offset:%3 sc1" : "=v"(r) : "v"(voff), "s"(sbase), "n"(OFF) : "memory");
}
DEVI void gstore16(u32x4 v, u32 voff, const void* sbase) {
  asm volatile("global_store_dwordx4 %0, %1, %2 sc1\n\ts_nop 1" ::"v"(voff), "v"(v), "s"(sbase) : "memory");
}
DEVI void barrier() {
  MEMFENCE();
  __builtin_amdgcn_s_barrier();
  MEMFENCE();
}
DEVI void lds_barrier() {
  asm volatile("s_waitcnt lgkmcnt(0)" ::: "memory");
  __builtin_amdgcn_s_barrier();
  MEMFENCE();
}
DEVI bf16x4 tr_read(const char* p) { return __builtin_amdgcn_ds_read_tr16_b64_v4bf16((lds_bf16x4_p)(p)); }
DEVI bf16x8 tr_frag(const char* p0, const char* p1) {
  return __builtin_shufflevector(tr_read(p0), tr_read(p1), 0, 1, 2, 3, 4, 5, 6, 7);
}
DEVI bf16x8 tr_frag_at(u32 a0, u32 a1) {
  return __builtin_shufflevector(__builtin_amdgcn_ds_read_tr16_b64_v4bf16((lds_bf16x4_p)(uintptr_t)a0),
                                 __builtin_amdgcn_ds_read_tr16_b64_v4bf16((lds_bf16x4_p)(uintptr_t)a1), 0, 1, 2, 3, 4, 5, 6, 7);
}
DEVI bf16x8 as_frag(u32x4 v) { return __builtin_bit_cast(bf16x8, v); }
#define MFMA32(a, b, c) __builtin_amdgcn_mfma_f32_32x32x16_bf16(a, b, c, 0, 0, 0)
#define MFMA16(a, b, c) __builtin_amdgcn_mfma_f32_16x16x32_bf16(a, b, c, 0, 0, 0)

DEVI void transpose8(const u32x4 (&r)[8], u32x4 (&o)[8]) {
#pragma unroll
  for (int k = 0; k < 8; ++k)
#pragma unroll
    for (int i = 0; i < 4; ++i)
      o[k][i] = (k & 1) ? perm_hi(r[2 * i][k >> 1], r[2 * i + 1][k >> 1]) : perm_lo(r[2 * i][k >> 1], r[2 * i + 1][k >> 1]);
}
DEVI u32 ln_pack(float s, float q) {
  const float mean = s * (1.f / DM);
  const float var = fmaxf(q * (1.f / DM) - mean * mean, 0.f);
  return pack_bf16(mean, __builtin_amdgcn_sqrtf(rb(rb(var) + 1e-5f)));
}

template <int NP>
DEVI void mod_part(const u32x4 (&wv)[8], const u32 (&sl)[NP], rsrc_t r, u32 dst, u32 tag, int lane) {
  float acc[8] = {};
#pragma unroll
  for (int j = 0; j < NP; ++j)
#pragma unroll
    for (int k = 0; k < 4; ++k) {
      acc[2 * k] = dot2(perm_lo(wv[2 * j][k], wv[2 * j + 1][k]), sl[j], acc[2 * k]);
      acc[2 * k + 1] = dot2(perm_hi(wv[2 * j][k], wv[2 * j + 1][k]), sl[j], acc[2 * k + 1]);
    }
#pragma unroll
  for (int i = 0; i < 4; ++i) acc[i] = rs32(acc[i], acc[i + 4]);
#pragma unroll
  for (int i = 0; i < 2; ++i) {
    acc[i] = rs16(acc[i], acc[i + 2]);
    acc[i] += dpp<0x128>(acc[i]);
  }
  st16c(r, only((lane & 8) == 0, dst + 8 * (8 * (lane & 7) + 2 * (lane >> 4))), u32x4{__float_as_uint(acc[0]), tag, __float_as_uint(acc[1]), tag});
}

constexpr int ES0 = ESTART, ES1 = ES0 + 21 > 66 ? ES0 + 21 : 66, ES2 = ES1 + 21 > 98 ? ES1 + 21 : 98;
static_assert(ES2 + 21 <= 125 && ES0 - 3 < 61, "activation streams");
constexpr int up_xpiece(int s) { return s < 28 && (s & 1) ? 5 + s / 2 : -1; }
constexpr int up_stage(int s) {
  const int t = s >> 5, ks = s & 31;
  if (t == 1 && ks >= 2 && ks < 14 && (ks & 1) == 0) return ks < 10 ? (ks - 2) / 2 : 3 + ks / 2;
  if (t == 2 && ks < 16 && (ks & 3) == 1) return 4 + ks / 4;
  if (t == 3 && ks < 8 && (ks & 1) == 1) return 10 + ks / 2;
  if (t == 3 && ks >= 12 && (ks & 1) == 0) return 18 + (ks - 12) / 2;
  return -1;
}
constexpr bool up_store(int s) { return s == ES0 + 20 || s == ES1 + 20 || s == ES2 + 20; }
constexpr int up_after(int a, int b, int with) {
  int n = with ? (up_xpiece(a) >= 0) + (up_stage(a) >= 0) : 0;
  for (int s = a + 1; s < b; ++s) n += up_store(s) + (up_xpiece(s) >= 0) + (up_stage(s) >= 0);
  return n;
}

static_assert(AWIN >= 7 && AWIN <= 14 && LO2 >= 3 && 13 - AWIN < LO2 && 20 - AWIN < LO3 && LO3 - 3 > LO2, "down schedule");
constexpr int dn_iss(int n) {
  const int lo = n < 14 ? -1 : n < 21 ? LO2 : LO3;
  return n - AWIN > lo ? n - AWIN : lo;
}
constexpr int dn_issued(int j) {
  int n = 0;
  while (n < 28 && dn_iss(n) < j) ++n;
  return n;
}

struct Params {
  const u16 *x, *la, *ai, *cr, *ci, *ldt, *dsk, *wg, *wu, *wd, *c, *wm;
  u16* out;
  char* ws;
};

__global__ __launch_bounds__(512, 2) void fused_block(Params P) {
  extern __shared__ __attribute__((aligned(16))) char smem[];
  DIAG_DECL
  const int tid = threadIdx.x, lane = tid & 63;
  const int w = __builtin_amdgcn_readfirstlane(tid >> 6);
  const int b = blockIdx.x, xcd = b & 7, bi = b >> 3;
  char* const ws = P.ws;
  const rsrc_t R = __builtin_amdgcn_make_buffer_rsrc(ws, 0, WS_USED, 0x00020000);
  const int mt = 4 * (xcd & 3) + (bi & 3), fb = 2 * (bi >> 2) + (xcd >> 2);
  const int par = fb & 1;
  const int m2 = 2 * xcd + (bi & 1), nt = bi >> 2, cp2 = (bi >> 1) & 1;
  const int ft = 7 * fb + w;
  const u32 l16 = lane * 16;
  const char* wsrc = ws + OFF_WP + (size_t)ft * 32768;
  u32x4 wf[32];
  u32 code_touch = 0;
  {
    const __attribute__((address_space(1))) char* pc;
    asm volatile("s_getpc_b64 %0" : "=s"(pc));
    if (w >= 6)
      code_touch = *(const __attribute__((address_space(1))) u32*)(pc + min((2 * (bi % 3) + (w & 1)) * 8192 + lane * 128, CODE_BYTES));
  }
  if (tid < 64) *(u32*)(smem + L_SYNC + 4 * tid) = 0;
  STAMP(32);
  u32x4 pl[8];
  u32 ps[8];
  if (w < 4) {
    const int d = 64 * xcd + 2 * bi + (w >> 1), n = 64 * (w & 1) + lane;
    ps[0] = P.la[d * NS + n];
    ps[1] = P.ai[d * NS + n];
    ps[2] = P.cr[d * NS + n];
    ps[3] = P.ci[d * NS + n];
    ps[4] = P.cr[d * NS + (n ^ 64)];
    ps[5] = P.ci[d * NS + (n ^ 64)];
    ps[6] = P.ldt[d];
    ps[7] = P.dsk[(d & ~1) + ((lane >> 2) & 1)];
  } else if (w == 4) {
#pragma unroll
    for (int i = 0; i < 8; ++i) pl[i] = *(const u32x4*)(P.x + (size_t)(8 * b + 2 * (lane & 3) + (i & 1)) * DM + 8 * (lane >> 2) + 128 * (i >> 1));
  } else if (w == 5) {
    const int ra = 32 * (b & 15) + 4 * (lane >> 3);
    const u32x2 cv = *(const u32x2*)(P.c + ra);
    ps[0] = cv[0];
    ps[1] = cv[1];
#pragma unroll
    for (int i = 0; i < 4; ++i) pl[i] = *(const u32x4*)(P.wm + (size_t)(ra + i) * 3072 + 64 * (b >> 4) + 8 * (lane & 7));
  } else {
    const int un = min(b, 223);
    const u16* src = (((un >> 2) & 1) ? P.wu : P.wg) + (size_t)(64 * (2 * (un & 3) + (w & 1)) + 8 * (lane >> 3)) * DF + 64 * (un >> 3) + 8 * (lane & 7);
    __builtin_amdgcn_s_sleep(WDELAY);
#pragma unroll
    for (int i = 0; i < 8; ++i) pl[i] = *(const u32x4*)(src + i * DF);
  }
  u32 tag;
  asm volatile("s_load_dword %0, %1, 0x0\n\ts_waitcnt lgkmcnt(0)" : "=s"(tag) : "s"(ws + OFF_E) : "memory");
  tag += 0x9E3779B9u;
  STAMP(34);
  barrier();
  STAMP(35);
  auto load_w = [&](auto J) {
    constexpr int j = decltype(J)::value;
    gload16<(j & 3) * 1024>(wf[j], l16, wsrc + (j >> 2) * 4096);
  };

  const u32 lds0 = (u32)(uintptr_t)(lds_char_p)smem;
  const char* ag[2] = {ws + OFF_ACT + ((size_t)(4 * m2 + 2 * cp2) * NFT + 14 * w + 7 * cp2) * 1024,
                       ws + OFF_ACT + ((size_t)(4 * m2 + 2 * cp2) * NFT + 14 * w + 7 * (cp2 ^ 1)) * 1024};
  const char* wdg[2] = {(const char*)(P.wd + (size_t)(224 * w + 112 * cp2) * DM + 64 * nt),
                        (const char*)(P.wd + (size_t)(224 * w + 112 * (cp2 ^ 1)) * DM + 64 * nt)};
  const u32 wdo = (lane >> 3) * 1024 + 16 * ((lane & 7) ^ ((lane >> 2) & 4));
  auto slot_lds = [&](int k) {
    const int sl = k < 9 ? k : k - 9;
    return sl == 4 ? L_SPARE + w * 2048 : ((sl < 4 ? sl : sl - 1) >> 1) * 32768 + w * 4096 + ((sl < 4 ? sl : sl - 1) & 1) * 2048;
  };
  auto stage_wd = [&](int j) { glds16p(wdo, wdg[j / 14] + (j % 14) * 8192, lds0 + slot_lds(j >> 1) + (j & 1) * 1024); };
  auto stamp_t = [&](int i) { STAMP(i); };
  const int q4 = (lane & 15) >> 2, p4 = lane & 3, hh = lane >> 5, blk = (lane >> 4) & 1;
  auto wd_frag = [&](int j) {
    const char* p = smem + slot_lds(j >> 1) + (8 * hh + q4) * 128 + 64 * ((j & 1) ^ (q4 >> 1)) + 32 * blk + 8 * p4;
    return tr_frag(p, p + 512);
  };
  bf16x8 wr[10];
  u32x4 av[28];
  auto load_a = [&](auto J) {
    constexpr int j = decltype(J)::value, g = j / 7, i = j % 7;
    gload16<(i & 3) * 1024>(av[j], l16, ag[g >> 1] + (size_t)(g & 1) * NFT * 1024 + (i >> 2) * 4096);
  };
  const char* fag[2] = {ws + OFF_FA + m2 * 4096 + (2 * w + cp2) * 64, ws + OFF_FA + m2 * 4096 + (2 * w + (cp2 ^ 1)) * 64};
  u32 af[4];
  auto load_flag = [&](auto G) { gload4<decltype(G)::value * 1024>(af[decltype(G)::value], 0u, fag[decltype(G)::value >> 1]); };
  auto check_flag = [&](auto G, auto YOUNGER) {
    constexpr int g = decltype(G)::value;
    wait_vm_reg<decltype(YOUNGER)::value>(af[g]);
    if (__builtin_expect((u32)__builtin_amdgcn_readfirstlane((int)af[g]) != tag, 0))
      poll_flag(R, OFF_FA + m2 * 4096 + g * 1024 + (2 * w + (cp2 ^ (g >> 1))) * 64, tag);
    MEMFENCE();
  };

  auto down_phase = [&](auto) {
    constexpr int FD = 3;
    bf16x8 wd[28];
    auto put_partial = [&](int qd, const f32x16& a) {
#pragma unroll
      for (int g = 0; g < 4; ++g)
        *(f32x4*)(smem + qd * 32768 + w * 4096 + g * 1024 + l16) = f32x4{a[4 * g], a[4 * g + 1], a[4 * g + 2], a[4 * g + 3]};
    };
    const int cl = 8 * w + 4 * hh;
    auto finish = [&](int qj) {
      f32x4 s = {0.f, 0.f, 0.f, 0.f};
#pragma unroll
      for (int v = 0; v < 8; ++v) s += *(const f32x4*)(smem + (2 * (w >> 2) + qj) * 32768 + v * 4096 + (w & 3) * 1024 + l16);
      const f32x4 gb = *(const f32x4*)(smem + L_GB + 4 * cl);
      const u32x2 xr = __builtin_bit_cast(u32x2, tr_read(smem + L_RES + qj * 4096 + (cl + q4) * 64 + 32 * (blk ^ hh) + 8 * p4));
      u32x2 o;
#pragma unroll
      for (int e = 0; e < 2; ++e) o[e] = pack_bf16(bflo(xr[e]) + gb[2 * e] * s[2 * e], bfhi(xr[e]) + gb[2 * e + 1] * s[2 * e + 1]);
      return o;
    };
    f32x16 acc[4] = {};
    barrier();
    st4c(R, only(w == 7 && lane == 0, OFF_FA + mt * 4096 + 2 * 1024 + fb * 64), tag);
    MEMFENCE();
#pragma unroll
    for (int j = 14; j < 18; ++j) stage_wd(j);
    check_flag(std::integral_constant<int, 0>{}, std::integral_constant<int, 6>{});
    static_for<0, 2>(load_a);
    load_flag(std::integral_constant<int, 1>{});
    wait_vm<8>();
    stamp_t(27);
    barrier();
    st4c(R, only(w == 7 && lane == 0, OFF_FA + mt * 4096 + 3 * 1024 + fb * 64), tag);
    MEMFENCE();
    static_for<2, 7>(load_a);
    check_flag(std::integral_constant<int, 1>{}, std::integral_constant<int, 6>{});
    static_for<7, dn_issued(0)>(load_a);
#pragma unroll
    for (int j = 0; j < 14; ++j) wd[j] = j < 10 ? wr[j] : wd_frag(j);
    stamp_t(28);
    u32x2 o0;
    static_for<0, 28>([&](auto J) {
      constexpr int j = decltype(J)::value, g = j / 7, k = 7 * (g >> 1) + j % 7;
      wait_vm_reg<dn_issued(j) - 1 - j>(av[j]);
      acc[g & 1] = MFMA32(wd[2 * k], as_frag(av[j]), acc[g & 1]);
      acc[2 + (g & 1)] = MFMA32(wd[2 * k + 1], as_frag(av[j]), acc[2 + (g & 1)]);
      pin(acc[g & 1]);
      pin(acc[2 + (g & 1)]);
      if constexpr (j == dn_iss(14)) check_flag(std::integral_constant<int, 2>{}, std::integral_constant<int, dn_issued(j) - dn_issued(j - FD)>{});
      if constexpr (j == dn_iss(21)) check_flag(std::integral_constant<int, 3>{}, std::integral_constant<int, dn_issued(j) - dn_issued(j - FD)>{});
      if constexpr (j == dn_iss(21)) stamp_t(29);
      if constexpr (j + FD == dn_iss(14)) load_flag(std::integral_constant<int, 2>{});
      if constexpr (j + FD == dn_iss(21)) load_flag(std::integral_constant<int, 3>{});
      static_for<dn_issued(j), dn_issued(j + 1)>(load_a);
      if constexpr (j >= 7 && j < 14) {
        wd[2 * j] = wd_frag(2 * j);
        wd[2 * j + 1] = wd_frag(2 * j + 1);
      }
      if constexpr (j == 20) {
        put_partial(0, acc[0]);
        put_partial(2, acc[2]);
      }
      if constexpr (j == 22) {
        lds_barrier();
        o0 = finish(0);
        pin(o0);
      }
    });
    stamp_t(14);
    put_partial(1, acc[1]);
    put_partial(3, acc[3]);
    u16* op = P.out + (size_t)(128 * m2 + 64 * cp2 + (lane & 31)) * DM + 64 * nt + cl;
    *(u32x2*)op = o0;
    stamp_t(15);
    lds_barrier();
    stamp_t(30);
    *(u32x2*)(op + 32 * DM) = finish(1);
  };

  auto x1_arrive = [&]() {
    if (lds_arrive(smem, 4) == 3) {
      st4c(R, only(lane < 8, OFF_FX + 4096 * lane + 16 * b), tag);
      lds_set(smem, 4, 1);
    }
  };

  if (w < 4) {
    const int ch = w >> 1, half = w & 1;
    const int c0 = 64 * xcd + 2 * bi;
    const int n = 64 * half + lane;
    int kidx[9];
#pragma unroll
    for (int e = 0; e < 5; ++e) {
      const int l0 = 1023 - 8 * n - 2 * e, l1 = l0 - 1;
      const u32 pb = pack_bf16((float)l0, (float)l1);
      kidx[2 * e] = L_KC + 4 * (w * 656 + ((l0 < 128) ? max(l0, 0) : (int)(pb & 0xffffu) - 133 * 128));
      if (e < 4) kidx[2 * e + 1] = L_KC + 4 * (w * 656 + ((l1 < 128) ? l1 : (int)(pb >> 16) - 133 * 128));
    }
#pragma unroll
    for (int e = 0; e < 9; ++e) pin(kidx[e]);
    if (lane < 2) *(u32x4*)(smem + CV_K + w * CV_KCOPY + 4096 + lane * 16) = u32x4{0, 0, 0, 0};
    __builtin_amdgcn_s_waitcnt(0x0F70);
#pragma unroll
    for (int i = 0; i < 8; ++i) pin(ps[i]);
    const float v_cr = bflo(ps[2]), v_ci = bflo(ps[3]), o_cr = bflo(ps[4]), o_ci = bflo(ps[5]);
    const float dt = rb(expf(bflo(ps[6])));
    const float zr = -rb(expf(bflo(ps[0]))) * dt, zi = bflo(ps[1]) * dt;
    STAMP(3);
    const float mx = wave_max(fmaxf(fmaxf(fabsf(v_cr), fabsf(v_ci)), fmaxf(fabsf(o_cr), fabsf(o_ci))));
    const int ex = min(max((int)(__float_as_uint(mx) >> 23), 1), 252);
    const float sdn = __uint_as_float((u32)(254 - ex) << 23);
    const float sup = __uint_as_float((u32)(ex + 1) << 23) * dt;
    {
      char* base = smem + w * KG_WAVEB + lane * 4;
      auto put = [&](int row, f32x2 v) { *(u32*)(base + row * KG_ROWB) = pack_f16(v); };
      const f32x2 e8 = cexpk(zr, zi, 8.f), e16 = cexpk(zr, zi, 16.f);
      const f32x2 e1 = cexpk(zr, -zi, 1.f), e2 = cexpk(zr, -zi, 2.f);
      const f32x2 e128 = cexpk(zr, -zi, 128.f), e256 = cexpk(zr, -zi, 256.f);
      f32x2 f0 = e16, f1 = e8;
      put(41, f32x2{1.f, 0.f});
      put(42, e8);
#pragma unroll
      for (int j = 2; j < 16; j += 2) {
        f1 = cmul(f1, e16);
        put(41 + j, f0);
        put(42 + j, f1);
        if (j < 14) f0 = cmul(f0, e16);
      }
      const f32x2 e4 = cmul(e2, e2);
      f32x2 g[2];
      g[0] = f32x2{v_cr * sdn, -v_ci * sdn};
      g[1] = cmul(g[0], e128);
#pragma unroll
      for (int i = 0; i < 17; ++i) {
        const f32x2 gi = g[i & 1];
        if (i < 2) {
          f32x2 a = gi, c = cmul(gi, e1);
#pragma unroll
          for (int e = 0; e < 8; e += 2) {
            put(8 * i + e, a);
            put(8 * i + e + 1, c);
            if (e < 6) { a = cmul(a, e2); c = cmul(c, e2); }
          }
        } else if (i < 4) {
          const f32x2 c = cmul(gi, e2);
          put(16 + 4 * (i - 2), gi);
          put(17 + 4 * (i - 2), c);
          put(18 + 4 * (i - 2), cmul(gi, e4));
          put(19 + 4 * (i - 2), cmul(c, e4));
        } else if (i < 8) {
          put(24 + 2 * (i - 4), gi);
          put(25 + 2 * (i - 4), cmul(gi, e4));
        } else {
          put(32 + (i - 8), gi);
        }
        if (i + 2 < 17) g[i & 1] = cmul(gi, e256);
      }
    }
    lds_set(smem, 0, w);
    STAMP(4);

    const int r = lane & 15, q = lane >> 4;
    {
      f32x4 acc[3] = {};
#pragma unroll
      for (int hf = 0; hf < 2; ++hf) {
        if (hf == 0) {
          if (ch) lds_wait<12>(smem, 0);
          else lds_wait<3>(smem, 0);
        }
        const char* rbase = smem + (2 * ch + hf) * KG_WAVEB + r * KG_ROWB + q * 16;
        f16x8 fr[4][4];
#pragma unroll
        for (int ks = 0; ks < 4; ++ks) {
          fr[ks][3] = *(const f16x8*)(rbase + ks * 64 + 41 * KG_ROWB);
#pragma unroll
          for (int t = 0; t < 3; ++t) fr[ks][t] = *(const f16x8*)(rbase + ks * 64 + 16 * t * KG_ROWB);
        }
        __builtin_amdgcn_sched_barrier(0);
#pragma unroll
        for (int ks = 0; ks < 4; ++ks)
#pragma unroll
          for (int t = 0; t < 3; ++t) acc[t] = __builtin_amdgcn_mfma_f32_16x16x32_f16(fr[ks][t], fr[ks][3], acc[t], 0, 0, 0);
      }
      pin(acc[0]); pin(acc[1]); pin(acc[2]);
      STAMP(12);
      float* kc = (float*)(smem + L_KC) + w * 656;
      *(f32x4*)(kc + 128 * (q >> 1) + 4 * (q & 1) + 8 * r) = acc[0] * sup;
      if (q < 2) {
        *(f32x4*)(kc + 256 + 64 * q + 4 * r) = acc[1] * sup;
      } else {
        *(f32x2*)(kc + 384 + 64 * (q - 2) + 2 * r) = f32x2{acc[1][0], acc[1][1]} * sup;
        *(f32x2*)(kc + 416 + 64 * (q - 2) + 2 * r) = f32x2{acc[1][2], acc[1][3]} * sup;
      }
#pragma unroll
      for (int e = 0; e < 4; ++e)
        if (4 * q + e <= 8) kc[512 + 16 * (4 * q + e) + r] = acc[2][e] * sup;
      MEMFENCE();
      float kv[9];
#pragma unroll
      for (int e = 0; e < 9; ++e) kv[e] = *(const float*)(smem + kidx[e]);
      const float ka = kc[639 - n], kb = kc[640 - n];
      if (n == 127) kv[8] = 0.f;
      const float kt = (n & 1) ? ka : kb;
      const u32 aa = pack_bf16(ka, ka), bb = pack_bf16(kb, kb);
      *(u32x4*)(smem + CV_K + (2 * ch) * CV_KCOPY + 16 * n) = u32x4{bb, pack_bf16(kb, kt), aa, aa};
      *(u32x4*)(smem + CV_K + (2 * ch + 1) * CV_KCOPY + 16 * n) = u32x4{bb, pack_bf16(kt, ka), aa, aa};
      u32x4 o, s;
#pragma unroll
      for (int e = 0; e < 4; ++e) {
        o[e] = pack_bf16(kv[2 * e], kv[2 * e + 1]);
        s[e] = pack_bf16(kv[2 * e + 1], kv[2 * e + 2]);
      }
      *(u32x4*)(smem + CV_K + (2 * ch) * CV_KCOPY + 2048 + 16 * n) = o;
      *(u32x4*)(smem + CV_K + (2 * ch + 1) * CV_KCOPY + 2048 + 16 * n) = s;
    }
    lds_set(smem, 8, w);
    STAMP(5);
    lds_wait2<15, 15>(smem, 8, 2);
    STAMP(7);
    const char* abase = smem + CV_K + (2 * ch + ((r & 1) ^ 1)) * CV_KCOPY + 2 * (2030 - (r & ~1) + 8 * q) - 64 * (4 * half + 3 + 56);
    const char* bbase = smem + CV_H + ch * CV_HB + 2 * (224 + 16 * r + 8 * q) - 64 * (4 * half + 3);
    f32x4 acc[8] = {};
    bf16x8 af[2][8], bfr[2][8];
    u32 ab[4];
#pragma unroll
    for (int i = 0; i < 4; ++i) {
      ab[i] = (u32)(uintptr_t)(lds_char_p)abase + 1024 * i;
      pin(ab[i]);
    }
    auto ldfr = [&](int rho, int i) {
      const lds_u32_p p = (lds_u32_p)(uintptr_t)(ab[(7 - i) >> 1] + 64 * (3 - rho) + 512 * ((7 - i) & 1));
      af[rho & 1][i] = as_frag(u32x4{p[0], p[1], p[2], p[3]});
      bfr[rho & 1][i] = *(const bf16x8*)(bbase + 64 * (3 - rho) + 512 * i);
    };
#pragma unroll
    for (int i = 0; i < 4; ++i) ldfr(0, i);
    static_for<0, 32>([&](auto S) {
      constexpr int st = decltype(S)::value, rho = st >> 3, m = st & 7;
      if constexpr (st + 4 < 32) ldfr((st + 4) >> 3, (st + 4) & 7);
#pragma unroll
      for (int o = 0; o <= m; ++o) {
        if (m + o < 8) {
          acc[m + o] = MFMA16(af[rho & 1][m], bfr[rho & 1][o], acc[m + o]);
          if (o != m) acc[m + o] = MFMA16(af[rho & 1][o], bfr[rho & 1][m], acc[m + o]);
        }
      }
#pragma unroll
      for (int i = 0; i < 3; ++i) {
        __builtin_amdgcn_sched_group_barrier(0x008, 1, 0);
        __builtin_amdgcn_sched_group_barrier(0x100, 1, 0);
      }
      __builtin_amdgcn_sched_barrier(0);
    });
    STAMP(8);
#pragma unroll
    for (int t = 0; t < 8; ++t) *(f32x4*)(smem + CV_P + w * 8192 + ch * 16 + 1024 * t + 64 * r + 16 * (q ^ ((r >> 1) & 3))) = acc[t];
    lds_set(smem, 3, w);
    {
      const int c = (lane >> 2) & 1;
      lds_wait2<15, 14>(smem, 3, 10);
      const float ga = *(const float*)(smem + L_EP + 4 * c), cb1 = *(const float*)(smem + L_EP + 8 + 4 * c), dk = bflo(ps[7]);
      STAMP(11);
#pragma unroll
      for (int i = 0; i < 2; ++i) {
        const int e = tid + 256 * i;
        const int l0 = 32 * (e >> 3) + 8 * ((e & 3) ^ (2 * ((c0 >> 2) & 1)));
        const char* pp = smem + CV_P + c * (16384 + 16);
        const int po = (4 * l0) ^ (16 * ((l0 >> 5) & 3));
        const f32x4 y0 = *(const f32x4*)(pp + po) + *(const f32x4*)(pp + 8192 + po);
        const f32x4 y1 = *(const f32x4*)(pp + (po ^ 16)) + *(const f32x4*)(pp + 8192 + (po ^ 16));
        const u32x4 hv = *(const u32x4*)(smem + CV_H + c * CV_HB + 480 + 2 * l0);
        const u32x4 xx = *(const u32x4*)(smem + CV_X + c * 4096 + 2 * l0);
        u32x4 o, os;
#pragma unroll
        for (int k = 0; k < 4; ++k) {
          const u32 yb = k < 2 ? pack_bf16(y0[2 * k], y0[2 * k + 1]) : pack_bf16(y1[2 * k - 4], y1[2 * k - 3]);
          const f32x2 zf = __builtin_elementwise_fma(unpack2(hv[k]), f32x2{dk, dk}, unpack2(yb));
          const f32x2 gz = unpack2(pack_bf16(zf.x, zf.y)) * ga;
          const f32x2 of = unpack2(xx[k]) + unpack2(pack_bf16(gz.x, gz.y));
          o[k] = pack_bf16(of.x, of.y);
          const f32x2 sf = unpack2(o[k]) * cb1;
          os[k] = pack_bf16(sf.x, sf.y);
        }
        const u32 go = (e >> 3) * 32768 + c0 * 64 + (e & 7) * 16;
        st16c(R, OFF_XS + go, os);
        st16c(R, OFF_X1 + go, o);
      }
      STAMP(16);
      drain_vm();
      x1_arrive();
      STAMP(9);
    }
  } else {
    if (w != 5) __builtin_amdgcn_s_setprio(SPRIO);
    __builtin_amdgcn_s_waitcnt(0x0F70);
    const int un = min(b, 223), cg = un >> 3, type = (un >> 2) & 1, k8 = 2 * (un & 3) + (w & 1);
    const int kg = lane >> 3, cp = lane & 7;
    if (w < 6) {
      if (w == 4) {
#pragma unroll
        for (int a = 0; a < 4; ++a) {
          pin(pl[2 * a]);
          pin(pl[2 * a + 1]);
#pragma unroll
          for (int k = 0; k < 4; ++k)
            st16c(R, OFF_XG + b * 16384 + (a * 4 + k) * 1024 + l16, u32x4{pl[2 * a][k], tag, pl[2 * a + 1][k], tag});
        }
        STAMP(3);
        float s[2][2] = {}, qq[2][2] = {};
#pragma unroll
        for (int i = 0; i < 8; ++i)
#pragma unroll
          for (int k = 0; k < 4; ++k) {
            s[i & 1][k & 1] = dot2(pl[i][k], ONES2, s[i & 1][k & 1]);
            qq[i & 1][k & 1] = dot2(pl[i][k], pl[i][k], qq[i & 1][k & 1]);
          }
        float v = rs16(rs32(s[0][0] + s[0][1], qq[0][0] + qq[0][1]), rs32(s[1][0] + s[1][1], qq[1][0] + qq[1][1]));
        v += dpp<0x124>(v);
        v += dpp<0x128>(v);
        auto sw = __builtin_amdgcn_permlane32_swap(__float_as_uint(v), __float_as_uint(v), false, false);
        const u32 pk = ln_pack(__uint_as_float(sw[0]), __uint_as_float(sw[1]));
        st8c(R, only(lane < 32 && (lane & 15) < 4, OFF_GST + 64 * b + 16 * (lane & 3) + 8 * (lane >> 4)), u32x2{pk, tag});
        STAMP(33);
        drain_vm();
        lds_set(smem, 5, 1);
        RSTAMP(36);
      } else {
        pin(ps[0]);
        pin(ps[1]);
        const u32 sl[2] = {silu2(ps[0]), silu2(ps[1])};
#pragma unroll
        for (int i = 0; i < 4; ++i) pin(pl[i]);
        u32x4 pb[2][8];
#pragma unroll
        for (int i = 0; i < 8; ++i)
          pb[i >> 2][i & 3] = *(const u32x4*)(P.wm + (size_t)(32 * (b & 15) + 4 * (lane >> 3) + (i & 3)) * 3072 + 1024 * (1 + (i >> 2)) + 64 * (b >> 4) + 8 * (lane & 7));
        MEMFENCE();
        const u32 dst = OFF_GM + (b & 15) * 24576 + 512 * (b >> 4);
        mod_part<2>(pl, sl, R, dst, tag, lane);
        STAMP(33);
        mod_part<2>(pb[0], sl, R, dst + 8192, tag, lane);
        mod_part<2>(pb[1], sl, R, dst + 16384, tag, lane);
        STAMP(4);
      }
      const int t = tid - 256, c0 = 64 * xcd + 2 * bi, pr = c0 >> 1;
      const u32 xo = OFF_XG + (t >> 2) * 16384 + (((pr >> 6) * 4 + (pr & 3)) * 16 + ((pr >> 2) & 15)) * 64 + (t & 3) * 16;
      const u32 mo = OFF_GM + (lane >> 2) * 24576 + 8 * (((lane >> 1) & 1) * DM + c0 + (lane & 1));
      if (lane < 30) *(u32x4*)(smem + CV_H + (w - 4) * CV_HB + lane * 16) = u32x4{0, 0, 0, 0};
      u32x4 gx[8], gs[8];
      u32x2 gm;
      u32 bad = 1;
      lds_wait<2>(smem, 5);
      __builtin_amdgcn_s_sleep(GDELAY);
      STAMP(12);
#pragma unroll 1
      for (int spin = 0; spin < SPIN_CAP; ++spin) {
        if (bad) {
          gm = ld8c(R, mo);
#pragma unroll
          for (int i = 0; i < 8; ++i) {
            gx[i] = ld16c(R, xo + i * (32 * 16384));
            gs[i] = ld16c(R, OFF_GST + 16 * t + 2048 * i);
          }
          bad = gm[1] ^ tag;
#pragma unroll
          for (int i = 0; i < 8; ++i) bad |= (gx[i][1] ^ tag) | (gx[i][3] ^ tag) | (gs[i][1] ^ tag) | (gs[i][3] ^ tag);
        }
        if (__all(bad == 0)) break;
        __builtin_amdgcn_s_sleep(8);
        MEMFENCE();
      }
      drain_vm();
      if (w == 5) lds_set(smem, 10, 0);
      STAMP(6);
      RSTAMP(37);
      const float md = rb(sum_hi4(__uint_as_float(gm[0])));
      const f32x2 sa = {rdlane(md, 0), rdlane(md, 1)}, ca1 = {rb(1.f + rdlane(md, 2)), rb(1.f + rdlane(md, 3))};
#pragma unroll
      for (int i = 0; i < 8; ++i) {
        f32x2 hv[2];
#pragma unroll
        for (int e = 0; e < 2; ++e) {
          const u32 st = gs[i][2 * e], xv = gx[i][2 * e];
          hv[e] = __builtin_elementwise_fma((f32x2{bflo(xv), bfhi(xv)} - bflo(st)) * __builtin_amdgcn_rcpf(bfhi(st)), ca1, sa);
        }
        *(u32*)(smem + CV_H + 480 + 512 * i + 4 * t) = pack_bf16(hv[0].x, hv[1].x);
        *(u32*)(smem + CV_H + CV_HB + 480 + 512 * i + 4 * t) = pack_bf16(hv[0].y, hv[1].y);
      }
      lds_set(smem, 2, w - 4);
      STAMP(7);
#pragma unroll
      for (int i = 0; i < 8; ++i) {
        *(u32*)(smem + CV_X + 512 * i + 4 * t) = perm_lo(gx[i][0], gx[i][2]);
        *(u32*)(smem + CV_X + 4096 + 512 * i + 4 * t) = perm_hi(gx[i][0], gx[i][2]);
      }
      lds_set(smem, 10, w - 2);
    } else {
      u32x4 t[8];
#pragma unroll
      for (int i = 0; i < 8; ++i) pin(pl[i]);
      transpose8(pl, t);
      pin(code_touch);
      STAMP(3);
      {
        char* tb = smem + L_TR + (w & 1) * 8192;
#pragma unroll
        for (int n = 0; n < 8; ++n)
          *(u32x4*)(tb + ((cp >> 1) * 8 + kg) * 256 + (((n & 3) + 8 * (n >> 2) + 4 * (cp & 1)) ^ (cp >> 1)) * 16) = t[n];
        MEMFENCE();
        if (b < 224) {
#pragma unroll
          for (int k = 0; k < 8; ++k) {
            const int row = 4 * k + (lane >> 4), fl = k >> 1;
            const u32x4 o = *(const u32x4*)(tb + row * 256 + ((lane & 15) ^ fl) * 16);
            st16c(R, OFF_WP + (((4 * cg + fl) * 32 + 4 * k8) * 64 + (row & 7) * 32 + 16 * type + (lane & 15)) * 16, o);
          }
        }
      }
      STAMP(4);
      asm volatile("s_waitcnt lgkmcnt(0)" ::: "memory");
      lds_set(smem, 2, w - 4);
      drain_vm();
      st4c(R, only(lane == 0 && b < 224, OFF_FW + (cg * 16 + type * 8 + k8) * 64), tag);
      STAMP(12);
      lds_wait<1>(smem, 10);
      if (w == 6) {
#pragma unroll 1
        for (int spin = 0; spin < SPIN_CAP; ++spin) {
          const u32 f = ld4c(R, OFF_FW + min(((7 * fb) >> 2) * 16 + lane, ((7 * fb + 6) >> 2) * 16 + 15) * 64);
          if (__all(f == tag)) break;
          __builtin_amdgcn_s_sleep(10);
          MEMFENCE();
        }
        drain_vm();
        lds_set(smem, 5, 0);
        STAMP(5);
      } else {
        {
          const int c0 = 64 * xcd + 2 * bi;
          float v = gather_sum<2>(R, OFF_GM + (lane >> 3) * 49152 + 8 * ((2 + ((lane >> 1) & 3)) * DM + c0 + (lane & 1)), 24576, tag);
          v += dpp<0x128>(v);
          v = rs16(v, v);
          v = rb(rs32(v, v));
          if ((lane & 6) == 4) v = rb(1.f + v);
          if (lane < 8 && (lane & 2) == 0) *(float*)(smem + L_EP + 2 * (lane & 4) + 4 * (lane & 1)) = v;
          st8c(R, only(lane >= 2 && lane < 8, OFF_FM + ((lane >> 1) == 2 ? 0 : (lane >> 2) + 1) * 4096 + 8 * (c0 + (lane & 1))),
               u32x2{__float_as_uint(v), tag});
          lds_set(smem, 10, 1);
          STAMP(7);
        }
#pragma unroll
        for (int i = 0; i < 7; ++i) t[i] = *(const u32x4*)(P.wd + (size_t)(7 * b + i) * DM + 8 * lane);
#pragma unroll
        for (int i = 0; i < 7; ++i) pin(t[i]);
        lds_wait<15>(smem, 3);
#pragma unroll 1
        for (int spin = 0; spin < SPIN_CAP; ++spin) {
          u32 bad = 0;
#pragma unroll
          for (int i = 0; i < 8; ++i) t[i] = ld16c(R, OFF_FM + i * 1024 + l16);
#pragma unroll
          for (int i = 0; i < 8; ++i) bad |= (t[i][1] ^ tag) | (t[i][3] ^ tag);
          if (__all(bad == 0)) break;
          __builtin_amdgcn_s_sleep(8);
          MEMFENCE();
        }
#pragma unroll
        for (int i = 0; i < 8; ++i) *(u32*)(smem + L_V + 256 * i + 4 * lane) = perm_hi(t[i][0], t[i][2]);
        lds_set(smem, 5, 2);
        STAMP(5);
      }
    }
    __builtin_amdgcn_s_setprio(0);
    if (w == 7) {
    const char* x1g = ws + OFF_X1 + (size_t)(4 * mt) * 32768;
    auto dma = [&](const char* src, int dst, int n) {
#pragma unroll 4
      for (int i = 0; i < n; ++i) glds16(l16, src + i * 1024, lds0 + dst + i * 1024);
    };
    const u32 fao = OFF_FA + mt * 4096;
    __builtin_amdgcn_s_setprio(3);
    lds_wait<4>(smem, 5);
    lds_wait<15>(smem, 1);
#pragma unroll 1
    for (int spin = 0; spin < SPIN_CAP; ++spin) {
      u32 f[4];
#pragma unroll
      for (int i = 0; i < 4; ++i) f[i] = ld4c(R, OFF_FX + 4096 * xcd + (lane + 64 * i) * 16);
      if (__all(((f[0] ^ tag) | (f[1] ^ tag) | (f[2] ^ tag) | (f[3] ^ tag)) == 0)) break;
      __builtin_amdgcn_s_sleep(1);
      MEMFENCE();
    }
    drain_vm();
    STAMP(10);
    lds_set(smem, 6, 1);
    __builtin_amdgcn_s_sleep(3);
    dma(x1g + (fb >> 2) * 32768 + (fb & 3) * 8192, L_SPARE, 8);
    barrier();
    STAMP(17);
    wait_vm<0>();
    {
      const int g = lane >> 4;
      const char* sb = smem + L_SPARE + (8 * (g >> 1) + 4 * (g & 1) + q4) * 64 + 8 * p4;
      const bf16x8 ones = as_frag(u32x4{ONES2, ONES2, ONES2, ONES2});
      bf16x8 f[2][4];
#pragma unroll
      for (int k2 = 0; k2 < 4; ++k2)
#pragma unroll
        for (int c = 0; c < 2; ++c) {
          const char* p = sb + 32 * (c ^ (g & 1)) + k2 * 2048;
          f[c][k2] = tr_frag(p, p + 1024);
        }
      __builtin_amdgcn_sched_barrier(0);
#pragma unroll
      for (int c = 0; c < 2; ++c) {
        f32x4 aq = {}, as = {};
#pragma unroll
        for (int k2 = 0; k2 < 4; ++k2) {
          aq = MFMA16(f[c][k2], f[c][k2], aq);
          as = MFMA16(ones, f[c][k2], as);
        }
        const float sq = p4 == 0 ? aq[0] : p4 == 1 ? aq[1] : p4 == 2 ? aq[2] : aq[3];
        st16c(R, only(g == q4, OFF_GS2 + ((mt * 4 + (fb & 3)) * 128 + 32 * (fb >> 2) + 16 * c + (lane & 15)) * 16),
                u32x4{__float_as_uint(as[0]), tag, __float_as_uint(sq), tag});
      }
    }
    stamp_t(16);
    st4c(R, only(b == 0 && lane == 0, OFF_E), tag);
    lds_barrier();
    stamp_t(18);
    barrier();
    stamp_t(19);
#pragma unroll 1
    for (int spin = 0; spin < SPIN_CAP; ++spin) {
      u32x4 g[2][4];
      u32 bad = 0;
#pragma unroll
      for (int i = 0; i < 8; ++i) g[i & 1][i >> 1] = ld16c(R, OFF_GS2 + mt * 8192 + i * 1024 + l16);
#pragma unroll
      for (int i = 0; i < 8; ++i) bad |= (g[i & 1][i >> 1][1] ^ tag) | (g[i & 1][i >> 1][3] ^ tag);
      const bool ok = __all(bad == 0) || spin == SPIN_CAP - 1;
      if (ok) {
#pragma unroll
        for (int c = 0; c < 2; ++c) {
          const float s1 = (__uint_as_float(g[c][0][0]) + __uint_as_float(g[c][1][0])) + (__uint_as_float(g[c][2][0]) + __uint_as_float(g[c][3][0]));
          const float s2 = (__uint_as_float(g[c][0][2]) + __uint_as_float(g[c][1][2])) + (__uint_as_float(g[c][2][2]) + __uint_as_float(g[c][3][2]));
          const u32 ms = ln_pack(s1, s2) ^ 0x8000u;
          *(u32x4*)(smem + L_SF + (64 * (c ^ par) + lane) * 16) = u32x4{perm_lo(ms, ms), ms, perm_hi(ms, ms), 0};
          const float k0 = -2.f * 0.7978845608028654f * 1.4426950408889634f, k1 = k0 * 0.044715f;
          const float sd = bfhi(ms), rs = __builtin_amdgcn_rcpf(sd);
          *(f32x4*)(smem + L_RS + (64 * (c ^ par) + lane) * 16) = f32x4{k0 * rs, k1 * rs * rs * rs, sd * sd, 0.f};
        }
        lds_set(smem, 7, 0);
        stamp_t(8);
      }
      if (ok) break;
      __builtin_amdgcn_s_sleep(4);
      MEMFENCE();
    }
    stamp_t(20);
    lds_barrier();
    stamp_t(21);
    dma(ws + OFF_X1 + (size_t)(4 * m2 + 2 * cp2) * 32768 + nt * 4096, L_RES, 4);
    dma(ws + OFF_X1 + (size_t)(4 * m2 + 2 * cp2 + 1) * 32768 + nt * 4096, L_RES + 4096, 4);
#pragma unroll
    for (int j = 0; j < 10; ++j) stage_wd(j);
    *(float*)(smem + L_GB + 4 * lane) = gather_sum<1>(R, OFF_FM + 2 * 4096 + 8 * (64 * nt + lane), 0, tag);
    drain_vm();
#pragma unroll
    for (int j = 0; j < 10; ++j) wr[j] = wd_frag(j);
#pragma unroll
    for (int j = 0; j < 10; ++j) pin(wr[j]);
#pragma unroll
    for (int j = 18; j < 28; ++j) stage_wd(j);
    stamp_t(22);
    lds_barrier();
    stamp_t(23);
    st4c(R, only(lane == 0, fao + fb * 64), tag);
#pragma unroll
    for (int j = 10; j < 14; ++j) stage_wd(j);
    lds_wait2<15, 7>(smem, 11, 12);
    st4c(R, only(lane == 0, fao + 1024 + fb * 64), tag);
    stamp_t(24);
    barrier();
    stamp_t(25);
    load_flag(std::integral_constant<int, 0>{});
    wait_vm<0>();
    __builtin_amdgcn_s_setprio(0);
    down_phase(std::integral_constant<int, 1>{});
  }
  }

  if (w < 7) {
    lds_wait<1>(smem, 5);
    if (w >= 4) {
      lds_wait<3>(smem, 2);
      static_for<0, NPARK>(load_w);
    } else {
      lds_wait<2>(smem, 4);
    }
    static_for<NPARK, 32>(load_w);
    if (w >= 4) {
      lds_wait<15>(smem, 8);
#pragma unroll 4
      for (int i = 0; i < 4 * NPARK / 3; ++i) {
        const int f = 3 * i + (w - 4), tw = f & 3, k = f >> 2;
        glds16(l16, ws + OFF_WP + (size_t)(7 * fb + tw) * 32768 + k * 1024, lds0 + L_PARK + (tw * NPARK + k) * 1024);
      }
      drain_vm();
      lds_set(smem, 9, w - 4);
      STAMP(9);
      lds_wait<15>(smem, 3);
    } else {
      lds_wait<7>(smem, 9);
#pragma unroll
      for (int j = 0; j < NPARK; ++j) wf[j] = *(const u32x4*)(smem + L_PARK + (w * NPARK + j) * 1024 + l16);
    }
    auto load_x = [&](int i) {
      const int pc = min(7 * i + w, 127);
      glds16(l16, ws + OFF_XS + (size_t)(4 * mt) * 32768 + ((pc ^ (64 * par)) << 10), lds0 + pc * 1024);
    };
    auto first_x = [&]() {
      lds_wait<2>(smem, 6);
#pragma unroll
      for (int i = 0; i < 5; ++i) load_x(i);
    };
    asm volatile("s_waitcnt vmcnt(0) lgkmcnt(0)" ::: "memory");
#pragma unroll
    for (int j = 0; j < 32; ++j) pin(wf[j]);
    STAMP(10);
    if (w < 4) lds_set(smem, 1, w);
    u32x4 aux;
    {
      lds_wait<4>(smem, 5);
      const char* vb = smem + L_V + (lane & 1) * 1024 + 16 * hh;
      f32x16 d = {};
      bf16x8 va[32];
#pragma unroll
      for (int j = 0; j < 12; ++j) va[j] = *(const bf16x8*)(vb + 32 * j);
      static_for<0, 32>([&](auto J) {
        constexpr int j = decltype(J)::value;
        if constexpr (j + 12 < 32) va[j + 12] = *(const bf16x8*)(vb + 32 * (j + 12));
        d = MFMA32(va[j], as_frag(wf[j]), d);
        pin(d);
        __builtin_amdgcn_sched_barrier(0);
      });
      const float c = d[0], sf = d[1];
      const float c0 = rb(c), c1 = rb(c - c0), b0 = rb(sf), b1 = rb(sf - b0);
      aux = u32x4{pack_bf16(c0, c1), pack_bf16(c - c0 - c1, b0), pack_bf16(b1, sf - b0 - b1), 0};
      if (hh) aux = u32x4{0, 0, 0, 0};
      pin(aux);
    }
    u32 xb[2][2];
#pragma unroll
    for (int i = 0; i < 2; ++i) {
      xb[i][0] = lds0 + 65536 * i + (8 * hh + q4) * 64 + 32 * blk + 8 * p4;
      xb[i][1] = lds0 + 65536 * i + (8 * hh + 4 + q4) * 64 + 32 * (blk ^ 1) + 8 * p4;
      pin(xb[i][0]);
      pin(xb[i][1]);
    }
    auto xfrag = [&](int s) { return tr_frag_at(xb[s >> 6][0] + (s & 63) * 1024, xb[s >> 6][1] + (s & 63) * 1024); };
    const char* actg[2] = {ws + OFF_ACT + ((size_t)(4 * mt + 2 * par) * NFT + ft) * 1024,
                           ws + OFF_ACT + ((size_t)(4 * mt + 2 * (par ^ 1)) * NFT + ft) * 1024};
    asm volatile("" : "+s"(actg[0]), "+s"(actg[1]));
    u32 sfo = lds0 + L_SF + (lane & 31) * 16;
    pin(sfo);
    STAMP(6);
    first_x();
    wait_vm<3>();
    barrier();
    STAMP(17);
    constexpr int XD = 3;
    bf16x8 xf[4];
#pragma unroll
    for (int s = 0; s < XD; ++s) xf[s] = xfrag(s);
    f32x16 acc[3];
    f32x4 gcs[2];
    float ea[4], em[4], prev = 0.f;
    u32x4 o;
    auto epi = [&](auto T, auto U) {
      constexpr int t = decltype(T)::value, u = decltype(U)::value, n = u >> 1;
      const f32x4& gc = gcs[t & 1];
      const f32x16& a = acc[t % 3];
      if constexpr ((u & 1) == 0) {
        if constexpr (n >= 3 && n < 11) {
          float v = em[(n - 3) & 3] * ea[(n - 3) & 3];
          pin(v);
          if constexpr ((n - 3) & 1) o[(n - 3) >> 1] = pack_bf16(prev, v);
          else prev = v;
          if constexpr (n == 10) gstore16(o, l16, actg[t >> 1] + (size_t)(t & 1) * NFT * 1024);
        }
        if constexpr (n >= 2 && n < 10) { ea[(n - 2) & 3] = __builtin_fmaf(ea[(n - 2) & 3], gc[2], gc[2]); pin(ea[(n - 2) & 3]); }
        if constexpr (n >= 1 && n < 9) { ea[(n - 1) & 3] *= a[n - 1]; pin(ea[(n - 1) & 3]); }
        if constexpr (n < 8) {
          ea[n & 3] = a[n] * a[n];
          em[n & 3] = a[n] * a[8 + n];
          pin(ea[n & 3]);
          pin(em[n & 3]);
        }
      } else {
        if constexpr (n >= 2 && n < 10) { ea[(n - 2) & 3] = __builtin_amdgcn_rcpf(ea[(n - 2) & 3]); pin(ea[(n - 2) & 3]); }
        if constexpr (n >= 1 && n < 9) { ea[(n - 1) & 3] = __builtin_amdgcn_exp2f(ea[(n - 1) & 3]); pin(ea[(n - 1) & 3]); }
        if constexpr (n < 8) { ea[n & 3] = __builtin_fmaf(ea[n & 3], gc[1], gc[0]); pin(ea[n & 3]); }
      }
    };
    bf16x8 sf;
    u32 peek;
    auto fold_load = [&](int t) {
      sf = *(const __attribute__((address_space(3))) bf16x8*)(uintptr_t)(sfo + t * 512);
      gcs[t & 1] = *(const __attribute__((address_space(3))) f32x4*)(uintptr_t)(sfo + (L_RS - L_SF) + t * 512);
    };
    auto fold = [&](int t) {
      acc[t % 3] = MFMA32(as_frag(aux), sf, acc[t % 3]);
      pin(acc[t % 3]);
    };
    constexpr int PUB = (ES1 + 33 > 102 ? ES1 + 33 : 102);
    static_for<0, 4>([&](auto T) {
      constexpr int t = decltype(T)::value;
      static_for<0, 32>([&](auto KS) {
        constexpr int ks = decltype(KS)::value, s = 32 * t + ks;
        if constexpr (s == 14 - XD) {
          wait_vm<up_after(-1, s, 0)>();
          barrier();
        }
        if constexpr (s == ES0 - 10) peek = lds_peek(smem, 7, 0);
        if constexpr (s == ES0 - 6) {
          if (__builtin_expect(!__builtin_amdgcn_readfirstlane((int)peek), 0)) lds_wait<1>(smem, 7);
          MEMFENCE();
          fold_load(0);
        }
        if constexpr (t > 0 && ks == 29) fold_load(t);
        if constexpr (s == PUB) {
          wait_vm<up_after(ES1 + 20, s, 1)>();
          lds_set(smem, 11 + (w >> 2), w & 3);
        }
        if constexpr (s + XD < 128) xf[(s + XD) & 3] = xfrag(s + XD);
        if constexpr (ks == 0) acc[t % 3] = MFMA32(as_frag(wf[0]), xf[s & 3], f32x16{});
        else acc[t % 3] = MFMA32(as_frag(wf[ks]), xf[s & 3], acc[t % 3]);
        pin(acc[t % 3]);
        if constexpr (s == ES0 - 3) fold(0);
        if constexpr (t > 0 && ks == 31) fold(t);
        if constexpr (s >= ES0 && s < ES0 + 21) epi(std::integral_constant<int, 0>{}, std::integral_constant<int, s - ES0>{});
        if constexpr (s >= ES1 && s < ES1 + 21) epi(std::integral_constant<int, 1>{}, std::integral_constant<int, s - ES1>{});
        if constexpr (s >= ES2 && s < ES2 + 21) epi(std::integral_constant<int, 2>{}, std::integral_constant<int, s - ES2>{});
        if constexpr (t == 3 && ks < 10) wr[ks] = wd_frag(ks);
        if constexpr (t == 3 && ks == 11) {
#pragma unroll
          for (int j = 0; j < 10; ++j) pin(wr[j]);
        }
        if constexpr (up_xpiece(s) >= 0) load_x(up_xpiece(s));
        if constexpr (up_stage(s) >= 0) stage_wd(up_stage(s));
        __builtin_amdgcn_sched_barrier(0);
      });
      stamp_t(18 + 2 * t);
      if constexpr (t == 0) wait_vm<up_after(9, 32, 0)>();
      if constexpr (t == 1) wait_vm<up_after(27, 64, 0)>();
      if constexpr (t == 2) wait_vm<up_after(ES0 + 20 > 77 ? ES0 + 20 : 77, 96, 0)>();
      lds_barrier();
      stamp_t(19 + 2 * t);
    });
    load_flag(std::integral_constant<int, 0>{});
#pragma unroll
    for (int i = 0; i < 8; i += 2) {
      float v[2];
#pragma unroll
      for (int e = 0; e < 2; ++e) {
        const f32x4& gc = gcs[1];
        const float a = acc[0][i + e];
        const float ex = __builtin_amdgcn_exp2f(a * __builtin_fmaf(a * a, gc[1], gc[0]));
        v[e] = a * acc[0][8 + i + e] * __builtin_amdgcn_rcpf(__builtin_fmaf(ex, gc[2], gc[2]));
      }
      o[i >> 1] = pack_bf16(v[0], v[1]);
    }
    gstore16(o, l16, actg[1] + (size_t)NFT * 1024);
    STAMP(26);
    wait_vm<up_after(ES2 + 20, 128, 1) + 2>();
    down_phase(std::integral_constant<int, 0>{});
  }
  DIAG_END(ws);
}

extern "C" void kernel_launch(void* const* d_in, const int* in_sizes, int n_in, void* d_out, int out_size, void* d_ws,
                              size_t ws_size, hipStream_t stream) {
  Params p;
  p.x = (const u16*)d_in[0];
  p.la = (const u16*)d_in[1];
  p.ai = (const u16*)d_in[2];
  p.cr = (const u16*)d_in[3];
  p.ci = (const u16*)d_in[4];
  p.ldt = (const u16*)d_in[5];
  p.dsk = (const u16*)d_in[6];
  p.wg = (const u16*)d_in[8];
  p.wu = (const u16*)d_in[9];
  p.wd = (const u16*)d_in[10];
  p.c = (const u16*)d_in[11];
  p.wm = (const u16*)d_in[12];
  p.out = (u16*)d_out;
  p.ws = (char*)d_ws;
  (void)hipFuncSetAttribute((const void*)fused_block, hipFuncAttributeMaxDynamicSharedMemorySize, SMEM);
  fused_block<<<256, 512, SMEM, stream>>>(p);
}
```

```cpp
#include <hip/hip_runtime.h>
#include <hip/hip_bf16.h>
#include <stdint.h>
#include <type_traits>

typedef unsigned short u16;
typedef unsigned int u32;
typedef unsigned long long u64;
typedef __attribute__((ext_vector_type(8))) __bf16 bf16x8;
typedef __attribute__((ext_vector_type(4))) __bf16 bf16x4;
typedef __attribute__((ext_vector_type(2))) __bf16 bf16x2;
typedef __attribute__((ext_vector_type(2))) _Float16 f16x2;
typedef __attribute__((ext_vector_type(8))) _Float16 f16x8;
typedef __attribute__((ext_vector_type(2))) float f32x2;
typedef __attribute__((ext_vector_type(4))) float f32x4;
typedef __attribute__((ext_vector_type(16))) float f32x16;
typedef __attribute__((ext_vector_type(4))) u32 u32x4;
typedef __attribute__((ext_vector_type(2))) u32 u32x2;
typedef __attribute__((address_space(3))) bf16x4* lds_bf16x4_p;
typedef __attribute__((address_space(3))) char* lds_char_p;
typedef __attribute__((address_space(3))) u32* lds_u32_p;
typedef __amdgpu_buffer_rsrc_t rsrc_t;

#define DEVI __device__ __forceinline__

constexpr int SL = 2048, DM = 512, NS = 128, DF = 1792;
constexpr int NFT = DF / 16;
constexpr int SPIN_CAP = 1 << 15;
#define GDELAY 2
#define ESTART 42
#define SDELAY 4
#define SPRIO 0
#define AWIN 7
#define LO2 7
#define LO3 14
#define VDELAY 16
#define XLATE 46
#define ADELAY 8
#define ESTART2 104
#define WDELAY 6
constexpr int CODE_BYTES = 38 * 1024;

constexpr u32 OFF_E = 0;
constexpr u32 OFF_GST = 4096;
constexpr u32 OFF_FX = 524288;
constexpr u32 OFF_FW = 81920;
constexpr u32 OFF_FA = 131072;
constexpr u32 OFF_FM = 114688;
constexpr u32 OFF_GS2 = 196608;
constexpr u32 OFF_WP = 3u << 20;
constexpr u32 OFF_X1 = OFF_WP + (4u << 20);
constexpr u32 OFF_ACT = OFF_X1 + (2u << 20);
constexpr u32 OFF_XG = OFF_ACT + (8u << 20);
constexpr u32 OFF_XS = OFF_XG + (4u << 20);
constexpr u32 OFF_GM = OFF_XS + (2u << 20);
constexpr u32 WS_USED = OFF_GM + 16 * 24576;

constexpr int KG_ROWB = 272;
constexpr int KG_WAVEB = 57 * KG_ROWB;
constexpr int L_KC = 4 * KG_WAVEB;
constexpr int CV_KCOPY = 4160;
constexpr int L_PARK = 0;
constexpr int NPARK = 21;
constexpr int CV_K = 4 * NPARK * 1024;
constexpr int CV_H = CV_K + 4 * CV_KCOPY;
constexpr int CV_HB = 2288 * 2;
constexpr int CV_X = CV_H + 2 * CV_HB;
constexpr int CV_P = CV_X + 8192;
constexpr int L_TR = CV_P;
static_assert(L_KC + 4 * 656 * 4 <= CV_K, "front LDS map");
constexpr int L_RES = 131072;
constexpr int L_SPARE = L_RES + 8192;
constexpr int L_SF = L_SPARE + 16384;
static_assert(CV_P + 32768 + 16 <= L_SF, "front LDS map");
constexpr int L_RS = L_SF + 2048;
constexpr int L_V = L_SF - 2112;
static_assert(CV_P + 32768 + 16 <= L_V, "front LDS map");
constexpr int L_GB = L_RS + 2048;
constexpr int L_SYNC = L_GB + 256;
constexpr int L_EP = L_SYNC + 256;
constexpr int SMEM = L_EP + 32;

#define VPAD
#define DIAG_DECL
#define STAMP(i)
#define RSTAMP(i)
#define DIAG_END(wsp)

template <int I, int N, class F>
DEVI void static_for(F&& f) {
  if constexpr (I < N) {
    f(std::integral_constant<int, I>{});
    static_for<I + 1, N>(f);
  }
}

DEVI float bflo(u32 v) { return __uint_as_float(v << 16); }
DEVI float bfhi(u32 v) { return __uint_as_float(v & 0xffff0000u); }
DEVI f32x2 unpack2(u32 v) { return f32x2{bflo(v), bfhi(v)}; }
DEVI float b2f(u16 v) { return __uint_as_float(((u32)v) << 16); }
DEVI u32 pack_bf16(float a, float b) {
  f32x2 v = {a, b};
  return __builtin_bit_cast(u32, __builtin_convertvector(v, bf16x2));
}
DEVI float rb(float a) { return bflo(pack_bf16(a, a)); }
DEVI u32 pack_f16(f32x2 v) { return __builtin_bit_cast(u32, __builtin_convertvector(v, f16x2)); }
DEVI u32 perm_lo(u32 a, u32 b) { return __builtin_amdgcn_perm(b, a, 0x05040100u); }
DEVI u32 perm_hi(u32 a, u32 b) { return __builtin_amdgcn_perm(b, a, 0x07060302u); }
DEVI float dot2(u32 a, u32 b, float c) {
  return __builtin_amdgcn_fdot2_f32_bf16(__builtin_bit_cast(bf16x2, a), __builtin_bit_cast(bf16x2, b), c, false);
}
constexpr u32 ONES2 = 0x3f803f80u;

template <int CTRL>
DEVI float dpp(float v) { return __int_as_float(__builtin_amdgcn_update_dpp(0, __float_as_int(v), CTRL, 0xf, 0xf, true)); }
DEVI float row_sum(float v) {
  v += dpp<0xB1>(v); v += dpp<0x4E>(v); v += dpp<0x141>(v); v += dpp<0x140>(v);
  return v;
}
DEVI float rs32(float a, float b) {
  auto r = __builtin_amdgcn_permlane32_swap(__float_as_uint(a), __float_as_uint(b), false, false);
  return __uint_as_float(r[0]) + __uint_as_float(r[1]);
}
DEVI float rs16(float a, float b) {
  auto r = __builtin_amdgcn_permlane16_swap(__float_as_uint(a), __float_as_uint(b), false, false);
  return __uint_as_float(r[0]) + __uint_as_float(r[1]);
}
DEVI float wave_max(float v) {
  v = fmaxf(v, dpp<0xB1>(v)); v = fmaxf(v, dpp<0x4E>(v)); v = fmaxf(v, dpp<0x141>(v)); v = fmaxf(v, dpp<0x140>(v));
  auto r = __builtin_amdgcn_permlane16_swap(__float_as_uint(v), __float_as_uint(v), false, false);
  v = fmaxf(__uint_as_float(r[0]), __uint_as_float(r[1]));
  auto s = __builtin_amdgcn_permlane32_swap(__float_as_uint(v), __float_as_uint(v), false, false);
  return fmaxf(__uint_as_float(s[0]), __uint_as_float(s[1]));
}

DEVI f32x2 cmul(f32x2 a, f32x2 b) {
  const f32x2 t = a.yy * f32x2{-b.y, b.x};
  return __builtin_elementwise_fma(a.xx, b, t);
}
DEVI f32x2 cexpk(float zr, float zi, float k) {
  const float mag = __builtin_amdgcn_exp2f(zr * (k * 1.4426950408889634f));
  const float rev = __builtin_amdgcn_fractf(zi * (k * 0.15915494309189535f));
  return f32x2{mag * __builtin_amdgcn_cosf(rev), mag * __builtin_amdgcn_sinf(rev)};
}

DEVI u32x4 ld16c(rsrc_t r, u32 off) { return __builtin_amdgcn_raw_buffer_load_b128(r, off, 0, 16); }
DEVI u32x2 ld8c(rsrc_t r, u32 off) { return __builtin_amdgcn_raw_buffer_load_b64(r, off, 0, 16); }
DEVI u32 ld4c(rsrc_t r, u32 off) { return __builtin_amdgcn_raw_buffer_load_b32(r, off, 0, 16); }
DEVI void st16c(rsrc_t r, u32 off, u32x4 v) { __builtin_amdgcn_raw_buffer_store_b128(v, r, off, 0, 16); }
DEVI void st8c(rsrc_t r, u32 off, u32x2 v) { __builtin_amdgcn_raw_buffer_store_b64(v, r, off, 0, 16); }
DEVI void st4c(rsrc_t r, u32 off, u32 v) { __builtin_amdgcn_raw_buffer_store_b32(v, r, off, 0, 16); }
DEVI u32 only(bool c, u32 off) { return c ? off : 0x80000000u; }
#define MEMFENCE() asm volatile("" ::: "memory")
DEVI void drain_vm() {
  __builtin_amdgcn_s_waitcnt(0x0F70);
  asm volatile("s_waitcnt vmcnt(0)" ::: "memory");
}

template <int N>
DEVI float gather_sum(rsrc_t r, u32 off, u32 stride, u32 tag) {
  float v = 0.f;
#pragma unroll 1
  for (int spin = 0; spin < SPIN_CAP; ++spin) {
    u32x2 g[N];
#pragma unroll
    for (int i = 0; i < N; ++i) g[i] = ld8c(r, off + i * stride);
    u32 bad = 0;
    v = 0.f;
#pragma unroll
    for (int i = 0; i < N; ++i) {
      v += __uint_as_float(g[i][0]);
      bad |= g[i][1] ^ tag;
    }
    if (__all(bad == 0)) break;
    __builtin_amdgcn_s_sleep(2);
    MEMFENCE();
  }
  MEMFENCE();
  return v;
}
DEVI float sum_hi4(float v) {
  v += dpp<0x124>(v);
  v += dpp<0x128>(v);
  v = rs16(v, v);
  return rs32(v, v);
}
DEVI u32 silu2(u32 c) {
  const float a = bflo(c), b = bfhi(c);
  return pack_bf16(a * __builtin_amdgcn_rcpf(1.f + __expf(-a)), b * __builtin_amdgcn_rcpf(1.f + __expf(-b)));
}
DEVI float rdlane(float v, int l) { return __int_as_float(__builtin_amdgcn_readlane(__float_as_int(v), l)); }
DEVI void poll_flag(rsrc_t r, u32 off, u32 tag) {
#pragma unroll 1
  for (int spin = 0; spin < SPIN_CAP; ++spin) {
    const u32 f = ld4c(r, off);
    if (__all(f == tag)) break;
    __builtin_amdgcn_s_sleep(1);
    MEMFENCE();
  }
  MEMFENCE();
}

DEVI void lds_set(char* smem, int grp, int word) {
  MEMFENCE();
  *(__attribute__((address_space(3))) volatile u32*)(smem + L_SYNC + 16 * grp + 4 * word) = 1u;
  MEMFENCE();
}
DEVI u32 lds_peek(char* smem, int grp, int word) {
  return *(__attribute__((address_space(3))) volatile u32*)(smem + L_SYNC + 16 * grp + 4 * word);
}
DEVI u32 lds_arrive(char* smem, int grp) {
  u32 old;
  const u32 addr = (u32)(uintptr_t)(lds_char_p)(smem + L_SYNC + 16 * grp);
  asm volatile("s_mov_b64 exec, 1\n\tds_add_rtn_u32 %0, %1, %2\n\ts_mov_b64 exec, -1\n\ts_waitcnt lgkmcnt(0)"
               : "=&v"(old) : "v"(addr), "v"(1u) : "memory");
  return (u32)__builtin_amdgcn_readfirstlane((int)old);
}
template <int MASK>
DEVI void lds_wait(char* smem, int grp) {
  typedef __attribute__((address_space(3))) volatile u32x4* lds_u32x4_p;
#pragma unroll 1
  for (int spin = 0; spin < (1 << 22); ++spin) {
    MEMFENCE();
    const u32x4 v = *(lds_u32x4_p)(smem + L_SYNC + 16 * grp);
    const u32 a = ((MASK & 1) ? v[0] : 1u) & ((MASK & 2) ? v[1] : 1u) & ((MASK & 4) ? v[2] : 1u) & ((MASK & 8) ? v[3] : 1u);
    if (__builtin_amdgcn_readfirstlane((int)a)) break;
    __builtin_amdgcn_s_sleep(1);
  }
  MEMFENCE();
}

template <int MA, int MB>
DEVI void lds_wait2(char* smem, int ga, int gb) {
  typedef __attribute__((address_space(3))) volatile u32x4* lds_u32x4_p;
#pragma unroll 1
  for (int spin = 0; spin < (1 << 22); ++spin) {
    MEMFENCE();
    const u32x4 v = *(lds_u32x4_p)(smem + L_SYNC + 16 * ga), u = *(lds_u32x4_p)(smem + L_SYNC + 16 * gb);
    const u32 a = ((MA & 1) ? v[0] : 1u) & ((MA & 2) ? v[1] : 1u) & ((MA & 4) ? v[2] : 1u) & ((MA & 8) ? v[3] : 1u) &
                  ((MB & 1) ? u[0] : 1u) & ((MB & 2) ? u[1] : 1u) & ((MB & 4) ? u[2] : 1u) & ((MB & 8) ? u[3] : 1u);
    if (__builtin_amdgcn_readfirstlane((int)a)) break;
  }
  MEMFENCE();
}

DEVI void glds16(u32 voff, const void* sbase, u32 ldsaddr) {
  asm volatile(VPAD "s_mov_b32 m0, %2\n\ts_nop 0\n\tglobal_load_lds_dwordx4 %0, %1 sc1" ::"v"(voff), "s"(sbase), "s"(ldsaddr) : "memory");
}
DEVI void glds16p2(u32 voff0, u32 voff1, const void* sbase, u32 ldsaddr) {
  asm volatile(VPAD "s_mov_b32 m0, %3\n\ts_nop 0\n\tglobal_load_lds_dwordx4 %0, %2\n\tglobal_load_lds_dwordx4 %1, %2 offset:1024"
               ::"v"(voff0), "v"(voff1), "s"(sbase), "s"(ldsaddr) : "memory");
}
template <int N>
DEVI void wait_vm() { asm volatile("s_waitcnt vmcnt(%0)" ::"n"(N) : "memory"); }
template <int N, class T>
DEVI void wait_vm_reg(T& r) { asm volatile("s_waitcnt vmcnt(%1)" : "+v"(r) : "n"(N)); }
template <class T>
DEVI void pin(T& r) { asm volatile("" : "+v"(r)); }
template <int OFF>
DEVI void gload16(u32x4& r, u32 voff, const void* sbase) {
  asm volatile(VPAD "global_load_dwordx4 %0, %1, %2 offset:%3 sc1" : "=v"(r) : "v"(voff), "s"(sbase), "n"(OFF) : "memory");
}
template <int OFF>
DEVI void gload4(u32& r, u32 voff, const void* sbase) {
  asm volatile(VPAD "global_load_dword %0, %1, %2 offset:%3 sc1" : "=v"(r) : "v"(voff), "s"(sbase), "n"(OFF) : "memory");
}
DEVI void gstore16(u32x4 v, u32 voff, const void* sbase) {
  asm volatile(VPAD "global_store_dwordx4 %0, %1, %2 sc1\n\ts_nop 1" ::"v"(voff), "v"(v), "s"(sbase) : "memory");
}
DEVI void barrier() {
  MEMFENCE();
  __builtin_amdgcn_s_barrier();
  MEMFENCE();
}
DEVI void lds_barrier() {
  asm volatile("s_waitcnt lgkmcnt(0)" ::: "memory");
  __builtin_amdgcn_s_barrier();
  MEMFENCE();
}
DEVI bf16x4 tr_read(const char* p) { return __builtin_amdgcn_ds_read_tr16_b64_v4bf16((lds_bf16x4_p)(p)); }
DEVI bf16x8 tr_frag(const char* p0, const char* p1) {
  return __builtin_shufflevector(tr_read(p0), tr_read(p1), 0, 1, 2, 3, 4, 5, 6, 7);
}
DEVI bf16x8 tr_frag_at(u32 a0, u32 a1) {
  return __builtin_shufflevector(__builtin_amdgcn_ds_read_tr16_b64_v4bf16((lds_bf16x4_p)(uintptr_t)a0),
                                 __builtin_amdgcn_ds_read_tr16_b64_v4bf16((lds_bf16x4_p)(uintptr_t)a1), 0, 1, 2, 3, 4, 5, 6, 7);
}
DEVI bf16x8 as_frag(u32x4 v) { return __builtin_bit_cast(bf16x8, v); }
#define MFMA32(a, b, c) __builtin_amdgcn_mfma_f32_32x32x16_bf16(a, b, c, 0, 0, 0)
#define MFMA16(a, b, c) __builtin_amdgcn_mfma_f32_16x16x32_bf16(a, b, c, 0, 0, 0)

DEVI void transpose8(const u32x4 (&r)[8], u32x4 (&o)[8]) {
#pragma unroll
  for (int k = 0; k < 8; ++k)
#pragma unroll
    for (int i = 0; i < 4; ++i)
      o[k][i] = (k & 1) ? perm_hi(r[2 * i][k >> 1], r[2 * i + 1][k >> 1]) : perm_lo(r[2 * i][k >> 1], r[2 * i + 1][k >> 1]);
}
DEVI u32 ln_pack(float s, float q) {
  const float mean = s * (1.f / DM);
  const float var = fmaxf(q * (1.f / DM) - mean * mean, 0.f);
  return pack_bf16(mean, __builtin_amdgcn_sqrtf(rb(rb(var) + 1e-5f)));
}

template <int NP>
DEVI void mod_part(const u32x4 (&wv)[8], const u32 (&sl)[NP], rsrc_t r, u32 dst, u32 tag, int lane) {
  float acc[8] = {};
#pragma unroll
  for (int j = 0; j < NP; ++j)
#pragma unroll
    for (int k = 0; k < 4; ++k) {
      acc[2 * k] = dot2(perm_lo(wv[2 * j][k], wv[2 * j + 1][k]), sl[j], acc[2 * k]);
      acc[2 * k + 1] = dot2(perm_hi(wv[2 * j][k], wv[2 * j + 1][k]), sl[j], acc[2 * k + 1]);
    }
#pragma unroll
  for (int i = 0; i < 4; ++i) acc[i] = rs32(acc[i], acc[i + 4]);
#pragma unroll
  for (int i = 0; i < 2; ++i) {
    acc[i] = rs16(acc[i], acc[i + 2]);
    acc[i] += dpp<0x128>(acc[i]);
  }
  st16c(r, only((lane & 8) == 0, dst + 8 * (8 * (lane & 7) + 2 * (lane >> 4))), u32x4{__float_as_uint(acc[0]), tag, __float_as_uint(acc[1]), tag});
}

constexpr int ES0 = ESTART, EL = 21, ES1 = ES0 + EL > 66 ? ES0 + EL : 66, ES2 = ESTART2;
static_assert(ES2 + EL <= 125 && ES2 >= 99 && ES2 >= ES1 + EL && ES0 - 3 < 61 && ES0 + EL - 1 > 56, "activation streams");
constexpr int XL = XLATE;
constexpr int up_xpiece(int s) { return s < 10 && (s & 1) ? 5 + s / 2 : s >= XL && s < XL + 10 ? 10 + (s - XL) : -1; }
constexpr int up_stage(int s) { return s == 33 ? 0 : s == 35 ? 1 : s == 80 ? 2 : s == 82 ? 3 : s >= 108 && (s & 3) == 0 ? 9 + (s - 108) / 4 : -1; }
constexpr bool up_store(int s) { return s == ES0 + EL - 1 || s == ES1 + EL - 1 || s == ES2 + EL - 1; }
constexpr int up_after(int a, int b, int with) {
  int n = with ? (up_xpiece(a) >= 0) + 2 * (up_stage(a) >= 0) : 0;
  for (int s = a + 1; s < b; ++s) n += up_store(s) + (up_xpiece(s) >= 0) + 2 * (up_stage(s) >= 0);
  return n;
}

static_assert(AWIN >= 7 && AWIN <= 14 && LO2 >= 3 && 13 - AWIN < LO2 && 20 - AWIN < LO3 && LO3 - 3 > LO2, "down schedule");
constexpr int dn_iss(int n) {
  const int lo = n < 14 ? -1 : n < 21 ? LO2 : LO3;
  return n - AWIN > lo ? n - AWIN : lo;
}
constexpr int dn_issued(int j) {
  int n = 0;
  while (n < 28 && dn_iss(n) < j) ++n;
  return n;
}

struct Params {
  const u16 *x, *la, *ai, *cr, *ci, *ldt, *dsk, *wg, *wu, *wd, *c, *wm;
  u16* out;
  char* ws;
};

__global__ __launch_bounds__(512, 2) void fused_block(Params P) {
  extern __shared__ __attribute__((aligned(16))) char smem[];
  DIAG_DECL
  const int tid = threadIdx.x, lane = tid & 63;
  const int w = __builtin_amdgcn_readfirstlane(tid >> 6);
  const int b = blockIdx.x, xcd = b & 7, bi = b >> 3;
  char* const ws = P.ws;
  const rsrc_t R = __builtin_amdgcn_make_buffer_rsrc(ws, 0, WS_USED, 0x00020000);
  const int mt = 4 * (xcd & 3) + (bi & 3), fb = 2 * (bi >> 2) + (xcd >> 2);
  const int par = fb & 1;
  const int m2 = 2 * xcd + (bi & 1), nt = bi >> 2, cp2 = (bi >> 1) & 1;
  const int ft = 7 * fb + w;
  const u32 l16 = lane * 16;
  const char* wsrc = ws + OFF_WP + (size_t)ft * 32768;
  u32x4 wf[32];
  u32 code_touch = 0;
  {
    const __attribute__((address_space(1))) char* pc;
    asm volatile("s_getpc_b64 %0" : "=s"(pc));
    if (w >= 6)
      code_touch = *(const __attribute__((address_space(1))) u32*)(pc + min((2 * (bi % 3) + (w & 1)) * 8192 + lane * 128, CODE_BYTES));
  }
  if (tid < 64) *(u32*)(smem + L_SYNC + 4 * tid) = 0;
  STAMP(32);
  u32x4 pl[8];
  u32 ps[8];
  if (w < 4) {
    const int d = 64 * xcd + 2 * bi + (w >> 1), n = 64 * (w & 1) + lane;
    ps[0] = P.la[d * NS + n];
    ps[1] = P.ai[d * NS + n];
    ps[2] = P.cr[d * NS + n];
    ps[3] = P.ci[d * NS + n];
    ps[4] = P.cr[d * NS + (n ^ 64)];
    ps[5] = P.ci[d * NS + (n ^ 64)];
    ps[6] = P.ldt[d];
    ps[7] = P.dsk[(d & ~1) + ((lane >> 2) & 1)];
  } else if (w == 4) {
#pragma unroll
    for (int i = 0; i < 8; ++i) pl[i] = *(const u32x4*)(P.x + (size_t)(8 * b + 2 * (lane & 3) + (i & 1)) * DM + 8 * (lane >> 2) + 128 * (i >> 1));
  } else if (w == 5) {
    const int ra = 32 * (b & 15) + 4 * (lane >> 3);
    const u32x2 cv = *(const u32x2*)(P.c + ra);
    ps[0] = cv[0];
    ps[1] = cv[1];
#pragma unroll
    for (int i = 0; i < 4; ++i) pl[i] = *(const u32x4*)(P.wm + (size_t)(ra + i) * 3072 + 64 * (b >> 4) + 8 * (lane & 7));
  } else {
    const int un = min(b, 223);
    const u16* src = (((un >> 2) & 1) ? P.wu : P.wg) + (size_t)(64 * (2 * (un & 3) + (w & 1)) + 8 * (lane >> 3)) * DF + 64 * (un >> 3) + 8 * (lane & 7);
    __builtin_amdgcn_s_sleep(WDELAY);
#pragma unroll
    for (int i = 0; i < 8; ++i) pl[i] = *(const u32x4*)(src + i * DF);
  }
  u32 tag;
  asm volatile("s_load_dword %0, %1, 0x0\n\ts_waitcnt lgkmcnt(0)" : "=s"(tag) : "s"(ws + OFF_E) : "memory");
  tag += 0x9E3779B9u;
  STAMP(34);
  barrier();
  STAMP(35);
  auto load_w = [&](auto J) {
    constexpr int j = decltype(J)::value;
    gload16<(j & 3) * 1024>(wf[j], l16, wsrc + (j >> 2) * 4096);
  };

  const u32 lds0 = (u32)(uintptr_t)(lds_char_p)smem;
  const char* ag[2] = {ws + OFF_ACT + ((size_t)(4 * m2 + cp2) * NFT + 14 * w + 7 * cp2) * 1024,
                       ws + OFF_ACT + ((size_t)(4 * m2 + cp2) * NFT + 14 * w + 7 * (cp2 ^ 1)) * 1024};
  const char* wdg[2] = {(const char*)(P.wd + (size_t)(224 * w + 112 * cp2) * DM + 64 * nt),
                        (const char*)(P.wd + (size_t)(224 * w + 112 * (cp2 ^ 1)) * DM + 64 * nt)};
  const u32 wdo = (lane >> 3) * 1024 + 16 * ((lane & 7) ^ ((lane >> 2) & 4));
  auto slot_lds = [&](int k, int wv) {
    const int sl = k < 9 ? k : k - 9;
    return sl == 4 ? L_SPARE + wv * 2048 : ((sl < 4 ? sl : sl - 1) >> 1) * 32768 + wv * 4096 + ((sl < 4 ? sl : sl - 1) & 1) * 2048;
  };
  auto stage_wd = [&](int k) { glds16p2(wdo, wdo + 7168, wdg[k / 7] + (k % 7) * 16384, lds0 + slot_lds(k, w)); };
  auto stage_all = [&](int k) {
    wait_vm<32>();
#pragma unroll 2
    for (int v = 0; v < 8; ++v) glds16p2(wdo, wdo + 7168, wdg[k / 7] + (k % 7) * 16384 + (v - 7) * (224 * 1024), lds0 + slot_lds(k, v));
  };
  auto stamp_t = [&](int i) { STAMP(i); };
  const int q4 = (lane & 15) >> 2, p4 = lane & 3, hh = lane >> 5, blk = (lane >> 4) & 1;
  u32 wfo[2][3];
#pragma unroll
  for (int i = 0; i < 2; ++i) {
    const u32 o = lds0 + (8 * hh + q4) * 128 + 64 * (i ^ (q4 >> 1)) + 32 * blk + 8 * p4;
    wfo[i][0] = o + w * 4096;
    wfo[i][1] = o + w * 4096 + 65536;
    wfo[i][2] = o + w * 2048 + L_SPARE;
  }
  auto wd_frag = [&](int j) {
    const int so = slot_lds(j >> 1, 0), r = so >= L_SPARE ? 2 : so >> 16;
    const u32 a = wfo[j & 1][r] + (r == 2 ? 0 : so & 65535);
    return tr_frag_at(a, a + 512);
  };
  bf16x8 wr[10];
  u32x4 av[28];
  auto load_a = [&](auto J) {
    constexpr int j = decltype(J)::value, g = j / 7, i = j % 7;
    gload16<(i & 3) * 1024>(av[j], l16, ag[g & 1] + (size_t)(g >> 1) * 2 * NFT * 1024 + (i >> 2) * 4096);
  };
  const char* fag[2] = {ws + OFF_FA + m2 * 4096 + (2 * w + cp2) * 64, ws + OFF_FA + m2 * 4096 + (2 * w + (cp2 ^ 1)) * 64};
  u32 af[4];
  auto load_flag = [&](auto G) { gload4<decltype(G)::value * 1024>(af[decltype(G)::value], 0u, fag[decltype(G)::value & 1]); };
  auto check_flag = [&](auto G, auto YOUNGER) {
    constexpr int g = decltype(G)::value;
    wait_vm_reg<decltype(YOUNGER)::value>(af[g]);
    if (__builtin_expect((u32)__builtin_amdgcn_readfirstlane((int)af[g]) != tag, 0))
      poll_flag(R, OFF_FA + m2 * 4096 + g * 1024 + (2 * w + (cp2 ^ (g & 1))) * 64, tag);
    MEMFENCE();
  };

  auto down_phase = [&](auto) {
    constexpr int FD = 3;
    bf16x8 wd[28];
    auto put_partial = [&](int qd, const f32x16& a) {
#pragma unroll
      for (int g = 0; g < 4; ++g)
        *(f32x4*)(smem + qd * 32768 + w * 4096 + g * 1024 + l16) = f32x4{a[4 * g], a[4 * g + 1], a[4 * g + 2], a[4 * g + 3]};
    };
    const int cl = 8 * w + 4 * hh;
    auto finish = [&](int qj) {
      f32x4 s = {0.f, 0.f, 0.f, 0.f};
#pragma unroll
      for (int v = 0; v < 8; ++v) s += *(const f32x4*)(smem + (2 * (w >> 2) + qj) * 32768 + v * 4096 + (w & 3) * 1024 + l16);
      const f32x4 gb = *(const f32x4*)(smem + L_GB + 4 * cl);
      const u32x2 xr = __builtin_bit_cast(u32x2, tr_read(smem + L_RES + qj * 4096 + (cl + q4) * 64 + 32 * (blk ^ hh) + 8 * p4));
      u32x2 o;
#pragma unroll
      for (int e = 0; e < 2; ++e) o[e] = pack_bf16(bflo(xr[e]) + gb[2 * e] * s[2 * e], bfhi(xr[e]) + gb[2 * e + 1] * s[2 * e + 1]);
      return o;
    };
    f32x16 acc[4] = {};
    barrier();
    st4c(R, only(w == 7 && lane == 0, OFF_FA + mt * 4096 + 2 * 1024 + fb * 64), tag);
    MEMFENCE();
    stage_wd(7);
    stage_wd(8);
    check_flag(std::integral_constant<int, 0>{}, std::integral_constant<int, 6>{});
    static_for<0, 2>(load_a);
    load_flag(std::integral_constant<int, 1>{});
    wait_vm<8>();
    stamp_t(27);
    barrier();
    st4c(R, only(w == 7 && lane == 0, OFF_FA + mt * 4096 + 3 * 1024 + fb * 64), tag);
    MEMFENCE();
    static_for<2, 7>(load_a);
    check_flag(std::integral_constant<int, 1>{}, std::integral_constant<int, 6>{});
    static_for<7, dn_issued(0)>(load_a);
#pragma unroll
    for (int j = 0; j < 14; ++j) wd[j] = j < 10 ? wr[j] : wd_frag(j);
    stamp_t(28);
    u16* op = P.out + (size_t)(64 * m2 + 32 * cp2 + (lane & 31)) * DM + 64 * nt + cl;
    u32x2 o0;
    static_for<0, 28>([&](auto J) {
      constexpr int j = decltype(J)::value, g = j / 7, k = j % 14;
      wait_vm_reg<dn_issued(j) - 1 - j>(av[j]);
      acc[g >> 1] = MFMA32(wd[2 * k], as_frag(av[j]), acc[g >> 1]);
      acc[2 + (g >> 1)] = MFMA32(wd[2 * k + 1], as_frag(av[j]), acc[2 + (g >> 1)]);
      pin(acc[g >> 1]);
      pin(acc[2 + (g >> 1)]);
      if constexpr (j == dn_iss(14)) check_flag(std::integral_constant<int, 2>{}, std::integral_constant<int, dn_issued(j) - dn_issued(j - FD)>{});
      if constexpr (j == dn_iss(21)) check_flag(std::integral_constant<int, 3>{}, std::integral_constant<int, dn_issued(j) - dn_issued(j - FD)>{});
      if constexpr (j == dn_iss(21)) stamp_t(29);
      if constexpr (j + FD == dn_iss(14)) load_flag(std::integral_constant<int, 2>{});
      if constexpr (j + FD == dn_iss(21)) load_flag(std::integral_constant<int, 3>{});
      static_for<dn_issued(j), dn_issued(j + 1)>(load_a);
      if constexpr (j < 7) {
        wd[14 + 2 * j] = wd_frag(14 + 2 * j);
        wd[15 + 2 * j] = wd_frag(15 + 2 * j);
      }
      if constexpr (j == 15) {
        put_partial(0, acc[0]);
        put_partial(2, acc[2]);
      }
      if constexpr (j == 21) {
        lds_barrier();
        o0 = finish(0);
        pin(o0);
      }
    });
    stamp_t(14);
    put_partial(1, acc[1]);
    put_partial(3, acc[3]);
    *(u32x2*)op = o0;
    stamp_t(15);
    lds_barrier();
    stamp_t(30);
    *(u32x2*)(op + 1024 * DM) = finish(1);
  };

  auto x1_piece = [&](int chunk) {
    const int c0 = 64 * xcd + 2 * bi, c = (lane >> 2) & 1, tile = 8 * chunk + (lane >> 3);
    const float ga = *(const float*)(smem + L_EP + 4 * c), cb1 = *(const float*)(smem + L_EP + 8 + 4 * c), dk = *(const float*)(smem + L_EP + 16 + 4 * c);
    const int l0 = 32 * tile + 8 * ((lane & 3) ^ (2 * ((c0 >> 2) & 1)));
    const char* pp = smem + CV_P + c * (16384 + 16);
    const int po = (4 * l0) ^ (16 * (tile & 3));
    const f32x4 y0 = *(const f32x4*)(pp + po) + *(const f32x4*)(pp + 8192 + po);
    const f32x4 y1 = *(const f32x4*)(pp + (po ^ 16)) + *(const f32x4*)(pp + 8192 + (po ^ 16));
    const u32x4 hv = *(const u32x4*)(smem + CV_H + c * CV_HB + 480 + 2 * l0);
    const u32x4 xx = *(const u32x4*)(smem + CV_X + c * 4096 + 2 * l0);
    u32x4 o, os;
#pragma unroll
    for (int k = 0; k < 4; ++k) {
      const u32 yb = k < 2 ? pack_bf16(y0[2 * k], y0[2 * k + 1]) : pack_bf16(y1[2 * k - 4], y1[2 * k - 3]);
      const f32x2 zf = __builtin_elementwise_fma(unpack2(hv[k]), f32x2{dk, dk}, unpack2(yb));
      const f32x2 gz = unpack2(pack_bf16(zf.x, zf.y)) * ga;
      const f32x2 of = unpack2(xx[k]) + unpack2(pack_bf16(gz.x, gz.y));
      o[k] = pack_bf16(of.x, of.y);
      const f32x2 sf = unpack2(o[k]) * cb1;
      os[k] = pack_bf16(sf.x, sf.y);
    }
    const u32 go = (4 * ((tile & 31) >> 1) + 2 * (tile >> 5) + (tile & 1)) * 32768 + c0 * 64 + (lane & 7) * 16;
    st16c(R, OFF_XS + go, os);
    st16c(R, OFF_X1 + go, o);
    MEMFENCE();
  };
  auto x1_arrive = [&](int hf) {
    const bool last = lds_arrive(smem, hf ? 14 : 4) == 3;
    st4c(R, only(last && lane < 8, OFF_FX + hf * 32768 + 4096 * lane + 16 * b), tag);
    MEMFENCE();
  };

  if (w < 4) {
    const int ch = w >> 1, half = w & 1;
    const int c0 = 64 * xcd + 2 * bi;
    const int n = 64 * half + lane;
    int kidx[9];
#pragma unroll
    for (int e = 0; e < 5; ++e) {
      const int l0 = 1023 - 8 * n - 2 * e, l1 = l0 - 1;
      const u32 pb = pack_bf16((float)l0, (float)l1);
      kidx[2 * e] = L_KC + 4 * (w * 656 + ((l0 < 128) ? max(l0, 0) : (int)(pb & 0xffffu) - 133 * 128));
      if (e < 4) kidx[2 * e + 1] = L_KC + 4 * (w * 656 + ((l1 < 128) ? l1 : (int)(pb >> 16) - 133 * 128));
    }
#pragma unroll
    for (int e = 0; e < 9; ++e) pin(kidx[e]);
    if (lane < 2) *(u32x4*)(smem + CV_K + w * CV_KCOPY + 4096 + lane * 16) = u32x4{0, 0, 0, 0};
    __builtin_amdgcn_s_waitcnt(0x0F70);
#pragma unroll
    for (int i = 0; i < 8; ++i) pin(ps[i]);
    const float v_cr = bflo(ps[2]), v_ci = bflo(ps[3]), o_cr = bflo(ps[4]), o_ci = bflo(ps[5]);
    const float dt = rb(expf(bflo(ps[6])));
    const float zr = -rb(expf(bflo(ps[0]))) * dt, zi = bflo(ps[1]) * dt;
    STAMP(3);
    const float mx = wave_max(fmaxf(fmaxf(fabsf(v_cr), fabsf(v_ci)), fmaxf(fabsf(o_cr), fabsf(o_ci))));
    const int ex = min(max((int)(__float_as_uint(mx) >> 23), 1), 252);
    const float sdn = __uint_as_float((u32)(254 - ex) << 23);
    const float sup = __uint_as_float((u32)(ex + 1) << 23) * dt;
    {
      char* base = smem + w * KG_WAVEB + lane * 4;
      auto put = [&](int row, f32x2 v) { *(u32*)(base + row * KG_ROWB) = pack_f16(v); };
      const f32x2 e8 = cexpk(zr, zi, 8.f), e16 = cexpk(zr, zi, 16.f);
      const f32x2 e1 = cexpk(zr, -zi, 1.f), e2 = cexpk(zr, -zi, 2.f);
      const f32x2 e128 = cexpk(zr, -zi, 128.f), e256 = cexpk(zr, -zi, 256.f);
      f32x2 f0 = e16, f1 = e8;
      put(41, f32x2{1.f, 0.f});
      put(42, e8);
#pragma unroll
      for (int j = 2; j < 16; j += 2) {
        f1 = cmul(f1, e16);
        put(41 + j, f0);
        put(42 + j, f1);
        if (j < 14) f0 = cmul(f0, e16);
      }
      const f32x2 e4 = cmul(e2, e2);
      f32x2 g[2];
      g[0] = f32x2{v_cr * sdn, -v_ci * sdn};
      g[1] = cmul(g[0], e128);
#pragma unroll
      for (int i = 0; i < 17; ++i) {
        const f32x2 gi = g[i & 1];
        if (i < 2) {
          f32x2 a = gi, c = cmul(gi, e1);
#pragma unroll
          for (int e = 0; e < 8; e += 2) {
            put(8 * i + e, a);
            put(8 * i + e + 1, c);
            if (e < 6) { a = cmul(a, e2); c = cmul(c, e2); }
          }
        } else if (i < 4) {
          const f32x2 c = cmul(gi, e2);
          put(16 + 4 * (i - 2), gi);
          put(17 + 4 * (i - 2), c);
          put(18 + 4 * (i - 2), cmul(gi, e4));
          put(19 + 4 * (i - 2), cmul(c, e4));
        } else if (i < 8) {
          put(24 + 2 * (i - 4), gi);
          put(25 + 2 * (i - 4), cmul(gi, e4));
        } else {
          put(32 + (i - 8), gi);
        }
        if (i + 2 < 17) g[i & 1] = cmul(gi, e256);
      }
    }
    lds_set(smem, 0, w);
    STAMP(4);

    const int r = lane & 15, q = lane >> 4;
    {
      f32x4 acc[3] = {};
#pragma unroll
      for (int hf = 0; hf < 2; ++hf) {
        if (hf == 0) {
          if (ch) lds_wait<12>(smem, 0);
          else lds_wait<3>(smem, 0);
        }
        const char* rbase = smem + (2 * ch + hf) * KG_WAVEB + r * KG_ROWB + q * 16;
        f16x8 fr[4][4];
#pragma unroll
        for (int ks = 0; ks < 4; ++ks) {
          fr[ks][3] = *(const f16x8*)(rbase + ks * 64 + 41 * KG_ROWB);
#pragma unroll
          for (int t = 0; t < 3; ++t) fr[ks][t] = *(const f16x8*)(rbase + ks * 64 + 16 * t * KG_ROWB);
        }
        __builtin_amdgcn_sched_barrier(0);
#pragma unroll
        for (int ks = 0; ks < 4; ++ks)
#pragma unroll
          for (int t = 0; t < 3; ++t) acc[t] = __builtin_amdgcn_mfma_f32_16x16x32_f16(fr[ks][t], fr[ks][3], acc[t], 0, 0, 0);
      }
      pin(acc[0]); pin(acc[1]); pin(acc[2]);
      STAMP(12);
      float* kc = (float*)(smem + L_KC) + w * 656;
      *(f32x4*)(kc + 128 * (q >> 1) + 4 * (q & 1) + 8 * r) = acc[0] * sup;
      if (q < 2) {
        *(f32x4*)(kc + 256 + 64 * q + 4 * r) = acc[1] * sup;
      } else {
        *(f32x2*)(kc + 384 + 64 * (q - 2) + 2 * r) = f32x2{acc[1][0], acc[1][1]} * sup;
        *(f32x2*)(kc + 416 + 64 * (q - 2) + 2 * r) = f32x2{acc[1][2], acc[1][3]} * sup;
      }
#pragma unroll
      for (int e = 0; e < 4; ++e)
        if (4 * q + e <= 8) kc[512 + 16 * (4 * q + e) + r] = acc[2][e] * sup;
      MEMFENCE();
      float kv[9];
#pragma unroll
      for (int e = 0; e < 9; ++e) kv[e] = *(const float*)(smem + kidx[e]);
      const float ka = kc[639 - n], kb = kc[640 - n];
      if (n == 127) kv[8] = 0.f;
      const float kt = (n & 1) ? ka : kb;
      const u32 aa = pack_bf16(ka, ka), bb = pack_bf16(kb, kb);
      *(u32x4*)(smem + CV_K + (2 * ch) * CV_KCOPY + 16 * n) = u32x4{bb, pack_bf16(kb, kt), aa, aa};
      *(u32x4*)(smem + CV_K + (2 * ch + 1) * CV_KCOPY + 16 * n) = u32x4{bb, pack_bf16(kt, ka), aa, aa};
      u32x4 o, s;
#pragma unroll
      for (int e = 0; e < 4; ++e) {
        o[e] = pack_bf16(kv[2 * e], kv[2 * e + 1]);
        s[e] = pack_bf16(kv[2 * e + 1], kv[2 * e + 2]);
      }
      *(u32x4*)(smem + CV_K + (2 * ch) * CV_KCOPY + 2048 + 16 * n) = o;
      *(u32x4*)(smem + CV_K + (2 * ch + 1) * CV_KCOPY + 2048 + 16 * n) = s;
    }
    lds_set(smem, 8, w);
    STAMP(5);
    lds_wait2<15, 15>(smem, 8, 2);
    STAMP(7);
    const char* abase = smem + CV_K + (2 * ch + ((r & 1) ^ 1)) * CV_KCOPY + 2 * (2030 - (r & ~1) + 8 * q) - 64 * (4 * half + 3 + 56);
    const char* bbase = smem + CV_H + ch * CV_HB + 2 * (224 + 16 * r + 8 * q) - 64 * (4 * half + 3);
    f32x4 acc[8] = {};
    bf16x8 af[2][8], bfr[2][8];
    u32 ab[4];
#pragma unroll
    for (int i = 0; i < 4; ++i) {
      ab[i] = (u32)(uintptr_t)(lds_char_p)abase + 1024 * i;
      pin(ab[i]);
    }
    auto ldfr = [&](int rho, int i) {
      const lds_u32_p p = (lds_u32_p)(uintptr_t)(ab[(7 - i) >> 1] + 64 * (3 - rho) + 512 * ((7 - i) & 1));
      af[rho & 1][i] = as_frag(u32x4{p[0], p[1], p[2], p[3]});
      bfr[rho & 1][i] = *(const bf16x8*)(bbase + 64 * (3 - rho) + 512 * i);
    };
    auto put_p = [&](int t0) {
#pragma unroll
      for (int t = t0; t < t0 + 4; ++t) *(f32x4*)(smem + CV_P + w * 8192 + ch * 16 + 1024 * t + 64 * r + 16 * (q ^ ((r >> 1) & 3))) = acc[t];
    };
    if (tid < 8 && (tid & 3) == 0) *(float*)(smem + L_EP + 16 + tid) = bflo(ps[7]);
#pragma unroll
    for (int i = 0; i < 4; ++i) ldfr(0, i);
    static_for<0, 48>([&](auto S) {
      constexpr int st = decltype(S)::value, nx = st + 4;
      constexpr bool pa = st < 16;
      constexpr int rho = pa ? st >> 2 : (st - 16) >> 3, m = pa ? st & 3 : (st - 16) & 7;
      if constexpr (nx < 16) ldfr(nx >> 2, nx & 3);
      else if constexpr (nx < 48) ldfr((nx - 16) >> 3, (nx - 16) & 7);
#pragma unroll
      for (int o = 0; o <= m; ++o) {
        if (pa ? m + o < 4 : (m + o >= 4 && m + o < 8)) {
          acc[m + o] = MFMA16(af[rho & 1][m], bfr[rho & 1][o], acc[m + o]);
          if (o != m) acc[m + o] = MFMA16(af[rho & 1][o], bfr[rho & 1][m], acc[m + o]);
        }
      }
#pragma unroll
      for (int i = 0; i < 3; ++i) {
        __builtin_amdgcn_sched_group_barrier(0x008, 1, 0);
        __builtin_amdgcn_sched_group_barrier(0x100, 1, 0);
      }
      __builtin_amdgcn_sched_barrier(0);
      if constexpr (st == 15) {
        put_p(0);
        lds_set(smem, 13, w);
        stamp_t(11);
      }
      if constexpr (st == 23) {
#pragma unroll
        for (int t = 4; t < 8; ++t) pin(acc[t]);
        lds_wait2<15, 14>(smem, 13, 10);
        x1_piece(w);
        stamp_t(16);
      }
      if constexpr (st == 39) {
#pragma unroll
        for (int t = 4; t < 8; ++t) pin(acc[t]);
        drain_vm();
        x1_arrive(0);
        stamp_t(9);
      }
    });
    STAMP(8);
    put_p(4);
    lds_set(smem, 3, w);
  } else {
    if (w != 5) __builtin_amdgcn_s_setprio(SPRIO);
    __builtin_amdgcn_s_waitcnt(0x0F70);
    const int un = min(b, 223), cg = un >> 3, type = (un >> 2) & 1, k8 = 2 * (un & 3) + (w & 1);
    const int kg = lane >> 3, cp = lane & 7;
    if (w < 6) {
      if (w == 4) {
#pragma unroll
        for (int a = 0; a < 4; ++a) {
          pin(pl[2 * a]);
          pin(pl[2 * a + 1]);
#pragma unroll
          for (int k = 0; k < 4; ++k)
            st16c(R, OFF_XG + b * 16384 + (a * 4 + k) * 1024 + l16, u32x4{pl[2 * a][k], tag, pl[2 * a + 1][k], tag});
        }
        STAMP(3);
        float s[2][2] = {}, qq[2][2] = {};
#pragma unroll
        for (int i = 0; i < 8; ++i)
#pragma unroll
          for (int k = 0; k < 4; ++k) {
            s[i & 1][k & 1] = dot2(pl[i][k], ONES2, s[i & 1][k & 1]);
            qq[i & 1][k & 1] = dot2(pl[i][k], pl[i][k], qq[i & 1][k & 1]);
          }
        float v = rs16(rs32(s[0][0] + s[0][1], qq[0][0] + qq[0][1]), rs32(s[1][0] + s[1][1], qq[1][0] + qq[1][1]));
        v += dpp<0x124>(v);
        v += dpp<0x128>(v);
        auto sw = __builtin_amdgcn_permlane32_swap(__float_as_uint(v), __float_as_uint(v), false, false);
        const u32 pk = ln_pack(__uint_as_float(sw[0]), __uint_as_float(sw[1]));
        st8c(R, only(lane < 32 && (lane & 15) < 4, OFF_GST + 64 * b + 16 * (lane & 3) + 8 * (lane >> 4)), u32x2{pk, tag});
        STAMP(33);
        drain_vm();
        lds_set(smem, 5, 1);
        RSTAMP(36);
      } else {
        pin(ps[0]);
        pin(ps[1]);
        const u32 sl[2] = {silu2(ps[0]), silu2(ps[1])};
#pragma unroll
        for (int i = 0; i < 4; ++i) pin(pl[i]);
        u32x4 pb[2][8];
#pragma unroll
        for (int i = 0; i < 8; ++i)
          pb[i >> 2][i & 3] = *(const u32x4*)(P.wm + (size_t)(32 * (b & 15) + 4 * (lane >> 3) + (i & 3)) * 3072 + 1024 * (1 + (i >> 2)) + 64 * (b >> 4) + 8 * (lane & 7));
        MEMFENCE();
        const u32 dst = OFF_GM + (b & 15) * 24576 + 512 * (b >> 4);
        mod_part<2>(pl, sl, R, dst, tag, lane);
        STAMP(33);
        mod_part<2>(pb[0], sl, R, dst + 8192, tag, lane);
        mod_part<2>(pb[1], sl, R, dst + 16384, tag, lane);
        STAMP(4);
      }
      const int t = tid - 256, c0 = 64 * xcd + 2 * bi, pr = c0 >> 1;
      const u32 xo = OFF_XG + (t >> 2) * 16384 + (((pr >> 6) * 4 + (pr & 3)) * 16 + ((pr >> 2) & 15)) * 64 + (t & 3) * 16;
      const u32 mo = OFF_GM + (lane >> 2) * 24576 + 8 * (((lane >> 1) & 1) * DM + c0 + (lane & 1));
      if (lane < 30) *(u32x4*)(smem + CV_H + (w - 4) * CV_HB + lane * 16) = u32x4{0, 0, 0, 0};
      u32x4 gx[8], gs[8];
      u32x2 gm;
      u32 bad = 1;
      lds_wait<2>(smem, 5);
      __builtin_amdgcn_s_sleep(GDELAY);
      STAMP(12);
#pragma unroll 1
      for (int spin = 0; spin < SPIN_CAP; ++spin) {
        if (bad) {
          gm = ld8c(R, mo);
#pragma unroll
          for (int i = 0; i < 8; ++i) {
            gx[i] = ld16c(R, xo + i * (32 * 16384));
            gs[i] = ld16c(R, OFF_GST + 16 * t + 2048 * i);
          }
          bad = gm[1] ^ tag;
#pragma unroll
          for (int i = 0; i < 8; ++i) bad |= (gx[i][1] ^ tag) | (gx[i][3] ^ tag) | (gs[i][1] ^ tag) | (gs[i][3] ^ tag);
        }
        if (__all(bad == 0)) break;
        __builtin_amdgcn_s_sleep(8);
        MEMFENCE();
      }
      drain_vm();
      if (w == 5) lds_set(smem, 10, 0);
      STAMP(6);
      RSTAMP(37);
      const float md = rb(sum_hi4(__uint_as_float(gm[0])));
      const f32x2 sa = {rdlane(md, 0), rdlane(md, 1)}, ca1 = {rb(1.f + rdlane(md, 2)), rb(1.f + rdlane(md, 3))};
#pragma unroll
      for (int i = 0; i < 8; ++i) {
        f32x2 hv[2];
#pragma unroll
        for (int e = 0; e < 2; ++e) {
          const u32 st = gs[i][2 * e], xv = gx[i][2 * e];
          hv[e] = __builtin_elementwise_fma((f32x2{bflo(xv), bfhi(xv)} - bflo(st)) * __builtin_amdgcn_rcpf(bfhi(st)), ca1, sa);
        }
        *(u32*)(smem + CV_H + 480 + 512 * i + 4 * t) = pack_bf16(hv[0].x, hv[1].x);
        *(u32*)(smem + CV_H + CV_HB + 480 + 512 * i + 4 * t) = pack_bf16(hv[0].y, hv[1].y);
      }
      lds_set(smem, 2, w - 4);
      STAMP(7);
#pragma unroll
      for (int i = 0; i < 8; ++i) {
        *(u32*)(smem + CV_X + 512 * i + 4 * t) = perm_lo(gx[i][0], gx[i][2]);
        *(u32*)(smem + CV_X + 4096 + 512 * i + 4 * t) = perm_hi(gx[i][0], gx[i][2]);
      }
      lds_set(smem, 10, w - 2);
    } else {
      u32x4 t[8];
#pragma unroll
      for (int i = 0; i < 8; ++i) pin(pl[i]);
      transpose8(pl, t);
      pin(code_touch);
      STAMP(3);
      {
        char* tb = smem + L_TR + (w & 1) * 8192;
#pragma unroll
        for (int n = 0; n < 8; ++n)
          *(u32x4*)(tb + ((cp >> 1) * 8 + kg) * 256 + (((n & 3) + 8 * (n >> 2) + 4 * (cp & 1)) ^ (cp >> 1)) * 16) = t[n];
        MEMFENCE();
        if (b < 224) {
#pragma unroll
          for (int k = 0; k < 8; ++k) {
            const int row = 4 * k + (lane >> 4), fl = k >> 1;
            const u32x4 o = *(const u32x4*)(tb + row * 256 + ((lane & 15) ^ fl) * 16);
            st16c(R, OFF_WP + (((4 * cg + fl) * 32 + 4 * k8) * 64 + (row & 7) * 32 + 16 * type + (lane & 15)) * 16, o);
          }
        }
      }
      STAMP(4);
      asm volatile("s_waitcnt lgkmcnt(0)" ::: "memory");
      lds_set(smem, 2, w - 4);
      drain_vm();
      st4c(R, only(lane == 0 && b < 224, OFF_FW + (cg * 16 + type * 8 + k8) * 64), tag);
      STAMP(12);
      lds_wait<1>(smem, 10);
      if (w == 6) {
#pragma unroll 1
        for (int spin = 0; spin < SPIN_CAP; ++spin) {
          const u32 f = ld4c(R, OFF_FW + min(((7 * fb) >> 2) * 16 + lane, ((7 * fb + 6) >> 2) * 16 + 15) * 64);
          if (__all(f == tag)) break;
          __builtin_amdgcn_s_sleep(10);
          MEMFENCE();
        }
        drain_vm();
        lds_set(smem, 5, 0);
        STAMP(5);
      } else {
        {
          const int c0 = 64 * xcd + 2 * bi;
          float v = gather_sum<2>(R, OFF_GM + (lane >> 3) * 49152 + 8 * ((2 + ((lane >> 1) & 3)) * DM + c0 + (lane & 1)), 24576, tag);
          v += dpp<0x128>(v);
          v = rs16(v, v);
          v = rb(rs32(v, v));
          if ((lane & 6) == 4) v = rb(1.f + v);
          if (lane < 8 && (lane & 2) == 0) *(float*)(smem + L_EP + 2 * (lane & 4) + 4 * (lane & 1)) = v;
          st8c(R, only(lane >= 2 && lane < 8, OFF_FM + ((lane >> 1) == 2 ? 0 : (lane >> 2) + 1) * 4096 + 8 * (c0 + (lane & 1))),
               u32x2{__float_as_uint(v), tag});
          lds_set(smem, 10, 1);
          STAMP(7);
        }
#pragma unroll
        for (int i = 0; i < 7; ++i) t[i] = *(const u32x4*)(P.wd + (size_t)(7 * b + i) * DM + 8 * lane);
#pragma unroll
        for (int i = 0; i < 7; ++i) pin(t[i]);
        __builtin_amdgcn_s_sleep(VDELAY);
#pragma unroll 1
        for (int spin = 0; spin < SPIN_CAP; ++spin) {
          u32 bad = 0;
#pragma unroll
          for (int i = 0; i < 8; ++i) t[i] = ld16c(R, OFF_FM + i * 1024 + l16);
#pragma unroll
          for (int i = 0; i < 8; ++i) bad |= (t[i][1] ^ tag) | (t[i][3] ^ tag);
          if (__all(bad == 0)) break;
          __builtin_amdgcn_s_sleep(8);
          MEMFENCE();
        }
#pragma unroll
        for (int i = 0; i < 8; ++i) *(u32*)(smem + L_V + 256 * i + 32 * (i >> 2) + 4 * lane) = perm_hi(t[i][0], t[i][2]);
        *(u32x4*)(smem + L_V + 2112 + l16) = u32x4{0, 0, 0, 0};
        lds_set(smem, 5, 2);
        STAMP(5);
      }
    }
    __builtin_amdgcn_s_setprio(0);
    if (w == 7) {
    const char* x1g = ws + OFF_X1 + (size_t)(4 * mt) * 32768;
    auto dma = [&](const char* src, int dst, int n) {
#pragma unroll 4
      for (int i = 0; i < n; ++i) glds16(l16, src + i * 1024, lds0 + dst + i * 1024);
    };
    const u32 fao = OFF_FA + mt * 4096;
    __builtin_amdgcn_s_setprio(3);
    auto x1_flags = [&](int hf) {
      u32 f[4];
#pragma unroll
      for (int i = 0; i < 4; ++i) f[i] = ld4c(R, OFF_FX + hf * 32768 + 4096 * xcd + (lane + 64 * i) * 16);
      return __all(((f[0] ^ tag) | (f[1] ^ tag) | (f[2] ^ tag) | (f[3] ^ tag)) == 0);
    };
    auto x1_poll = [&](int hf) {
#pragma unroll 1
      for (int spin = 0; spin < SPIN_CAP && !x1_flags(hf); ++spin) {
        __builtin_amdgcn_s_sleep(2);
        MEMFENCE();
      }
      drain_vm();
    };
    auto ln2_partial = [&](int hf, int raw) {
      dma(x1g + (2 * hf + (fb >> 3)) * 32768 + (fb & 7) * 4096, raw, 4);
      wait_vm<0>();
      const int g = lane >> 4;
      const char* sb = smem + raw + (8 * (g >> 1) + 4 * (g & 1) + q4) * 64 + 8 * p4;
      const bf16x8 ones = as_frag(u32x4{ONES2, ONES2, ONES2, ONES2});
      bf16x8 f[2][2];
#pragma unroll
      for (int k2 = 0; k2 < 2; ++k2)
#pragma unroll
        for (int c = 0; c < 2; ++c) {
          const char* p = sb + 32 * (c ^ (g & 1)) + k2 * 2048;
          f[c][k2] = tr_frag(p, p + 1024);
        }
#pragma unroll
      for (int c = 0; c < 2; ++c) {
        f32x4 aq = {}, as = {};
#pragma unroll
        for (int k2 = 0; k2 < 2; ++k2) {
          aq = MFMA16(f[c][k2], f[c][k2], aq);
          as = MFMA16(ones, f[c][k2], as);
        }
        const float sq = p4 == 0 ? aq[0] : p4 == 1 ? aq[1] : p4 == 2 ? aq[2] : aq[3];
        st16c(R, only(g == q4, OFF_GS2 + (((mt * 2 + hf) * 8 + (fb & 7)) * 64 + 32 * (fb >> 3) + 16 * c + (lane & 15)) * 16),
              u32x4{__float_as_uint(as[0]), tag, __float_as_uint(sq), tag});
      }
      MEMFENCE();
    };
    auto ln2_gather = [&](int hf) {
#pragma unroll 1
      for (int spin = 0; spin < SPIN_CAP; ++spin) {
        u32x4 g[8];
        u32 bad = 0;
        float s1 = 0.f, s2 = 0.f;
#pragma unroll
        for (int i = 0; i < 8; ++i) g[i] = ld16c(R, OFF_GS2 + (mt * 2 + hf) * 8192 + i * 1024 + l16);
#pragma unroll
        for (int i = 0; i < 8; ++i) {
          bad |= (g[i][1] ^ tag) | (g[i][3] ^ tag);
          s1 += __uint_as_float(g[i][0]);
          s2 += __uint_as_float(g[i][2]);
        }
        const u32 ms = ln_pack(s1, s2) ^ 0x8000u;
        *(u32x4*)(smem + L_SF + (64 * hf + (lane ^ (32 * par))) * 16) = u32x4{perm_lo(ms, ms), ms, perm_hi(ms, ms), 0};
        const float k0 = -2.f * 0.7978845608028654f * 1.4426950408889634f, k1 = k0 * 0.044715f;
        const float sd = bfhi(ms), rs = __builtin_amdgcn_rcpf(sd);
        *(f32x4*)(smem + L_RS + (64 * hf + (lane ^ (32 * par))) * 16) = f32x4{k0 * rs, k1 * rs * rs * rs, sd * sd, 0.f};
        if (__all(bad == 0)) break;
        __builtin_amdgcn_s_sleep(4);
        MEMFENCE();
      }
      lds_set(smem, 7, hf);
    };
    lds_wait<15>(smem, 3);
    __builtin_amdgcn_s_sleep(ADELAY);
    x1_poll(0);
    lds_set(smem, 6, 1);
    STAMP(10);
    ln2_partial(0, CV_P);
    stamp_t(16);
    lds_barrier();
    STAMP(17);
    barrier();
    __builtin_amdgcn_s_sleep(SDELAY);
    ln2_gather(0);
    stamp_t(18);
    barrier();
    stamp_t(19);
    x1_poll(1);
    lds_set(smem, 6, 2);
    stamp_t(9);
    ln2_partial(1, L_SPARE);
    st4c(R, only(b == 0 && lane == 0, OFF_E), tag);
    stamp_t(20);
    lds_barrier();
    stamp_t(21);
    stage_all(4);
    barrier();
    ln2_gather(1);
    stamp_t(8);
#pragma unroll
    for (int k = 0; k < 4; ++k) stage_wd(k);
    wait_vm<8>();
    stamp_t(22);
    lds_barrier();
    stamp_t(23);
    st4c(R, only(lane == 0, fao + fb * 64), tag);
    stage_all(5);
    stage_all(6);
    dma(ws + OFF_X1 + (size_t)(4 * m2 + cp2) * 32768 + nt * 4096, L_RES, 4);
    dma(ws + OFF_X1 + (size_t)(4 * m2 + cp2 + 2) * 32768 + nt * 4096, L_RES + 4096, 4);
    wait_vm<40>();
#pragma unroll
    for (int j = 0; j < 10; ++j) wr[j] = wd_frag(j);
#pragma unroll
    for (int j = 0; j < 10; ++j) pin(wr[j]);
    lds_wait2<15, 7>(smem, 11, 12);
    st4c(R, only(lane == 0, fao + 1024 + fb * 64), tag);
    MEMFENCE();
#pragma unroll
    for (int k = 9; k < 14; ++k) stage_wd(k);
    stamp_t(24);
    barrier();
    stamp_t(25);
    load_flag(std::integral_constant<int, 0>{});
    *(float*)(smem + L_GB + 4 * lane) = gather_sum<1>(R, OFF_FM + 2 * 4096 + 8 * (64 * nt + lane), 0, tag);
    asm volatile("s_waitcnt vmcnt(0) lgkmcnt(0)" ::: "memory");
    __builtin_amdgcn_s_setprio(0);
    down_phase(std::integral_constant<int, 1>{});
  }
  }

  if (w < 7) {
    lds_wait<1>(smem, 5);
    if (w >= 4) {
      lds_wait<3>(smem, 2);
      static_for<0, NPARK>(load_w);
    }
    static_for<NPARK, 32>(load_w);
    if (w >= 4) {
      lds_wait<15>(smem, 8);
#pragma unroll 4
      for (int i = 0; i < 4 * NPARK / 3; ++i) {
        const int f = 3 * i + (w - 4), tw = f & 3, k = f >> 2;
        glds16(l16, ws + OFF_WP + (size_t)(7 * fb + tw) * 32768 + k * 1024, lds0 + L_PARK + (tw * NPARK + k) * 1024);
      }
      drain_vm();
      lds_set(smem, 9, w - 4);
      STAMP(9);
    } else {
      lds_wait<7>(smem, 9);
#pragma unroll
      for (int j = 0; j < NPARK; ++j) wf[j] = *(const u32x4*)(smem + L_PARK + (w * NPARK + j) * 1024 + l16);
      asm volatile("s_waitcnt lgkmcnt(0)" ::: "memory");
      lds_set(smem, 1, w);
    }
    if (w >= 3) {
      lds_wait2<15, 14>(smem, 3, 10);
      x1_piece(w == 3 ? 7 : w);
      STAMP(16);
    }
    auto load_x = [&](int i) {
      const int pc = (i < 10 ? 0 : 64) + min(7 * (i % 10) + w, 63);
      glds16(l16, ws + OFF_XS + (size_t)(4 * mt) * 32768 + ((pc ^ (32 * par)) << 10), lds0 + pc * 1024);
    };
    auto first_x = [&]() {
#pragma unroll
      for (int i = 0; i < 5; ++i) load_x(i);
    };
    wait_vm<2>();
    if (w < 3) drain_vm();
#pragma unroll
    for (int j = 0; j < 32; ++j) pin(wf[j]);
    STAMP(10);
    lds_wait<15>(smem, 1);
    const bool early = __builtin_amdgcn_readfirstlane((int)lds_peek(smem, 6, 1));
    MEMFENCE();
    if (early) first_x();
    u32x4 aux;
    {
      lds_wait<4>(smem, 5);
      const char* vb = smem + L_V + (((lane ^ (lane >> 4)) & 1) ? 2112 : (lane & 2) * 528) + 16 * hh;
      f32x4 d = {};
      bf16x8 va[32];
#pragma unroll
      for (int j = 0; j < 12; ++j) va[j] = *(const bf16x8*)(vb + 32 * j);
      static_for<0, 32>([&](auto J) {
        constexpr int j = decltype(J)::value;
        if constexpr (j + 12 < 32) va[j + 12] = *(const bf16x8*)(vb + 32 * (j + 12));
        d = MFMA16(va[j], as_frag(wf[j]), d);
        pin(d);
        __builtin_amdgcn_sched_barrier(0);
      });
      const float c = blk ? d[1] : d[0], sf = blk ? d[3] : d[2];
      const float c0 = rb(c), c1 = rb(c - c0), b0 = rb(sf), b1 = rb(sf - b0);
      aux = u32x4{pack_bf16(c0, c1), pack_bf16(c - c0 - c1, b0), pack_bf16(b1, sf - b0 - b1), 0};
      if (hh) aux = u32x4{0, 0, 0, 0};
      pin(aux);
    }
    u32 xb[2][2];
#pragma unroll
    for (int i = 0; i < 2; ++i) {
      xb[i][0] = lds0 + 65536 * i + (8 * hh + q4) * 64 + 32 * blk + 8 * p4;
      xb[i][1] = lds0 + 65536 * i + (8 * hh + 4 + q4) * 64 + 32 * (blk ^ 1) + 8 * p4;
      pin(xb[i][0]);
      pin(xb[i][1]);
    }
    auto xfrag = [&](int s) { return tr_frag_at(xb[s >> 6][0] + (s & 63) * 1024, xb[s >> 6][1] + (s & 63) * 1024); };
    const char* actg[2] = {ws + OFF_ACT + ((size_t)(4 * mt + par) * NFT + ft) * 1024,
                           ws + OFF_ACT + ((size_t)(4 * mt + (par ^ 1)) * NFT + ft) * 1024};
    asm volatile("" : "+s"(actg[0]), "+s"(actg[1]));
    u32 sfo = lds0 + L_SF + (lane & 31) * 16;
    pin(sfo);
    u32 pubo = lds0 + L_SYNC + 16 * 11 + 4 * w;
    pin(pubo);
#pragma unroll
    for (int i = 0; i < 6; ++i) pin(wfo[i & 1][i >> 1]);
    if (w >= 3) {
      if (early) wait_vm<5>();
      else wait_vm<0>();
      x1_arrive(1);
    }
    STAMP(6);
    if (!early) {
      lds_wait<2>(smem, 6);
      first_x();
    }
    wait_vm<3>();
    barrier();
    STAMP(17);
    constexpr int XD = 3;
    bf16x8 xf[4];
#pragma unroll
    for (int s = 0; s < XD; ++s) xf[s] = xfrag(s);
    f32x16 acc[3];
    f32x4 gcs[2];
    float ea[4], em[4], prev = 0.f;
    u32x4 o;
    auto epi = [&](auto T, auto U) {
      constexpr int t = decltype(T)::value, u = decltype(U)::value, n = u >> 1;
      const f32x4& gc = gcs[t & 1];
      const f32x16& a = acc[t % 3];
      if constexpr ((u & 1) == 0) {
        if constexpr (n >= 3 && n < 11) {
          float v = em[(n - 3) & 3] * ea[(n - 3) & 3];
          pin(v);
          if constexpr ((n - 3) & 1) o[(n - 3) >> 1] = pack_bf16(prev, v);
          else prev = v;
          if constexpr (n == 10) gstore16(o, l16, actg[t & 1] + (size_t)(t >> 1) * 2 * NFT * 1024);
        }
        if constexpr (n >= 2 && n < 10) { ea[(n - 2) & 3] = __builtin_fmaf(ea[(n - 2) & 3], gc[2], gc[2]); pin(ea[(n - 2) & 3]); }
        if constexpr (n >= 1 && n < 9) { ea[(n - 1) & 3] *= a[n - 1]; pin(ea[(n - 1) & 3]); }
        if constexpr (n < 8) {
          ea[n & 3] = a[n] * a[n];
          em[n & 3] = a[n] * a[8 + n];
          pin(ea[n & 3]);
          pin(em[n & 3]);
        }
      } else {
        if constexpr (n >= 2 && n < 10) { ea[(n - 2) & 3] = __builtin_amdgcn_rcpf(ea[(n - 2) & 3]); pin(ea[(n - 2) & 3]); }
        if constexpr (n >= 1 && n < 9) { ea[(n - 1) & 3] = __builtin_amdgcn_exp2f(ea[(n - 1) & 3]); pin(ea[(n - 1) & 3]); }
        if constexpr (n < 8) { ea[n & 3] = __builtin_fmaf(ea[n & 3], gc[1], gc[0]); pin(ea[n & 3]); }
      }
    };
    bf16x8 sf;
    u32 peek, peek2;
    auto fold_load = [&](int t) {
      sf = *(const __attribute__((address_space(3))) bf16x8*)(uintptr_t)(sfo + t * 512);
      gcs[t & 1] = *(const __attribute__((address_space(3))) f32x4*)(uintptr_t)(sfo + (L_RS - L_SF) + t * 512);
    };
    auto fold = [&](int t) {
      acc[t % 3] = MFMA32(as_frag(aux), sf, acc[t % 3]);
      pin(acc[t % 3]);
    };
    constexpr int PUB = (ES1 + EL + 12 > 100 ? ES1 + EL + 12 : 100);
    static_for<0, 4>([&](auto T) {
      constexpr int t = decltype(T)::value;
      static_for<0, 32>([&](auto KS) {
        constexpr int ks = decltype(KS)::value, s = 32 * t + ks;
        if constexpr (s == 14 - XD) {
          wait_vm<up_after(-1, s, 0)>();
          barrier();
        }
        if constexpr (s == ES0 - 10) peek = lds_peek(smem, 7, 0);
        if constexpr (s == ES0 - 6) {
          if (__builtin_expect(!__builtin_amdgcn_readfirstlane((int)peek), 0)) lds_wait<1>(smem, 7);
          MEMFENCE();
          fold_load(0);
        }
        if constexpr (s == XL - 5) peek2 = lds_peek(smem, 6, 2);
        if constexpr (s == XL - 1) {
          if (__builtin_expect(!__builtin_amdgcn_readfirstlane((int)peek2), 0)) lds_wait<4>(smem, 6);
          MEMFENCE();
        }
        if constexpr (s == 64) {
#pragma unroll
          for (int i = 0; i < XD; ++i) xf[(s + i) & 3] = xfrag(s + i);
        }
        if constexpr (s == 78 - XD) {
          wait_vm<up_after(XL + 4, s, 0)>();
          barrier();
        }
        if constexpr (s == ES2 - 10) peek = lds_peek(smem, 7, 1);
        if constexpr (s == ES2 - 6) {
          if (__builtin_expect(!__builtin_amdgcn_readfirstlane((int)peek), 0)) lds_wait<2>(smem, 7);
          MEMFENCE();
          fold_load(2);
        }
        if constexpr ((t & 1) && ks == 29) fold_load(t);
        if constexpr (s == PUB) {
          wait_vm<up_after(ES1 + EL - 1, s, 1)>();
          *(__attribute__((address_space(3))) volatile u32*)(uintptr_t)pubo = 1u;
          MEMFENCE();
        }
        if constexpr (s + XD < 128 && !(s < 64 && s + XD >= 64)) xf[(s + XD) & 3] = xfrag(s + XD);
        if constexpr (ks == 0) acc[t % 3] = MFMA32(as_frag(wf[0]), xf[s & 3], f32x16{});
        else acc[t % 3] = MFMA32(as_frag(wf[ks]), xf[s & 3], acc[t % 3]);
        pin(acc[t % 3]);
        if constexpr (s == ES0 - 3) fold(0);
        if constexpr (s == ES2 - 3) fold(2);
        if constexpr ((t & 1) && ks == 31) fold(t);
        if constexpr (s >= ES0 && s < ES0 + EL) epi(std::integral_constant<int, 0>{}, std::integral_constant<int, s - ES0>{});
        if constexpr (s >= ES1 && s < ES1 + EL) epi(std::integral_constant<int, 1>{}, std::integral_constant<int, s - ES1>{});
        if constexpr (s >= ES2 && s < ES2 + EL) epi(std::integral_constant<int, 2>{}, std::integral_constant<int, s - ES2>{});
        if constexpr (s == 102) wait_vm<up_after(82, s, 0)>();
        if constexpr (t == 3 && ks < 10) wr[ks < 4 ? ks : ks < 6 ? ks + 4 : ks - 2] = wd_frag(ks < 4 ? ks : ks < 6 ? ks + 4 : ks - 2);
        if constexpr (t == 3 && ks == 11) {
#pragma unroll
          for (int j = 0; j < 10; ++j) pin(wr[j]);
        }
        if constexpr (up_xpiece(s) >= 0) load_x(up_xpiece(s));
        if constexpr (up_stage(s) >= 0) stage_wd(up_stage(s));
        __builtin_amdgcn_sched_barrier(0);
      });
      stamp_t(18 + 2 * t);
      if constexpr (t == 0) wait_vm<up_after(9, 32, 0)>();
      if constexpr (t == 1) wait_vm<up_after(XL + 1, 64, 0)>();
      if constexpr (t == 2) wait_vm<up_after(ES0 + EL - 1, 96, 1)>();
      lds_barrier();
      stamp_t(19 + 2 * t);
    });
    load_flag(std::integral_constant<int, 0>{});
#pragma unroll
    for (int i = 0; i < 8; i += 2) {
      float v[2];
#pragma unroll
      for (int e = 0; e < 2; ++e) {
        const f32x4& gc = gcs[1];
        const float a = acc[0][i + e];
        const float ex = __builtin_amdgcn_exp2f(a * __builtin_fmaf(a * a, gc[1], gc[0]));
        v[e] = a * acc[0][8 + i + e] * __builtin_amdgcn_rcpf(__builtin_fmaf(ex, gc[2], gc[2]));
      }
      o[i >> 1] = pack_bf16(v[0], v[1]);
    }
    gstore16(o, l16, actg[1] + (size_t)2 * NFT * 1024);
    STAMP(26);
    wait_vm<up_after(ES2 + EL - 1, 128, 1) + 2>();
    down_phase(std::integral_constant<int, 0>{});
  }
  DIAG_END(ws);
}

extern "C" void kernel_launch(void* const* d_in, const int* in_sizes, int n_in, void* d_out, int out_size, void* d_ws,
                              size_t ws_size, hipStream_t stream) {
  Params p;
  p.x = (const u16*)d_in[0];
  p.la = (const u16*)d_in[1];
  p.ai = (const u16*)d_in[2];
  p.cr = (const u16*)d_in[3];
  p.ci = (const u16*)d_in[4];
  p.ldt = (const u16*)d_in[5];
  p.dsk = (const u16*)d_in[6];
  p.wg = (const u16*)d_in[8];
  p.wu = (const u16*)d_in[9];
  p.wd = (const u16*)d_in[10];
  p.c = (const u16*)d_in[11];
  p.wm = (const u16*)d_in[12];
  p.out = (u16*)d_out;
  p.ws = (char*)d_ws;
  (void)hipFuncSetAttribute((const void*)fused_block, hipFuncAttributeMaxDynamicSharedMemorySize, SMEM);
  fused_block<<<256, 512, SMEM, stream>>>(p);
}
```

```cpp
#include <hip/hip_runtime.h>
#include <hip/hip_bf16.h>
#include <stdint.h>
#include <type_traits>

typedef unsigned short u16;
typedef unsigned int u32;
typedef unsigned long long u64;
typedef __attribute__((ext_vector_type(8))) __bf16 bf16x8;
typedef __attribute__((ext_vector_type(4))) __bf16 bf16x4;
typedef __attribute__((ext_vector_type(2))) __bf16 bf16x2;
typedef __attribute__((ext_vector_type(2))) _Float16 f16x2;
typedef __attribute__((ext_vector_type(8))) _Float16 f16x8;
typedef __attribute__((ext_vector_type(2))) float f32x2;
typedef __attribute__((ext_vector_type(4))) float f32x4;
typedef __attribute__((ext_vector_type(16))) float f32x16;
typedef __attribute__((ext_vector_type(4))) u32 u32x4;
typedef __attribute__((ext_vector_type(2))) u32 u32x2;
typedef __attribute__((address_space(3))) bf16x4* lds_bf16x4_p;
typedef __attribute__((address_space(3))) char* lds_char_p;
typedef __attribute__((address_space(3))) u32* lds_u32_p;
typedef __amdgpu_buffer_rsrc_t rsrc_t;

#define DEVI __device__ __forceinline__

constexpr int SL = 2048, DM = 512, NS = 128, DF = 1792;
constexpr int NFT = DF / 16;
constexpr int SPIN_CAP = 1 << 15;
#define GDELAY 2
#define ESTART 42
#define SDELAY 4
#define SPRIO 0
#define AWIN 7
#define LO2 7
#define LO3 14
#define VDELAY 16
#define XLATE 46
#define ADELAY 8
#define ESTART2 104
#define WDELAY 6
constexpr int CODE_BYTES = 44 * 1024;

constexpr u32 OFF_E = 0;
constexpr u32 OFF_GST = 4096;
constexpr u32 OFF_FX = 524288;
constexpr u32 OFF_FW = 81920;
constexpr u32 OFF_FA = 131072;
constexpr u32 OFF_FM = 114688;
constexpr u32 OFF_GS2 = 196608;
constexpr u32 OFF_GM = 622592;
static_assert(OFF_GM + 16 * 24576 <= (1u << 20), "control block");
constexpr u32 OFF_XG = 1u << 20;
constexpr u32 OFF_WP = OFF_XG + (4u << 20);
constexpr u32 OFF_X1 = OFF_WP + 112 * 32768;
constexpr u32 OFF_XS = OFF_X1 + (2u << 20);
constexpr u32 OFF_ACT = OFF_XS + (2u << 20);
constexpr u32 WS_USED = OFF_ACT + (7u << 20);

constexpr int KG_ROWB = 272;
constexpr int KG_WAVEB = 57 * KG_ROWB;
constexpr int L_KC = 4 * KG_WAVEB;
constexpr int CV_KCOPY = 4160;
constexpr int L_PARK = 0;
constexpr int NPARK = 21;
constexpr int CV_K = 4 * NPARK * 1024;
constexpr int CV_H = CV_K + 4 * CV_KCOPY;
constexpr int CV_HB = 2288 * 2;
constexpr int CV_X = CV_H + 2 * CV_HB;
constexpr int CV_P = CV_X + 8192;
constexpr int L_TR = CV_P;
static_assert(L_KC + 4 * 656 * 4 <= CV_K, "front LDS map");
constexpr int L_RES = 131072;
constexpr int L_SPARE = L_RES + 8192;
constexpr int L_SF = L_SPARE + 16384;
static_assert(CV_P + 32768 + 16 <= L_SF, "front LDS map");
constexpr int L_RS = L_SF + 2048;
constexpr int L_V = L_SF - 2112;
static_assert(CV_P + 32768 + 16 <= L_V, "front LDS map");
constexpr int L_GB = L_RS + 2048;
constexpr int L_SYNC = L_GB + 256;
constexpr int L_EP = L_SYNC + 256;
constexpr int SMEM = L_EP + 32;

#define VPAD
#define DIAG_DECL
#define STAMP(i)
#define RSTAMP(i)
#define DIAG_END(wsp)

template <int I, int N, class F>
DEVI void static_for(F&& f) {
  if constexpr (I < N) {
    f(std::integral_constant<int, I>{});
    static_for<I + 1, N>(f);
  }
}

DEVI float bflo(u32 v) { return __uint_as_float(v << 16); }
DEVI float bfhi(u32 v) { return __uint_as_float(v & 0xffff0000u); }
DEVI f32x2 unpack2(u32 v) { return f32x2{bflo(v), bfhi(v)}; }
DEVI float b2f(u16 v) { return __uint_as_float(((u32)v) << 16); }
DEVI u32 pack_bf16(float a, float b) {
  f32x2 v = {a, b};
  return __builtin_bit_cast(u32, __builtin_convertvector(v, bf16x2));
}
DEVI float rb(float a) { return bflo(pack_bf16(a, a)); }
DEVI u32 pack_f16(f32x2 v) { return __builtin_bit_cast(u32, __builtin_convertvector(v, f16x2)); }
DEVI u32 perm_lo(u32 a, u32 b) { return __builtin_amdgcn_perm(b, a, 0x05040100u); }
DEVI u32 perm_hi(u32 a, u32 b) { return __builtin_amdgcn_perm(b, a, 0x07060302u); }
DEVI float dot2(u32 a, u32 b, float c) {
  return __builtin_amdgcn_fdot2_f32_bf16(__builtin_bit_cast(bf16x2, a), __builtin_bit_cast(bf16x2, b), c, false);
}
constexpr u32 ONES2 = 0x3f803f80u;

template <int CTRL>
DEVI float dpp(float v) { return __int_as_float(__builtin_amdgcn_update_dpp(0, __float_as_int(v), CTRL, 0xf, 0xf, true)); }
DEVI float row_sum(float v) {
  v += dpp<0xB1>(v); v += dpp<0x4E>(v); v += dpp<0x141>(v); v += dpp<0x140>(v);
  return v;
}
DEVI float rs32(float a, float b) {
  auto r = __builtin_amdgcn_permlane32_swap(__float_as_uint(a), __float_as_uint(b), false, false);
  return __uint_as_float(r[0]) + __uint_as_float(r[1]);
}
DEVI float rs16(float a, float b) {
  auto r = __builtin_amdgcn_permlane16_swap(__float_as_uint(a), __float_as_uint(b), false, false);
  return __uint_as_float(r[0]) + __uint_as_float(r[1]);
}
DEVI float wave_max(float v) {
  v = fmaxf(v, dpp<0xB1>(v)); v = fmaxf(v, dpp<0x4E>(v)); v = fmaxf(v, dpp<0x141>(v)); v = fmaxf(v, dpp<0x140>(v));
  auto r = __builtin_amdgcn_permlane16_swap(__float_as_uint(v), __float_as_uint(v), false, false);
  v = fmaxf(__uint_as_float(r[0]), __uint_as_float(r[1]));
  auto s = __builtin_amdgcn_permlane32_swap(__float_as_uint(v), __float_as_uint(v), false, false);
  return fmaxf(__uint_as_float(s[0]), __uint_as_float(s[1]));
}

DEVI f32x2 cmul(f32x2 a, f32x2 b) {
  const f32x2 t = a.yy * f32x2{-b.y, b.x};
  return __builtin_elementwise_fma(a.xx, b, t);
}
DEVI f32x2 cexpk(float zr, float zi, float k) {
  const float mag = __builtin_amdgcn_exp2f(zr * (k * 1.4426950408889634f));
  const float rev = __builtin_amdgcn_fractf(zi * (k * 0.15915494309189535f));
  return f32x2{mag * __builtin_amdgcn_cosf(rev), mag * __builtin_amdgcn_sinf(rev)};
}

DEVI u32x4 ld16c(rsrc_t r, u32 off) { return __builtin_amdgcn_raw_buffer_load_b128(r, off, 0, 16); }
DEVI u32x2 ld8c(rsrc_t r, u32 off) { return __builtin_amdgcn_raw_buffer_load_b64(r, off, 0, 16); }
DEVI u32 ld4c(rsrc_t r, u32 off) { return __builtin_amdgcn_raw_buffer_load_b32(r, off, 0, 16); }
DEVI void st16c(rsrc_t r, u32 off, u32x4 v) { __builtin_amdgcn_raw_buffer_store_b128(v, r, off, 0, 16); }
DEVI void st8c(rsrc_t r, u32 off, u32x2 v) { __builtin_amdgcn_raw_buffer_store_b64(v, r, off, 0, 16); }
DEVI void st4c(rsrc_t r, u32 off, u32 v) { __builtin_amdgcn_raw_buffer_store_b32(v, r, off, 0, 16); }
DEVI u32 only(bool c, u32 off) { return c ? off : 0x80000000u; }
#define MEMFENCE() asm volatile("" ::: "memory")
DEVI void drain_vm() {
  __builtin_amdgcn_s_waitcnt(0x0F70);
  asm volatile("s_waitcnt vmcnt(0)" ::: "memory");
}

template <int N>
DEVI float gather_sum(rsrc_t r, u32 off, u32 stride, u32 tag) {
  float v = 0.f;
#pragma unroll 1
  for (int spin = 0; spin < SPIN_CAP; ++spin) {
    u32x2 g[N];
#pragma unroll
    for (int i = 0; i < N; ++i) g[i] = ld8c(r, off + i * stride);
    u32 bad = 0;
    v = 0.f;
#pragma unroll
    for (int i = 0; i < N; ++i) {
      v += __uint_as_float(g[i][0]);
      bad |= g[i][1] ^ tag;
    }
    if (__all(bad == 0)) break;
    __builtin_amdgcn_s_sleep(2);
    MEMFENCE();
  }
  MEMFENCE();
  return v;
}
DEVI float sum_hi4(float v) {
  v += dpp<0x124>(v);
  v += dpp<0x128>(v);
  v = rs16(v, v);
  return rs32(v, v);
}
DEVI u32 silu2(u32 c) {
  const float a = bflo(c), b = bfhi(c);
  return pack_bf16(a * __builtin_amdgcn_rcpf(1.f + __expf(-a)), b * __builtin_amdgcn_rcpf(1.f + __expf(-b)));
}
DEVI float rdlane(float v, int l) { return __int_as_float(__builtin_amdgcn_readlane(__float_as_int(v), l)); }
DEVI void poll_flag(rsrc_t r, u32 off, u32 tag) {
#pragma unroll 1
  for (int spin = 0; spin < SPIN_CAP; ++spin) {
    const u32 f = ld4c(r, off);
    if (__all(f == tag)) break;
    __builtin_amdgcn_s_sleep(1);
    MEMFENCE();
  }
  MEMFENCE();
}

DEVI void lds_set(char* smem, int grp, int word) {
  MEMFENCE();
  *(__attribute__((address_space(3))) volatile u32*)(smem + L_SYNC + 16 * grp + 4 * word) = 1u;
  MEMFENCE();
}
DEVI u32 lds_peek(char* smem, int grp, int word) {
  return *(__attribute__((address_space(3))) volatile u32*)(smem + L_SYNC + 16 * grp + 4 * word);
}
DEVI u32 lds_arrive(char* smem, int grp) {
  u32 old;
  const u32 addr = (u32)(uintptr_t)(lds_char_p)(smem + L_SYNC + 16 * grp);
  asm volatile("s_mov_b64 exec, 1\n\tds_add_rtn_u32 %0, %1, %2\n\ts_mov_b64 exec, -1\n\ts_waitcnt lgkmcnt(0)"
               : "=&v"(old) : "v"(addr), "v"(1u) : "memory");
  return (u32)__builtin_amdgcn_readfirstlane((int)old);
}
template <int MASK>
DEVI void lds_wait(char* smem, int grp) {
  typedef __attribute__((address_space(3))) volatile u32x4* lds_u32x4_p;
#pragma unroll 1
  for (int spin = 0; spin < (1 << 22); ++spin) {
    MEMFENCE();
    const u32x4 v = *(lds_u32x4_p)(smem + L_SYNC + 16 * grp);
    const u32 a = ((MASK & 1) ? v[0] : 1u) & ((MASK & 2) ? v[1] : 1u) & ((MASK & 4) ? v[2] : 1u) & ((MASK & 8) ? v[3] : 1u);
    if (__builtin_amdgcn_readfirstlane((int)a)) break;
    __builtin_amdgcn_s_sleep(1);
  }
  MEMFENCE();
}

template <int MA, int MB>
DEVI void lds_wait2(char* smem, int ga, int gb) {
  typedef __attribute__((address_space(3))) volatile u32x4* lds_u32x4_p;
#pragma unroll 1
  for (int spin = 0; spin < (1 << 22); ++spin) {
    MEMFENCE();
    const u32x4 v = *(lds_u32x4_p)(smem + L_SYNC + 16 * ga), u = *(lds_u32x4_p)(smem + L_SYNC + 16 * gb);
    const u32 a = ((MA & 1) ? v[0] : 1u) & ((MA & 2) ? v[1] : 1u) & ((MA & 4) ? v[2] : 1u) & ((MA & 8) ? v[3] : 1u) &
                  ((MB & 1) ? u[0] : 1u) & ((MB & 2) ? u[1] : 1u) & ((MB & 4) ? u[2] : 1u) & ((MB & 8) ? u[3] : 1u);
    if (__builtin_amdgcn_readfirstlane((int)a)) break;
  }
  MEMFENCE();
}

DEVI void glds16(u32 voff, const void* sbase, u32 ldsaddr) {
  asm volatile(VPAD "s_mov_b32 m0, %2\n\ts_nop 0\n\tglobal_load_lds_dwordx4 %0, %1 sc1" ::"v"(voff), "s"(sbase), "s"(ldsaddr) : "memory");
}
DEVI void glds16p2(u32 voff0, u32 voff1, const void* sbase, u32 ldsaddr) {
  asm volatile(VPAD "s_mov_b32 m0, %3\n\ts_nop 0\n\tglobal_load_lds_dwordx4 %0, %2\n\tglobal_load_lds_dwordx4 %1, %2 offset:1024"
               ::"v"(voff0), "v"(voff1), "s"(sbase), "s"(ldsaddr) : "memory");
}
template <int N>
DEVI void wait_vm() { asm volatile("s_waitcnt vmcnt(%0)" ::"n"(N) : "memory"); }
template <int N, class T>
DEVI void wait_vm_reg(T& r) { asm volatile("s_waitcnt vmcnt(%1)" : "+v"(r) : "n"(N)); }
template <class T>
DEVI void pin(T& r) { asm volatile("" : "+v"(r)); }
template <int OFF>
DEVI void gload16(u32x4& r, u32 voff, const void* sbase) {
  asm volatile(VPAD "global_load_dwordx4 %0, %1, %2 offset:%3 sc1" : "=v"(r) : "v"(voff), "s"(sbase), "n"(OFF) : "memory");
}
template <int OFF>
DEVI void gload4(u32& r, u32 voff, const void* sbase) {
  asm volatile(VPAD "global_load_dword %0, %1, %2 offset:%3 sc1" : "=v"(r) : "v"(voff), "s"(sbase), "n"(OFF) : "memory");
}
DEVI void gstore16(u32x4 v, u32 voff, const void* sbase) {
  asm volatile(VPAD "global_store_dwordx4 %0, %1, %2 sc1\n\ts_nop 1" ::"v"(voff), "v"(v), "s"(sbase) : "memory");
}
DEVI void barrier() {
  MEMFENCE();
  __builtin_amdgcn_s_barrier();
  MEMFENCE();
}
DEVI void lds_barrier() {
  asm volatile("s_waitcnt lgkmcnt(0)" ::: "memory");
  __builtin_amdgcn_s_barrier();
  MEMFENCE();
}
DEVI bf16x4 tr_read(const char* p) { return __builtin_amdgcn_ds_read_tr16_b64_v4bf16((lds_bf16x4_p)(p)); }
DEVI bf16x8 tr_frag(const char* p0, const char* p1) {
  return __builtin_shufflevector(tr_read(p0), tr_read(p1), 0, 1, 2, 3, 4, 5, 6, 7);
}
DEVI bf16x8 tr_frag_at(u32 a0, u32 a1) {
  return __builtin_shufflevector(__builtin_amdgcn_ds_read_tr16_b64_v4bf16((lds_bf16x4_p)(uintptr_t)a0),
                                 __builtin_amdgcn_ds_read_tr16_b64_v4bf16((lds_bf16x4_p)(uintptr_t)a1), 0, 1, 2, 3, 4, 5, 6, 7);
}
DEVI bf16x8 as_frag(u32x4 v) { return __builtin_bit_cast(bf16x8, v); }
#define MFMA32(a, b, c) __builtin_amdgcn_mfma_f32_32x32x16_bf16(a, b, c, 0, 0, 0)
#define MFMA16(a, b, c) __builtin_amdgcn_mfma_f32_16x16x32_bf16(a, b, c, 0, 0, 0)

DEVI void transpose8(const u32x4 (&r)[8], u32x4 (&o)[8]) {
#pragma unroll
  for (int k = 0; k < 8; ++k)
#pragma unroll
    for (int i = 0; i < 4; ++i)
      o[k][i] = (k & 1) ? perm_hi(r[2 * i][k >> 1], r[2 * i + 1][k >> 1]) : perm_lo(r[2 * i][k >> 1], r[2 * i + 1][k >> 1]);
}
DEVI u32 ln_pack(float s, float q) {
  const float mean = s * (1.f / DM);
  const float var = fmaxf(q * (1.f / DM) - mean * mean, 0.f);
  return pack_bf16(mean, __builtin_amdgcn_sqrtf(rb(rb(var) + 1e-5f)));
}

template <int NP>
DEVI void mod_part(const u32x4 (&wv)[8], const u32 (&sl)[NP], rsrc_t r, u32 dst, u32 tag, int lane) {
  float acc[8] = {};
#pragma unroll
  for (int j = 0; j < NP; ++j)
#pragma unroll
    for (int k = 0; k < 4; ++k) {
      acc[2 * k] = dot2(perm_lo(wv[2 * j][k], wv[2 * j + 1][k]), sl[j], acc[2 * k]);
      acc[2 * k + 1] = dot2(perm_hi(wv[2 * j][k], wv[2 * j + 1][k]), sl[j], acc[2 * k + 1]);
    }
#pragma unroll
  for (int i = 0; i < 4; ++i) acc[i] = rs32(acc[i], acc[i + 4]);
#pragma unroll
  for (int i = 0; i < 2; ++i) {
    acc[i] = rs16(acc[i], acc[i + 2]);
    acc[i] += dpp<0x128>(acc[i]);
  }
  st16c(r, only((lane & 8) == 0, dst + 8 * (8 * (lane & 7) + 2 * (lane >> 4))), u32x4{__float_as_uint(acc[0]), tag, __float_as_uint(acc[1]), tag});
}

constexpr int ES0 = ESTART, EL = 21, ES1 = ES0 + EL > 66 ? ES0 + EL : 66, ES2 = ESTART2;
static_assert(ES2 + EL <= 125 && ES2 >= 99 && ES2 >= ES1 + EL && ES0 - 3 < 61 && ES0 + EL - 1 > 56, "activation streams");
constexpr int XL = XLATE;
constexpr int up_xpiece(int s) { return s < 10 && (s & 1) ? 5 + s / 2 : s >= XL && s < XL + 10 ? 10 + (s - XL) : -1; }
constexpr int up_stage(int s) { return s == 33 ? 0 : s == 35 ? 1 : s == 80 ? 2 : s == 82 ? 3 : s >= 108 && (s & 3) == 0 ? 9 + (s - 108) / 4 : -1; }
constexpr bool up_store(int s) { return s == ES0 + EL - 1 || s == ES1 + EL - 1 || s == ES2 + EL - 1; }
constexpr int up_after(int a, int b, int with) {
  int n = with ? (up_xpiece(a) >= 0) + 2 * (up_stage(a) >= 0) : 0;
  for (int s = a + 1; s < b; ++s) n += up_store(s) + (up_xpiece(s) >= 0) + 2 * (up_stage(s) >= 0);
  return n;
}

static_assert(AWIN >= 7 && AWIN <= 14 && LO2 >= 3 && 13 - AWIN < LO2 && 20 - AWIN < LO3 && LO3 - 3 > LO2, "down schedule");
constexpr int dn_iss(int n) {
  const int lo = n < 14 ? -1 : n < 21 ? LO2 : LO3;
  return n - AWIN > lo ? n - AWIN : lo;
}
constexpr int dn_issued(int j) {
  int n = 0;
  while (n < 28 && dn_iss(n) < j) ++n;
  return n;
}

struct Params {
  const u16 *x, *la, *ai, *cr, *ci, *ldt, *dsk, *wg, *wu, *wd, *c, *wm;
  u16* out;
  char* ws;
};

__global__ __launch_bounds__(512, 2) void fused_block(Params P) {
  extern __shared__ __attribute__((aligned(16))) char smem[];
  DIAG_DECL
  const int tid = threadIdx.x, lane = tid & 63;
  const int w = __builtin_amdgcn_readfirstlane(tid >> 6);
  const int b = blockIdx.x, xcd = b & 7, bi = b >> 3;
  char* const ws = P.ws;
  const rsrc_t R = __builtin_amdgcn_make_buffer_rsrc(ws, 0, WS_USED, 0x00020000);
  const int mt = 4 * (xcd & 3) + (bi & 3), fb = 2 * (bi >> 2) + (xcd >> 2);
  const int par = fb & 1;
  const int m2 = 2 * xcd + (bi & 1), nt = bi >> 2, cp2 = (bi >> 1) & 1;
  const int ft = 7 * fb + w;
  const u32 l16 = lane * 16;
  const char* wsrc = ws + OFF_WP + (size_t)ft * 32768;
  u32x4 wf[32];
  u32 code_touch = 0;
  {
    const __attribute__((address_space(1))) char* pc;
    asm volatile("s_getpc_b64 %0" : "=s"(pc));
    if (w >= 6)
      code_touch = *(const __attribute__((address_space(1))) u32*)(pc + min((2 * (bi % 3) + (w & 1)) * 8192 + lane * 128, CODE_BYTES));
  }
  if (tid < 64) *(u32*)(smem + L_SYNC + 4 * tid) = 0;
  STAMP(32);
  u32x4 pl[8];
  u32 ps[8];
  if (w < 4) {
    const int d = 64 * xcd + 2 * bi + (w >> 1), n = 64 * (w & 1) + lane;
    ps[0] = P.la[d * NS + n];
    ps[1] = P.ai[d * NS + n];
    ps[2] = P.cr[d * NS + n];
    ps[3] = P.ci[d * NS + n];
    ps[4] = P.cr[d * NS + (n ^ 64)];
    ps[5] = P.ci[d * NS + (n ^ 64)];
    ps[6] = P.ldt[d];
    ps[7] = P.dsk[(d & ~1) + ((lane >> 2) & 1)];
  } else if (w == 4) {
#pragma unroll
    for (int i = 0; i < 8; ++i) pl[i] = *(const u32x4*)(P.x + (size_t)(8 * b + 2 * (lane & 3) + (i & 1)) * DM + 8 * (lane >> 2) + 128 * (i >> 1));
  } else if (w == 5) {
    const int ra = 32 * (b & 15) + 4 * (lane >> 3);
    const u32x2 cv = *(const u32x2*)(P.c + ra);
    ps[0] = cv[0];
    ps[1] = cv[1];
#pragma unroll
    for (int i = 0; i < 4; ++i) pl[i] = *(const u32x4*)(P.wm + (size_t)(ra + i) * 3072 + 64 * (b >> 4) + 8 * (lane & 7));
  } else {
    const int un = min(b, 223);
    const u16* src = (((un >> 2) & 1) ? P.wu : P.wg) + (size_t)(64 * (2 * (un & 3) + (w & 1)) + 8 * (lane >> 3)) * DF + 64 * (un >> 3) + 8 * (lane & 7);
    __builtin_amdgcn_s_sleep(WDELAY);
#pragma unroll
    for (int i = 0; i < 8; ++i) pl[i] = *(const u32x4*)(src + i * DF);
  }
  u32 tag;
  asm volatile("s_load_dword %0, %1, 0x0\n\ts_waitcnt lgkmcnt(0)" : "=s"(tag) : "s"(ws + OFF_E) : "memory");
  tag += 0x9E3779B9u;
  STAMP(34);
  barrier();
  STAMP(35);
  auto load_w = [&](auto J) {
    constexpr int j = decltype(J)::value;
    gload16<(j & 3) * 1024>(wf[j], l16, wsrc + (j >> 2) * 4096);
  };

  const u32 lds0 = (u32)(uintptr_t)(lds_char_p)smem;
  const char* ag[2] = {ws + OFF_ACT + ((size_t)(4 * m2 + cp2) * NFT + 14 * w + 7 * cp2) * 1024,
                       ws + OFF_ACT + ((size_t)(4 * m2 + cp2) * NFT + 14 * w + 7 * (cp2 ^ 1)) * 1024};
  const char* wdg[2] = {(const char*)(P.wd + (size_t)(224 * w + 112 * cp2) * DM + 64 * nt),
                        (const char*)(P.wd + (size_t)(224 * w + 112 * (cp2 ^ 1)) * DM + 64 * nt)};
  const u32 wdo = (lane >> 3) * 1024 + 16 * ((lane & 7) ^ ((lane >> 2) & 4));
  auto slot_lds = [&](int k, int wv) {
    const int sl = k < 9 ? k : k - 9;
    return sl == 4 ? L_SPARE + wv * 2048 : ((sl < 4 ? sl : sl - 1) >> 1) * 32768 + wv * 4096 + ((sl < 4 ? sl : sl - 1) & 1) * 2048;
  };
  auto stage_wd = [&](int k) { glds16p2(wdo, wdo + 7168, wdg[k / 7] + (k % 7) * 16384, lds0 + slot_lds(k, w)); };
  auto stage_all = [&](int k) {
    wait_vm<32>();
#pragma unroll 2
    for (int v = 0; v < 8; ++v) glds16p2(wdo, wdo + 7168, wdg[k / 7] + (k % 7) * 16384 + (v - 7) * (224 * 1024), lds0 + slot_lds(k, v));
  };
  auto stamp_t = [&](int i) { STAMP(i); };
  const int q4 = (lane & 15) >> 2, p4 = lane & 3, hh = lane >> 5, blk = (lane >> 4) & 1;
  u32 wfo[2][3];
#pragma unroll
  for (int i = 0; i < 2; ++i) {
    const u32 o = lds0 + (8 * hh + q4) * 128 + 64 * (i ^ (q4 >> 1)) + 32 * blk + 8 * p4;
    wfo[i][0] = o + w * 4096;
    wfo[i][1] = o + w * 4096 + 65536;
    wfo[i][2] = o + w * 2048 + L_SPARE;
  }
  auto wd_frag = [&](int j) {
    const int so = slot_lds(j >> 1, 0), r = so >= L_SPARE ? 2 : so >> 16;
    const u32 a = wfo[j & 1][r] + (r == 2 ? 0 : so & 65535);
    return tr_frag_at(a, a + 512);
  };
  bf16x8 wr[10];
  u32x4 av[28];
  auto load_a = [&](auto J) {
    constexpr int j = decltype(J)::value, g = j / 7, i = j % 7;
    gload16<(i & 3) * 1024>(av[j], l16, ag[g & 1] + (size_t)(g >> 1) * 2 * NFT * 1024 + (i >> 2) * 4096);
  };
  const char* fag[2] = {ws + OFF_FA + m2 * 4096 + (2 * w + cp2) * 64, ws + OFF_FA + m2 * 4096 + (2 * w + (cp2 ^ 1)) * 64};
  u32 af[4];
  auto load_flag = [&](auto G) { gload4<decltype(G)::value * 1024>(af[decltype(G)::value], 0u, fag[decltype(G)::value & 1]); };
  auto check_flag = [&](auto G, auto YOUNGER) {
    constexpr int g = decltype(G)::value;
    wait_vm_reg<decltype(YOUNGER)::value>(af[g]);
    if (__builtin_expect((u32)__builtin_amdgcn_readfirstlane((int)af[g]) != tag, 0))
      poll_flag(R, OFF_FA + m2 * 4096 + g * 1024 + (2 * w + (cp2 ^ (g & 1))) * 64, tag);
    MEMFENCE();
  };

  auto down_phase = [&](auto) {
    constexpr int FD = 3;
    bf16x8 wd[28];
    auto put_partial = [&](int qd, const f32x16& a) {
#pragma unroll
      for (int g = 0; g < 4; ++g)
        *(f32x4*)(smem + qd * 32768 + w * 4096 + g * 1024 + l16) = f32x4{a[4 * g], a[4 * g + 1], a[4 * g + 2], a[4 * g + 3]};
    };
    const int cl = 8 * w + 4 * hh;
    auto finish = [&](int qj) {
      f32x4 s = {0.f, 0.f, 0.f, 0.f};
#pragma unroll
      for (int v = 0; v < 8; ++v) s += *(const f32x4*)(smem + (2 * (w >> 2) + qj) * 32768 + v * 4096 + (w & 3) * 1024 + l16);
      const f32x4 gb = *(const f32x4*)(smem + L_GB + 4 * cl);
      const u32x2 xr = __builtin_bit_cast(u32x2, tr_read(smem + L_RES + qj * 4096 + (cl + q4) * 64 + 32 * (blk ^ hh) + 8 * p4));
      u32x2 o;
#pragma unroll
      for (int e = 0; e < 2; ++e) o[e] = pack_bf16(bflo(xr[e]) + gb[2 * e] * s[2 * e], bfhi(xr[e]) + gb[2 * e + 1] * s[2 * e + 1]);
      return o;
    };
    f32x16 acc[4] = {};
    barrier();
    st4c(R, only(w == 7 && lane == 0, OFF_FA + mt * 4096 + 2 * 1024 + fb * 64), tag);
    MEMFENCE();
    stage_wd(7);
    stage_wd(8);
    check_flag(std::integral_constant<int, 0>{}, std::integral_constant<int, 6>{});
    static_for<0, 2>(load_a);
    load_flag(std::integral_constant<int, 1>{});
    wait_vm<8>();
    stamp_t(27);
    barrier();
    st4c(R, only(w == 7 && lane == 0, OFF_FA + mt * 4096 + 3 * 1024 + fb * 64), tag);
    MEMFENCE();
    static_for<2, 7>(load_a);
    check_flag(std::integral_constant<int, 1>{}, std::integral_constant<int, 6>{});
    static_for<7, dn_issued(0)>(load_a);
#pragma unroll
    for (int j = 0; j < 14; ++j) wd[j] = j < 10 ? wr[j] : wd_frag(j);
    stamp_t(28);
    u16* op = P.out + (size_t)(64 * m2 + 32 * cp2 + (lane & 31)) * DM + 64 * nt + cl;
    u32x2 o0;
    static_for<0, 28>([&](auto J) {
      constexpr int j = decltype(J)::value, g = j / 7, k = j % 14;
      wait_vm_reg<dn_issued(j) - 1 - j>(av[j]);
      acc[g >> 1] = MFMA32(wd[2 * k], as_frag(av[j]), acc[g >> 1]);
      acc[2 + (g >> 1)] = MFMA32(wd[2 * k + 1], as_frag(av[j]), acc[2 + (g >> 1)]);
      pin(acc[g >> 1]);
      pin(acc[2 + (g >> 1)]);
      if constexpr (j == dn_iss(14)) check_flag(std::integral_constant<int, 2>{}, std::integral_constant<int, dn_issued(j) - dn_issued(j - FD)>{});
      if constexpr (j == dn_iss(21)) check_flag(std::integral_constant<int, 3>{}, std::integral_constant<int, dn_issued(j) - dn_issued(j - FD)>{});
      if constexpr (j == dn_iss(21)) stamp_t(29);
      if constexpr (j + FD == dn_iss(14)) load_flag(std::integral_constant<int, 2>{});
      if constexpr (j + FD == dn_iss(21)) load_flag(std::integral_constant<int, 3>{});
      static_for<dn_issued(j), dn_issued(j + 1)>(load_a);
      if constexpr (j < 7) {
        wd[14 + 2 * j] = wd_frag(14 + 2 * j);
        wd[15 + 2 * j] = wd_frag(15 + 2 * j);
      }
      if constexpr (j == 15) {
        put_partial(0, acc[0]);
        put_partial(2, acc[2]);
      }
      if constexpr (j == 21) {
        lds_barrier();
        o0 = finish(0);
        pin(o0);
      }
    });
    stamp_t(14);
    put_partial(1, acc[1]);
    put_partial(3, acc[3]);
    *(u32x2*)op = o0;
    stamp_t(15);
    lds_barrier();
    stamp_t(30);
    *(u32x2*)(op + 1024 * DM) = finish(1);
  };

  auto x1_piece = [&](int chunk) {
    const int c0 = 64 * xcd + 2 * bi, c = (lane >> 2) & 1, tile = 8 * chunk + (lane >> 3);
    const float ga = *(const float*)(smem + L_EP + 4 * c), cb1 = *(const float*)(smem + L_EP + 8 + 4 * c), dk = *(const float*)(smem + L_EP + 16 + 4 * c);
    const int l0 = 32 * tile + 8 * ((lane & 3) ^ (2 * ((c0 >> 2) & 1)));
    const char* pp = smem + CV_P + c * (16384 + 16);
    const int po = (4 * l0) ^ (16 * (tile & 3));
    const f32x4 y0 = *(const f32x4*)(pp + po) + *(const f32x4*)(pp + 8192 + po);
    const f32x4 y1 = *(const f32x4*)(pp + (po ^ 16)) + *(const f32x4*)(pp + 8192 + (po ^ 16));
    const u32x4 hv = *(const u32x4*)(smem + CV_H + c * CV_HB + 480 + 2 * l0);
    const u32x4 xx = *(const u32x4*)(smem + CV_X + c * 4096 + 2 * l0);
    u32x4 o, os;
#pragma unroll
    for (int k = 0; k < 4; ++k) {
      const u32 yb = k < 2 ? pack_bf16(y0[2 * k], y0[2 * k + 1]) : pack_bf16(y1[2 * k - 4], y1[2 * k - 3]);
      const f32x2 zf = __builtin_elementwise_fma(unpack2(hv[k]), f32x2{dk, dk}, unpack2(yb));
      const f32x2 gz = unpack2(pack_bf16(zf.x, zf.y)) * ga;
      const f32x2 of = unpack2(xx[k]) + unpack2(pack_bf16(gz.x, gz.y));
      o[k] = pack_bf16(of.x, of.y);
      const f32x2 sf = unpack2(o[k]) * cb1;
      os[k] = pack_bf16(sf.x, sf.y);
    }
    const u32 go = (4 * ((tile & 31) >> 1) + 2 * (tile >> 5) + (tile & 1)) * 32768 + c0 * 64 + (lane & 7) * 16;
    st16c(R, OFF_XS + go, os);
    st16c(R, OFF_X1 + go, o);
    MEMFENCE();
  };
  auto x1_arrive = [&](int hf) {
    const bool last = lds_arrive(smem, hf ? 14 : 4) == 3;
    st4c(R, only(last && lane < 8, OFF_FX + hf * 32768 + 4096 * lane + 16 * b), tag);
    MEMFENCE();
  };

  if (w < 4) {
    const int ch = w >> 1, half = w & 1;
    const int c0 = 64 * xcd + 2 * bi;
    const int n = 64 * half + lane;
    int kidx[9];
#pragma unroll
    for (int e = 0; e < 5; ++e) {
      const int l0 = 1023 - 8 * n - 2 * e, l1 = l0 - 1;
      const u32 pb = pack_bf16((float)l0, (float)l1);
      kidx[2 * e] = L_KC + 4 * (w * 656 + ((l0 < 128) ? max(l0, 0) : (int)(pb & 0xffffu) - 133 * 128));
      if (e < 4) kidx[2 * e + 1] = L_KC + 4 * (w * 656 + ((l1 < 128) ? l1 : (int)(pb >> 16) - 133 * 128));
    }
#pragma unroll
    for (int e = 0; e < 9; ++e) pin(kidx[e]);
    if (lane < 2) *(u32x4*)(smem + CV_K + w * CV_KCOPY + 4096 + lane * 16) = u32x4{0, 0, 0, 0};
    __builtin_amdgcn_s_waitcnt(0x0F70);
#pragma unroll
    for (int i = 0; i < 8; ++i) pin(ps[i]);
    const float v_cr = bflo(ps[2]), v_ci = bflo(ps[3]), o_cr = bflo(ps[4]), o_ci = bflo(ps[5]);
    const float dt = rb(expf(bflo(ps[6])));
    const float zr = -rb(expf(bflo(ps[0]))) * dt, zi = bflo(ps[1]) * dt;
    STAMP(3);
    const float mx = wave_max(fmaxf(fmaxf(fabsf(v_cr), fabsf(v_ci)), fmaxf(fabsf(o_cr), fabsf(o_ci))));
    const int ex = min(max((int)(__float_as_uint(mx) >> 23), 1), 252);
    const float sdn = __uint_as_float((u32)(254 - ex) << 23);
    const float sup = __uint_as_float((u32)(ex + 1) << 23) * dt;
    {
      char* base = smem + w * KG_WAVEB + lane * 4;
      auto put = [&](int row, f32x2 v) { *(u32*)(base + row * KG_ROWB) = pack_f16(v); };
      const f32x2 e8 = cexpk(zr, zi, 8.f), e16 = cexpk(zr, zi, 16.f);
      const f32x2 e1 = cexpk(zr, -zi, 1.f), e2 = cexpk(zr, -zi, 2.f);
      const f32x2 e128 = cexpk(zr, -zi, 128.f), e256 = cexpk(zr, -zi, 256.f);
      f32x2 f0 = e16, f1 = e8;
      put(41, f32x2{1.f, 0.f});
      put(42, e8);
#pragma unroll
      for (int j = 2; j < 16; j += 2) {
        f1 = cmul(f1, e16);
        put(41 + j, f0);
        put(42 + j, f1);
        if (j < 14) f0 = cmul(f0, e16);
      }
      const f32x2 e4 = cmul(e2, e2);
      f32x2 g[2];
      g[0] = f32x2{v_cr * sdn, -v_ci * sdn};
      g[1] = cmul(g[0], e128);
#pragma unroll
      for (int i = 0; i < 17; ++i) {
        const f32x2 gi = g[i & 1];
        if (i < 2) {
          f32x2 a = gi, c = cmul(gi, e1);
#pragma unroll
          for (int e = 0; e < 8; e += 2) {
            put(8 * i + e, a);
            put(8 * i + e + 1, c);
            if (e < 6) { a = cmul(a, e2); c = cmul(c, e2); }
          }
        } else if (i < 4) {
          const f32x2 c = cmul(gi, e2);
          put(16 + 4 * (i - 2), gi);
          put(17 + 4 * (i - 2), c);
          put(18 + 4 * (i - 2), cmul(gi, e4));
          put(19 + 4 * (i - 2), cmul(c, e4));
        } else if (i < 8) {
          put(24 + 2 * (i - 4), gi);
          put(25 + 2 * (i - 4), cmul(gi, e4));
        } else {
          put(32 + (i - 8), gi);
        }
        if (i + 2 < 17) g[i & 1] = cmul(gi, e256);
      }
    }
    lds_set(smem, 0, w);
    STAMP(4);

    const int r = lane & 15, q = lane >> 4;
    {
      f32x4 acc[3] = {};
#pragma unroll
      for (int hf = 0; hf < 2; ++hf) {
        if (hf == 0) {
          if (ch) lds_wait<12>(smem, 0);
          else lds_wait<3>(smem, 0);
        }
        const char* rbase = smem + (2 * ch + hf) * KG_WAVEB + r * KG_ROWB + q * 16;
        f16x8 fr[4][4];
#pragma unroll
        for (int ks = 0; ks < 4; ++ks) {
          fr[ks][3] = *(const f16x8*)(rbase + ks * 64 + 41 * KG_ROWB);
#pragma unroll
          for (int t = 0; t < 3; ++t) fr[ks][t] = *(const f16x8*)(rbase + ks * 64 + 16 * t * KG_ROWB);
        }
        __builtin_amdgcn_sched_barrier(0);
#pragma unroll
        for (int ks = 0; ks < 4; ++ks)
#pragma unroll
          for (int t = 0; t < 3; ++t) acc[t] = __builtin_amdgcn_mfma_f32_16x16x32_f16(fr[ks][t], fr[ks][3], acc[t], 0, 0, 0);
      }
      pin(acc[0]); pin(acc[1]); pin(acc[2]);
      STAMP(12);
      float* kc = (float*)(smem + L_KC) + w * 656;
      *(f32x4*)(kc + 128 * (q >> 1) + 4 * (q & 1) + 8 * r) = acc[0] * sup;
      if (q < 2) {
        *(f32x4*)(kc + 256 + 64 * q + 4 * r) = acc[1] * sup;
      } else {
        *(f32x2*)(kc + 384 + 64 * (q - 2) + 2 * r) = f32x2{acc[1][0], acc[1][1]} * sup;
        *(f32x2*)(kc + 416 + 64 * (q - 2) + 2 * r) = f32x2{acc[1][2], acc[1][3]} * sup;
      }
#pragma unroll
      for (int e = 0; e < 4; ++e)
        if (4 * q + e <= 8) kc[512 + 16 * (4 * q + e) + r] = acc[2][e] * sup;
      MEMFENCE();
      float kv[9];
#pragma unroll
      for (int e = 0; e < 9; ++e) kv[e] = *(const float*)(smem + kidx[e]);
      const float ka = kc[639 - n], kb = kc[640 - n];
      if (n == 127) kv[8] = 0.f;
      const float kt = (n & 1) ? ka : kb;
      const u32 aa = pack_bf16(ka, ka), bb = pack_bf16(kb, kb);
      *(u32x4*)(smem + CV_K + (2 * ch) * CV_KCOPY + 16 * n) = u32x4{bb, pack_bf16(kb, kt), aa, aa};
      *(u32x4*)(smem + CV_K + (2 * ch + 1) * CV_KCOPY + 16 * n) = u32x4{bb, pack_bf16(kt, ka), aa, aa};
      u32x4 o, s;
#pragma unroll
      for (int e = 0; e < 4; ++e) {
        o[e] = pack_bf16(kv[2 * e], kv[2 * e + 1]);
        s[e] = pack_bf16(kv[2 * e + 1], kv[2 * e + 2]);
      }
      *(u32x4*)(smem + CV_K + (2 * ch) * CV_KCOPY + 2048 + 16 * n) = o;
      *(u32x4*)(smem + CV_K + (2 * ch + 1) * CV_KCOPY + 2048 + 16 * n) = s;
    }
    lds_set(smem, 8, w);
    STAMP(5);
    lds_wait2<15, 15>(smem, 8, 2);
    STAMP(7);
    const char* abase = smem + CV_K + (2 * ch + ((r & 1) ^ 1)) * CV_KCOPY + 2 * (2030 - (r & ~1) + 8 * q) - 64 * (4 * half + 3 + 56);
    const char* bbase = smem + CV_H + ch * CV_HB + 2 * (224 + 16 * r + 8 * q) - 64 * (4 * half + 3);
    f32x4 acc[8] = {};
    bf16x8 af[2][8], bfr[2][8];
    u32 ab[4];
#pragma unroll
    for (int i = 0; i < 4; ++i) {
      ab[i] = (u32)(uintptr_t)(lds_char_p)abase + 1024 * i;
      pin(ab[i]);
    }
    auto ldfr = [&](int rho, int i) {
      const lds_u32_p p = (lds_u32_p)(uintptr_t)(ab[(7 - i) >> 1] + 64 * (3 - rho) + 512 * ((7 - i) & 1));
      af[rho & 1][i] = as_frag(u32x4{p[0], p[1], p[2], p[3]});
      bfr[rho & 1][i] = *(const bf16x8*)(bbase + 64 * (3 - rho) + 512 * i);
    };
    auto put_p = [&](int t0) {
#pragma unroll
      for (int t = t0; t < t0 + 4; ++t) *(f32x4*)(smem + CV_P + w * 8192 + ch * 16 + 1024 * t + 64 * r + 16 * (q ^ ((r >> 1) & 3))) = acc[t];
    };
    if (tid < 8 && (tid & 3) == 0) *(float*)(smem + L_EP + 16 + tid) = bflo(ps[7]);
#pragma unroll
    for (int i = 0; i < 4; ++i) ldfr(0, i);
    static_for<0, 48>([&](auto S) {
      constexpr int st = decltype(S)::value, nx = st + 4;
      constexpr bool pa = st < 16;
      constexpr int rho = pa ? st >> 2 : (st - 16) >> 3, m = pa ? st & 3 : (st - 16) & 7;
      if constexpr (nx < 16) ldfr(nx >> 2, nx & 3);
      else if constexpr (nx < 48) ldfr((nx - 16) >> 3, (nx - 16) & 7);
#pragma unroll
      for (int o = 0; o <= m; ++o) {
        if (pa ? m + o < 4 : (m + o >= 4 && m + o < 8)) {
          acc[m + o] = MFMA16(af[rho & 1][m], bfr[rho & 1][o], acc[m + o]);
          if (o != m) acc[m + o] = MFMA16(af[rho & 1][o], bfr[rho & 1][m], acc[m + o]);
        }
      }
#pragma unroll
      for (int i = 0; i < 3; ++i) {
        __builtin_amdgcn_sched_group_barrier(0x008, 1, 0);
        __builtin_amdgcn_sched_group_barrier(0x100, 1, 0);
      }
      __builtin_amdgcn_sched_barrier(0);
      if constexpr (st == 15) {
        put_p(0);
        lds_set(smem, 13, w);
        stamp_t(11);
      }
      if constexpr (st == 23) {
#pragma unroll
        for (int t = 4; t < 8; ++t) pin(acc[t]);
        lds_wait2<15, 14>(smem, 13, 10);
        x1_piece(w);
        stamp_t(16);
      }
      if constexpr (st == 39) {
#pragma unroll
        for (int t = 4; t < 8; ++t) pin(acc[t]);
        drain_vm();
        x1_arrive(0);
        stamp_t(9);
      }
    });
    STAMP(8);
    put_p(4);
    lds_set(smem, 3, w);
  } else {
    if (w != 5) __builtin_amdgcn_s_setprio(SPRIO);
    __builtin_amdgcn_s_waitcnt(0x0F70);
    const int un = min(b, 223), cg = un >> 3, type = (un >> 2) & 1, k8 = 2 * (un & 3) + (w & 1);
    const int kg = lane >> 3, cp = lane & 7;
    if (w < 6) {
      if (w == 4) {
#pragma unroll
        for (int a = 0; a < 4; ++a) {
          pin(pl[2 * a]);
          pin(pl[2 * a + 1]);
#pragma unroll
          for (int k = 0; k < 4; ++k)
            st16c(R, OFF_XG + b * 16384 + (a * 4 + k) * 1024 + l16, u32x4{pl[2 * a][k], tag, pl[2 * a + 1][k], tag});
        }
        STAMP(3);
        float s[2][2] = {}, qq[2][2] = {};
#pragma unroll
        for (int i = 0; i < 8; ++i)
#pragma unroll
          for (int k = 0; k < 4; ++k) {
            s[i & 1][k & 1] = dot2(pl[i][k], ONES2, s[i & 1][k & 1]);
            qq[i & 1][k & 1] = dot2(pl[i][k], pl[i][k], qq[i & 1][k & 1]);
          }
        float v = rs16(rs32(s[0][0] + s[0][1], qq[0][0] + qq[0][1]), rs32(s[1][0] + s[1][1], qq[1][0] + qq[1][1]));
        v += dpp<0x124>(v);
        v += dpp<0x128>(v);
        auto sw = __builtin_amdgcn_permlane32_swap(__float_as_uint(v), __float_as_uint(v), false, false);
        const u32 pk = ln_pack(__uint_as_float(sw[0]), __uint_as_float(sw[1]));
        st8c(R, only(lane < 32 && (lane & 15) < 4, OFF_GST + 64 * b + 16 * (lane & 3) + 8 * (lane >> 4)), u32x2{pk, tag});
        STAMP(33);
        drain_vm();
        lds_set(smem, 5, 1);
        RSTAMP(36);
      } else {
        pin(ps[0]);
        pin(ps[1]);
        const u32 sl[2] = {silu2(ps[0]), silu2(ps[1])};
#pragma unroll
        for (int i = 0; i < 4; ++i) pin(pl[i]);
        u32x4 pb[2][8];
#pragma unroll
        for (int i = 0; i < 8; ++i)
          pb[i >> 2][i & 3] = *(const u32x4*)(P.wm + (size_t)(32 * (b & 15) + 4 * (lane >> 3) + (i & 3)) * 3072 + 1024 * (1 + (i >> 2)) + 64 * (b >> 4) + 8 * (lane & 7));
        MEMFENCE();
        const u32 dst = OFF_GM + (b & 15) * 24576 + 512 * (b >> 4);
        mod_part<2>(pl, sl, R, dst, tag, lane);
        STAMP(33);
        mod_part<2>(pb[0], sl, R, dst + 8192, tag, lane);
        mod_part<2>(pb[1], sl, R, dst + 16384, tag, lane);
        STAMP(4);
      }
      const int t = tid - 256, c0 = 64 * xcd + 2 * bi, pr = c0 >> 1;
      const u32 xo = OFF_XG + (t >> 2) * 16384 + (((pr >> 6) * 4 + (pr & 3)) * 16 + ((pr >> 2) & 15)) * 64 + (t & 3) * 16;
      const u32 mo = OFF_GM + (lane >> 2) * 24576 + 8 * (((lane >> 1) & 1) * DM + c0 + (lane & 1));
      if (lane < 30) *(u32x4*)(smem + CV_H + (w - 4) * CV_HB + lane * 16) = u32x4{0, 0, 0, 0};
      u32x4 gx[8], gs[8];
      u32x2 gm;
      u32 bad = 1;
      lds_wait<2>(smem, 5);
      __builtin_amdgcn_s_sleep(GDELAY);
      STAMP(12);
#pragma unroll 1
      for (int spin = 0; spin < SPIN_CAP; ++spin) {
        if (bad) {
          gm = ld8c(R, mo);
#pragma unroll
          for (int i = 0; i < 8; ++i) {
            gx[i] = ld16c(R, xo + i * (32 * 16384));
            gs[i] = ld16c(R, OFF_GST + 16 * t + 2048 * i);
          }
          bad = gm[1] ^ tag;
#pragma unroll
          for (int i = 0; i < 8; ++i) bad |= (gx[i][1] ^ tag) | (gx[i][3] ^ tag) | (gs[i][1] ^ tag) | (gs[i][3] ^ tag);
        }
        if (__all(bad == 0)) break;
        __builtin_amdgcn_s_sleep(8);
        MEMFENCE();
      }
      drain_vm();
      if (w == 5) lds_set(smem, 10, 0);
      STAMP(6);
      RSTAMP(37);
      const float md = rb(sum_hi4(__uint_as_float(gm[0])));
      const f32x2 sa = {rdlane(md, 0), rdlane(md, 1)}, ca1 = {rb(1.f + rdlane(md, 2)), rb(1.f + rdlane(md, 3))};
#pragma unroll
      for (int i = 0; i < 8; ++i) {
        f32x2 hv[2];
#pragma unroll
        for (int e = 0; e < 2; ++e) {
          const u32 st = gs[i][2 * e], xv = gx[i][2 * e];
          hv[e] = __builtin_elementwise_fma((f32x2{bflo(xv), bfhi(xv)} - bflo(st)) * __builtin_amdgcn_rcpf(bfhi(st)), ca1, sa);
        }
        *(u32*)(smem + CV_H + 480 + 512 * i + 4 * t) = pack_bf16(hv[0].x, hv[1].x);
        *(u32*)(smem + CV_H + CV_HB + 480 + 512 * i + 4 * t) = pack_bf16(hv[0].y, hv[1].y);
      }
      lds_set(smem, 2, w - 4);
      STAMP(7);
#pragma unroll
      for (int i = 0; i < 8; ++i) {
        *(u32*)(smem + CV_X + 512 * i + 4 * t) = perm_lo(gx[i][0], gx[i][2]);
        *(u32*)(smem + CV_X + 4096 + 512 * i + 4 * t) = perm_hi(gx[i][0], gx[i][2]);
      }
      lds_set(smem, 10, w - 2);
    } else {
      u32x4 t[8];
#pragma unroll
      for (int i = 0; i < 8; ++i) pin(pl[i]);
      transpose8(pl, t);
      pin(code_touch);
      STAMP(3);
      {
        char* tb = smem + L_TR + (w & 1) * 8192;
#pragma unroll
        for (int n = 0; n < 8; ++n)
          *(u32x4*)(tb + ((cp >> 1) * 8 + kg) * 256 + (((n & 3) + 8 * (n >> 2) + 4 * (cp & 1)) ^ (cp >> 1)) * 16) = t[n];
        MEMFENCE();
        if (b < 224) {
#pragma unroll
          for (int k = 0; k < 8; ++k) {
            const int row = 4 * k + (lane >> 4), fl = k >> 1;
            const u32x4 o = *(const u32x4*)(tb + row * 256 + ((lane & 15) ^ fl) * 16);
            st16c(R, OFF_WP + (((4 * cg + fl) * 32 + 4 * k8) * 64 + (row & 7) * 32 + 16 * type + (lane & 15)) * 16, o);
          }
        }
      }
      STAMP(4);
      asm volatile("s_waitcnt lgkmcnt(0)" ::: "memory");
      lds_set(smem, 2, w - 4);
      drain_vm();
      st4c(R, only(lane == 0 && b < 224, OFF_FW + (cg * 16 + type * 8 + k8) * 64), tag);
      STAMP(12);
      lds_wait<1>(smem, 10);
      if (w == 6) {
#pragma unroll 1
        for (int spin = 0; spin < SPIN_CAP; ++spin) {
          const u32 f = ld4c(R, OFF_FW + min(((7 * fb) >> 2) * 16 + lane, ((7 * fb + 6) >> 2) * 16 + 15) * 64);
          if (__all(f == tag)) break;
          __builtin_amdgcn_s_sleep(10);
          MEMFENCE();
        }
        drain_vm();
        lds_set(smem, 5, 0);
        STAMP(5);
      } else {
        {
          const int c0 = 64 * xcd + 2 * bi;
          float v = gather_sum<2>(R, OFF_GM + (lane >> 3) * 49152 + 8 * ((2 + ((lane >> 1) & 3)) * DM + c0 + (lane & 1)), 24576, tag);
          v += dpp<0x128>(v);
          v = rs16(v, v);
          v = rb(rs32(v, v));
          if ((lane & 6) == 4) v = rb(1.f + v);
          if (lane < 8 && (lane & 2) == 0) *(float*)(smem + L_EP + 2 * (lane & 4) + 4 * (lane & 1)) = v;
          st8c(R, only(lane >= 2 && lane < 8, OFF_FM + ((lane >> 1) == 2 ? 0 : (lane >> 2) + 1) * 4096 + 8 * (c0 + (lane & 1))),
               u32x2{__float_as_uint(v), tag});
          lds_set(smem, 10, 1);
          STAMP(7);
        }
#pragma unroll
        for (int i = 0; i < 7; ++i) t[i] = *(const u32x4*)(P.wd + (size_t)(7 * b + i) * DM + 8 * lane);
#pragma unroll
        for (int i = 0; i < 7; ++i) pin(t[i]);
        __builtin_amdgcn_s_sleep(VDELAY);
#pragma unroll 1
        for (int spin = 0; spin < SPIN_CAP; ++spin) {
          u32 bad = 0;
#pragma unroll
          for (int i = 0; i < 8; ++i) t[i] = ld16c(R, OFF_FM + i * 1024 + l16);
#pragma unroll
          for (int i = 0; i < 8; ++i) bad |= (t[i][1] ^ tag) | (t[i][3] ^ tag);
          if (__all(bad == 0)) break;
          __builtin_amdgcn_s_sleep(8);
          MEMFENCE();
        }
#pragma unroll
        for (int i = 0; i < 8; ++i) *(u32*)(smem + L_V + 256 * i + 32 * (i >> 2) + 4 * lane) = perm_hi(t[i][0], t[i][2]);
        *(u32x4*)(smem + L_V + 2112 + l16) = u32x4{0, 0, 0, 0};
        lds_set(smem, 5, 2);
        STAMP(5);
      }
    }
    __builtin_amdgcn_s_setprio(0);
    if (w == 7) {
    const char* x1g = ws + OFF_X1 + (size_t)(4 * mt) * 32768;
    auto dma = [&](const char* src, int dst, int n) {
#pragma unroll 4
      for (int i = 0; i < n; ++i) glds16(l16, src + i * 1024, lds0 + dst + i * 1024);
    };
    const u32 fao = OFF_FA + mt * 4096;
    __builtin_amdgcn_s_setprio(3);
    auto x1_flags = [&](int hf) {
      u32 f[4];
#pragma unroll
      for (int i = 0; i < 4; ++i) f[i] = ld4c(R, OFF_FX + hf * 32768 + 4096 * xcd + (lane + 64 * i) * 16);
      return __all(((f[0] ^ tag) | (f[1] ^ tag) | (f[2] ^ tag) | (f[3] ^ tag)) == 0);
    };
    auto x1_poll = [&](int hf) {
#pragma unroll 1
      for (int spin = 0; spin < SPIN_CAP && !x1_flags(hf); ++spin) {
        __builtin_amdgcn_s_sleep(2);
        MEMFENCE();
      }
      drain_vm();
    };
    auto ln2_partial = [&](int hf, int raw) {
      dma(x1g + (2 * hf + (fb >> 3)) * 32768 + (fb & 7) * 4096, raw, 4);
      wait_vm<0>();
      const int g = lane >> 4;
      const char* sb = smem + raw + (8 * (g >> 1) + 4 * (g & 1) + q4) * 64 + 8 * p4;
      const bf16x8 ones = as_frag(u32x4{ONES2, ONES2, ONES2, ONES2});
      bf16x8 f[2][2];
#pragma unroll
      for (int k2 = 0; k2 < 2; ++k2)
#pragma unroll
        for (int c = 0; c < 2; ++c) {
          const char* p = sb + 32 * (c ^ (g & 1)) + k2 * 2048;
          f[c][k2] = tr_frag(p, p + 1024);
        }
#pragma unroll
      for (int c = 0; c < 2; ++c) {
        f32x4 aq = {}, as = {};
#pragma unroll
        for (int k2 = 0; k2 < 2; ++k2) {
          aq = MFMA16(f[c][k2], f[c][k2], aq);
          as = MFMA16(ones, f[c][k2], as);
        }
        const float sq = p4 == 0 ? aq[0] : p4 == 1 ? aq[1] : p4 == 2 ? aq[2] : aq[3];
        st16c(R, only(g == q4, OFF_GS2 + (((mt * 2 + hf) * 8 + (fb & 7)) * 64 + 32 * (fb >> 3) + 16 * c + (lane & 15)) * 16),
              u32x4{__float_as_uint(as[0]), tag, __float_as_uint(sq), tag});
      }
      MEMFENCE();
    };
    auto ln2_gather = [&](int hf) {
#pragma unroll 1
      for (int spin = 0; spin < SPIN_CAP; ++spin) {
        u32x4 g[8];
        u32 bad = 0;
        float s1 = 0.f, s2 = 0.f;
#pragma unroll
        for (int i = 0; i < 8; ++i) g[i] = ld16c(R, OFF_GS2 + (mt * 2 + hf) * 8192 + i * 1024 + l16);
#pragma unroll
        for (int i = 0; i < 8; ++i) {
          bad |= (g[i][1] ^ tag) | (g[i][3] ^ tag);
          s1 += __uint_as_float(g[i][0]);
          s2 += __uint_as_float(g[i][2]);
        }
        const u32 ms = ln_pack(s1, s2) ^ 0x8000u;
        *(u32x4*)(smem + L_SF + (64 * hf + (lane ^ (32 * par))) * 16) = u32x4{perm_lo(ms, ms), ms, perm_hi(ms, ms), 0};
        const float k0 = -2.f * 0.7978845608028654f * 1.4426950408889634f, k1 = k0 * 0.044715f;
        const float sd = bfhi(ms), rs = __builtin_amdgcn_rcpf(sd);
        *(f32x4*)(smem + L_RS + (64 * hf + (lane ^ (32 * par))) * 16) = f32x4{k0 * rs, k1 * rs * rs * rs, sd * sd, 0.f};
        if (__all(bad == 0)) break;
        __builtin_amdgcn_s_sleep(4);
        MEMFENCE();
      }
      lds_set(smem, 7, hf);
    };
    lds_wait<15>(smem, 3);
    __builtin_amdgcn_s_sleep(ADELAY);
    x1_poll(0);
    lds_set(smem, 6, 1);
    STAMP(10);
    ln2_partial(0, CV_P);
    stamp_t(16);
    lds_barrier();
    STAMP(17);
    barrier();
    __builtin_amdgcn_s_sleep(SDELAY);
    ln2_gather(0);
    stamp_t(18);
    barrier();
    stamp_t(19);
    x1_poll(1);
    lds_set(smem, 6, 2);
    stamp_t(9);
    ln2_partial(1, L_SPARE);
    st4c(R, only(b == 0 && lane == 0, OFF_E), tag);
    stamp_t(20);
    lds_barrier();
    stamp_t(21);
    stage_all(4);
    barrier();
    ln2_gather(1);
    stamp_t(8);
#pragma unroll
    for (int k = 0; k < 4; ++k) stage_wd(k);
    wait_vm<8>();
    stamp_t(22);
    lds_barrier();
    stamp_t(23);
    st4c(R, only(lane == 0, fao + fb * 64), tag);
    stage_all(5);
    stage_all(6);
    dma(ws + OFF_X1 + (size_t)(4 * m2 + cp2) * 32768 + nt * 4096, L_RES, 4);
    dma(ws + OFF_X1 + (size_t)(4 * m2 + cp2 + 2) * 32768 + nt * 4096, L_RES + 4096, 4);
    wait_vm<40>();
#pragma unroll
    for (int j = 0; j < 10; ++j) wr[j] = wd_frag(j);
#pragma unroll
    for (int j = 0; j < 10; ++j) pin(wr[j]);
    lds_wait2<15, 7>(smem, 11, 12);
    st4c(R, only(lane == 0, fao + 1024 + fb * 64), tag);
    MEMFENCE();
#pragma unroll
    for (int k = 9; k < 14; ++k) stage_wd(k);
    stamp_t(24);
    barrier();
    stamp_t(25);
    load_flag(std::integral_constant<int, 0>{});
    *(float*)(smem + L_GB + 4 * lane) = gather_sum<1>(R, OFF_FM + 2 * 4096 + 8 * (64 * nt + lane), 0, tag);
    asm volatile("s_waitcnt vmcnt(0) lgkmcnt(0)" ::: "memory");
    __builtin_amdgcn_s_setprio(0);
    down_phase(std::integral_constant<int, 1>{});
  }
  }

  if (w < 7) {
    lds_wait<1>(smem, 5);
    if (w >= 4) {
      lds_wait<3>(smem, 2);
      static_for<0, NPARK>(load_w);
    }
    static_for<NPARK, 32>(load_w);
    if (w >= 4) {
      lds_wait<15>(smem, 8);
#pragma unroll 4
      for (int i = 0; i < 4 * NPARK / 3; ++i) {
        const int f = 3 * i + (w - 4), tw = f & 3, k = f >> 2;
        glds16(l16, ws + OFF_WP + (size_t)(7 * fb + tw) * 32768 + k * 1024, lds0 + L_PARK + (tw * NPARK + k) * 1024);
      }
      drain_vm();
      lds_set(smem, 9, w - 4);
      STAMP(9);
    } else {
      lds_wait<7>(smem, 9);
#pragma unroll
      for (int j = 0; j < NPARK; ++j) wf[j] = *(const u32x4*)(smem + L_PARK + (w * NPARK + j) * 1024 + l16);
      asm volatile("s_waitcnt lgkmcnt(0)" ::: "memory");
      lds_set(smem, 1, w);
    }
    if (w >= 3) {
      lds_wait2<15, 14>(smem, 3, 10);
      x1_piece(w == 3 ? 7 : w);
      STAMP(16);
    }
    auto load_x = [&](int i) {
      const int pc = (i < 10 ? 0 : 64) + min(7 * (i % 10) + w, 63);
      glds16(l16, ws + OFF_XS + (size_t)(4 * mt) * 32768 + ((pc ^ (32 * par)) << 10), lds0 + pc * 1024);
    };
    auto first_x = [&]() {
#pragma unroll
      for (int i = 0; i < 5; ++i) load_x(i);
    };
    wait_vm<2>();
    if (w < 3) drain_vm();
#pragma unroll
    for (int j = 0; j < 32; ++j) pin(wf[j]);
    STAMP(10);
    lds_wait<15>(smem, 1);
    const bool early = __builtin_amdgcn_readfirstlane((int)lds_peek(smem, 6, 1));
    MEMFENCE();
    if (early) first_x();
    u32x4 aux;
    {
      lds_wait<4>(smem, 5);
      const char* vb = smem + L_V + (((lane ^ (lane >> 4)) & 1) ? 2112 : (lane & 2) * 528) + 16 * hh;
      f32x4 d = {};
      bf16x8 va[32];
#pragma unroll
      for (int j = 0; j < 12; ++j) va[j] = *(const bf16x8*)(vb + 32 * j);
      static_for<0, 32>([&](auto J) {
        constexpr int j = decltype(J)::value;
        if constexpr (j + 12 < 32) va[j + 12] = *(const bf16x8*)(vb + 32 * (j + 12));
        d = MFMA16(va[j], as_frag(wf[j]), d);
        pin(d);
        __builtin_amdgcn_sched_barrier(0);
      });
      const float c = blk ? d[1] : d[0], sf = blk ? d[3] : d[2];
      const float c0 = rb(c), c1 = rb(c - c0), b0 = rb(sf), b1 = rb(sf - b0);
      aux = u32x4{pack_bf16(c0, c1), pack_bf16(c - c0 - c1, b0), pack_bf16(b1, sf - b0 - b1), 0};
      if (hh) aux = u32x4{0, 0, 0, 0};
      pin(aux);
    }
    u32 xb[2][2];
#pragma unroll
    for (int i = 0; i < 2; ++i) {
      xb[i][0] = lds0 + 65536 * i + (8 * hh + q4) * 64 + 32 * blk + 8 * p4;
      xb[i][1] = lds0 + 65536 * i + (8 * hh + 4 + q4) * 64 + 32 * (blk ^ 1) + 8 * p4;
      pin(xb[i][0]);
      pin(xb[i][1]);
    }
    auto xfrag = [&](int s) { return tr_frag_at(xb[s >> 6][0] + (s & 63) * 1024, xb[s >> 6][1] + (s & 63) * 1024); };
    const char* actg[2] = {ws + OFF_ACT + ((size_t)(4 * mt + par) * NFT + ft) * 1024,
                           ws + OFF_ACT + ((size_t)(4 * mt + (par ^ 1)) * NFT + ft) * 1024};
    asm volatile("" : "+s"(actg[0]), "+s"(actg[1]));
    u32 sfo = lds0 + L_SF + (lane & 31) * 16;
    pin(sfo);
    u32 pubo = lds0 + L_SYNC + 16 * 11 + 4 * w;
    pin(pubo);
#pragma unroll
    for (int i = 0; i < 6; ++i) pin(wfo[i & 1][i >> 1]);
    if (w >= 3) {
      if (early) wait_vm<5>();
      else wait_vm<0>();
      x1_arrive(1);
    }
    STAMP(6);
    if (!early) {
      lds_wait<2>(smem, 6);
      first_x();
    }
    wait_vm<3>();
    barrier();
    STAMP(17);
    constexpr int XD = 3;
    bf16x8 xf[4];
#pragma unroll
    for (int s = 0; s < XD; ++s) xf[s] = xfrag(s);
    f32x16 acc[3];
    f32x4 gcs[2];
    float ea[4], em[4], prev = 0.f;
    u32x4 o;
    auto epi = [&](auto T, auto U) {
      constexpr int t = decltype(T)::value, u = decltype(U)::value, n = u >> 1;
      const f32x4& gc = gcs[t & 1];
      const f32x16& a = acc[t % 3];
      if constexpr ((u & 1) == 0) {
        if constexpr (n >= 3 && n < 11) {
          float v = em[(n - 3) & 3] * ea[(n - 3) & 3];
          pin(v);
          if constexpr ((n - 3) & 1) o[(n - 3) >> 1] = pack_bf16(prev, v);
          else prev = v;
          if constexpr (n == 10) gstore16(o, l16, actg[t & 1] + (size_t)(t >> 1) * 2 * NFT * 1024);
        }
        if constexpr (n >= 2 && n < 10) { ea[(n - 2) & 3] = __builtin_fmaf(ea[(n - 2) & 3], gc[2], gc[2]); pin(ea[(n - 2) & 3]); }
        if constexpr (n >= 1 && n < 9) { ea[(n - 1) & 3] *= a[n - 1]; pin(ea[(n - 1) & 3]); }
        if constexpr (n < 8) {
          ea[n & 3] = a[n] * a[n];
          em[n & 3] = a[n] * a[8 + n];
          pin(ea[n & 3]);
          pin(em[n & 3]);
        }
      } else {
        if constexpr (n >= 2 && n < 10) { ea[(n - 2) & 3] = __builtin_amdgcn_rcpf(ea[(n - 2) & 3]); pin(ea[(n - 2) & 3]); }
        if constexpr (n >= 1 && n < 9) { ea[(n - 1) & 3] = __builtin_amdgcn_exp2f(ea[(n - 1) & 3]); pin(ea[(n - 1) & 3]); }
        if constexpr (n < 8) { ea[n & 3] = __builtin_fmaf(ea[n & 3], gc[1], gc[0]); pin(ea[n & 3]); }
      }
    };
    bf16x8 sf;
    u32 peek, peek2;
    auto fold_load = [&](int t) {
      sf = *(const __attribute__((address_space(3))) bf16x8*)(uintptr_t)(sfo + t * 512);
      gcs[t & 1] = *(const __attribute__((address_space(3))) f32x4*)(uintptr_t)(sfo + (L_RS - L_SF) + t * 512);
    };
    auto fold = [&](int t) {
      acc[t % 3] = MFMA32(as_frag(aux), sf, acc[t % 3]);
      pin(acc[t % 3]);
    };
    constexpr int PUB = (ES1 + EL + 12 > 100 ? ES1 + EL + 12 : 100);
    static_for<0, 4>([&](auto T) {
      constexpr int t = decltype(T)::value;
      static_for<0, 32>([&](auto KS) {
        constexpr int ks = decltype(KS)::value, s = 32 * t + ks;
        if constexpr (s == 14 - XD) {
          wait_vm<up_after(-1, s, 0)>();
          barrier();
        }
        if constexpr (s == ES0 - 10) peek = lds_peek(smem, 7, 0);
        if constexpr (s == ES0 - 6) {
          if (__builtin_expect(!__builtin_amdgcn_readfirstlane((int)peek), 0)) lds_wait<1>(smem, 7);
          MEMFENCE();
          fold_load(0);
        }
        if constexpr (s == XL - 5) peek2 = lds_peek(smem, 6, 2);
        if constexpr (s == XL - 1) {
          if (__builtin_expect(!__builtin_amdgcn_readfirstlane((int)peek2), 0)) lds_wait<4>(smem, 6);
          MEMFENCE();
        }
        if constexpr (s == 64) {
#pragma unroll
          for (int i = 0; i < XD; ++i) xf[(s + i) & 3] = xfrag(s + i);
        }
        if constexpr (s == 78 - XD) {
          wait_vm<up_after(XL + 4, s, 0)>();
          barrier();
        }
        if constexpr (s == ES2 - 10) peek = lds_peek(smem, 7, 1);
        if constexpr (s == ES2 - 6) {
          if (__builtin_expect(!__builtin_amdgcn_readfirstlane((int)peek), 0)) lds_wait<2>(smem, 7);
          MEMFENCE();
          fold_load(2);
        }
        if constexpr ((t & 1) && ks == 29) fold_load(t);
        if constexpr (s == PUB) {
          wait_vm<up_after(ES1 + EL - 1, s, 1)>();
          *(__attribute__((address_space(3))) volatile u32*)(uintptr_t)pubo = 1u;
          MEMFENCE();
        }
        if constexpr (s + XD < 128 && !(s < 64 && s + XD >= 64)) xf[(s + XD) & 3] = xfrag(s + XD);
        if constexpr (ks == 0) acc[t % 3] = MFMA32(as_frag(wf[0]), xf[s & 3], f32x16{});
        else acc[t % 3] = MFMA32(as_frag(wf[ks]), xf[s & 3], acc[t % 3]);
        pin(acc[t % 3]);
        if constexpr (s == ES0 - 3) fold(0);
        if constexpr (s == ES2 - 3) fold(2);
        if constexpr ((t & 1) && ks == 31) fold(t);
        if constexpr (s >= ES0 && s < ES0 + EL) epi(std::integral_constant<int, 0>{}, std::integral_constant<int, s - ES0>{});
        if constexpr (s >= ES1 && s < ES1 + EL) epi(std::integral_constant<int, 1>{}, std::integral_constant<int, s - ES1>{});
        if constexpr (s >= ES2 && s < ES2 + EL) epi(std::integral_constant<int, 2>{}, std::integral_constant<int, s - ES2>{});
        if constexpr (s == 102) wait_vm<up_after(82, s, 0)>();
        if constexpr (t == 3 && ks < 10) wr[ks < 4 ? ks : ks < 6 ? ks + 4 : ks - 2] = wd_frag(ks < 4 ? ks : ks < 6 ? ks + 4 : ks - 2);
        if constexpr (t == 3 && ks == 11) {
#pragma unroll
          for (int j = 0; j < 10; ++j) pin(wr[j]);
        }
        if constexpr (up_xpiece(s) >= 0) load_x(up_xpiece(s));
        if constexpr (up_stage(s) >= 0) stage_wd(up_stage(s));
        __builtin_amdgcn_sched_barrier(0);
      });
      stamp_t(18 + 2 * t);
      if constexpr (t == 0) wait_vm<up_after(9, 32, 0)>();
      if constexpr (t == 1) wait_vm<up_after(XL + 1, 64, 0)>();
      if constexpr (t == 2) wait_vm<up_after(ES0 + EL - 1, 96, 1)>();
      lds_barrier();
      stamp_t(19 + 2 * t);
    });
    load_flag(std::integral_constant<int, 0>{});
#pragma unroll
    for (int i = 0; i < 8; i += 2) {
      float v[2];
#pragma unroll
      for (int e = 0; e < 2; ++e) {
        const f32x4& gc = gcs[1];
        const float a = acc[0][i + e];
        const float ex = __builtin_amdgcn_exp2f(a * __builtin_fmaf(a * a, gc[1], gc[0]));
        v[e] = a * acc[0][8 + i + e] * __builtin_amdgcn_rcpf(__builtin_fmaf(ex, gc[2], gc[2]));
      }
      o[i >> 1] = pack_bf16(v[0], v[1]);
    }
    gstore16(o, l16, actg[1] + (size_t)2 * NFT * 1024);
    STAMP(26);
    wait_vm<up_after(ES2 + EL - 1, 128, 1) + 2>();
    down_phase(std::integral_constant<int, 0>{});
  }
  DIAG_END(ws);
}

extern "C" void kernel_launch(void* const* d_in, const int* in_sizes, int n_in, void* d_out, int out_size, void* d_ws,
                              size_t ws_size, hipStream_t stream) {
  Params p;
  p.x = (const u16*)d_in[0];
  p.la = (const u16*)d_in[1];
  p.ai = (const u16*)d_in[2];
  p.cr = (const u16*)d_in[3];
  p.ci = (const u16*)d_in[4];
  p.ldt = (const u16*)d_in[5];
  p.dsk = (const u16*)d_in[6];
  p.wg = (const u16*)d_in[8];
  p.wu = (const u16*)d_in[9];
  p.wd = (const u16*)d_in[10];
  p.c = (const u16*)d_in[11];
  p.wm = (const u16*)d_in[12];
  p.out = (u16*)d_out;
  p.ws = (char*)d_ws;
  (void)hipFuncSetAttribute((const void*)fused_block, hipFuncAttributeMaxDynamicSharedMemorySize, SMEM);
  fused_block<<<256, 512, SMEM, stream>>>(p);
}
```

```cpp
#include <hip/hip_runtime.h>
#include <hip/hip_bf16.h>
#include <stdint.h>
#include <type_traits>

typedef unsigned short u16;
typedef unsigned int u32;
typedef unsigned long long u64;
typedef __attribute__((ext_vector_type(8))) __bf16 bf16x8;
typedef __attribute__((ext_vector_type(4))) __bf16 bf16x4;
typedef __attribute__((ext_vector_type(2))) __bf16 bf16x2;
typedef __attribute__((ext_vector_type(2))) _Float16 f16x2;
typedef __attribute__((ext_vector_type(8))) _Float16 f16x8;
typedef __attribute__((ext_vector_type(2))) float f32x2;
typedef __attribute__((ext_vector_type(4))) float f32x4;
typedef __attribute__((ext_vector_type(16))) float f32x16;
typedef __attribute__((ext_vector_type(4))) u32 u32x4;
typedef __attribute__((ext_vector_type(2))) u32 u32x2;
typedef __attribute__((address_space(3))) bf16x4* lds_bf16x4_p;
typedef __attribute__((address_space(3))) char* lds_char_p;
typedef __attribute__((address_space(3))) u32* lds_u32_p;
typedef __amdgpu_buffer_rsrc_t rsrc_t;

#define DEVI __device__ __forceinline__

constexpr int SL = 2048, DM = 512, NS = 128, DF = 1792;
constexpr int NFT = DF / 16;
constexpr int SPIN_CAP = 1 << 17;
constexpr int GDELAY = 8, ESTART = 42, SDELAY = 4, SPRIO = 1, AWIN = 7, LO2 = 7, LO3 = 14, VDELAY = 16, XLATE = 46, ADELAY = 8, ESTART2 = 104, ARR = 39, EPA = 23, WDELAY = 6;
constexpr int CODE_BYTES = 44 * 1024;

constexpr u32 OFF_E = 0;
constexpr u32 OFF_GST = 4096;
constexpr u32 OFF_FX = 524288;
constexpr u32 OFF_FW = 81920;
constexpr u32 OFF_FA = 131072;
constexpr u32 OFF_FM = 114688;
constexpr u32 OFF_GS2 = 196608;
constexpr u32 OFF_GM = 622592;
static_assert(OFF_GM + 16 * 24576 <= (1u << 20), "control block");
constexpr u32 OFF_XG = 1u << 20;
constexpr u32 OFF_WP = OFF_XG + (4u << 20);
constexpr u32 OFF_X1 = OFF_WP + 112 * 32768;
constexpr u32 OFF_XS = OFF_X1 + (2u << 20);
constexpr u32 OFF_ACT = OFF_XS + (2u << 20);
constexpr u32 WS_USED = OFF_ACT + (7u << 20);

constexpr int KG_ROWB = 272;
constexpr int KG_WAVEB = 57 * KG_ROWB;
constexpr int L_KC = 4 * KG_WAVEB;
constexpr int CV_KCOPY = 4160;
constexpr int L_PARK = 0;
constexpr int NPARK = 21;
constexpr int CV_K = 4 * NPARK * 1024;
constexpr int CV_H = CV_K + 4 * CV_KCOPY;
constexpr int CV_HB = 2288 * 2;
constexpr int CV_X = CV_H + 2 * CV_HB;
constexpr int CV_P = CV_X + 8192;
constexpr int L_TR = CV_P;
static_assert(L_KC + 4 * 656 * 4 <= CV_K, "front LDS map");
constexpr int L_RES = 131072;
constexpr int L_SPARE = L_RES + 8192;
constexpr int L_SF = L_SPARE + 16384;
static_assert(CV_P + 32768 + 16 <= L_SF, "front LDS map");
constexpr int L_RS = L_SF + 2048;
constexpr int L_V = L_SF - 2112;
static_assert(CV_P + 32768 + 16 <= L_V, "front LDS map");
constexpr int L_GB = L_RS + 2048;
constexpr int L_SYNC = L_GB + 256;
constexpr int L_EP = L_SYNC + 256;
constexpr int SMEM = L_EP + 32;

template <int I, int N, class F>
DEVI void static_for(F&& f) {
  if constexpr (I < N) {
    f(std::integral_constant<int, I>{});
    static_for<I + 1, N>(f);
  }
}

DEVI float bflo(u32 v) { return __uint_as_float(v << 16); }
DEVI float bfhi(u32 v) { return __uint_as_float(v & 0xffff0000u); }
DEVI f32x2 unpack2(u32 v) { return f32x2{bflo(v), bfhi(v)}; }
DEVI u32 pack_bf16(float a, float b) {
  f32x2 v = {a, b};
  return __builtin_bit_cast(u32, __builtin_convertvector(v, bf16x2));
}
DEVI float rb(float a) { return bflo(pack_bf16(a, a)); }
DEVI u32 pack_f16(f32x2 v) { return __builtin_bit_cast(u32, __builtin_convertvector(v, f16x2)); }
DEVI u32 perm_lo(u32 a, u32 b) { return __builtin_amdgcn_perm(b, a, 0x05040100u); }
DEVI u32 perm_hi(u32 a, u32 b) { return __builtin_amdgcn_perm(b, a, 0x07060302u); }
DEVI float dot2(u32 a, u32 b, float c) {
  return __builtin_amdgcn_fdot2_f32_bf16(__builtin_bit_cast(bf16x2, a), __builtin_bit_cast(bf16x2, b), c, false);
}
constexpr u32 ONES2 = 0x3f803f80u;

template <int CTRL>
DEVI float dpp(float v) { return __int_as_float(__builtin_amdgcn_update_dpp(0, __float_as_int(v), CTRL, 0xf, 0xf, true)); }
DEVI float rs32(float a, float b) {
  auto r = __builtin_amdgcn_permlane32_swap(__float_as_uint(a), __float_as_uint(b), false, false);
  return __uint_as_float(r[0]) + __uint_as_float(r[1]);
}
DEVI float rs16(float a, float b) {
  auto r = __builtin_amdgcn_permlane16_swap(__float_as_uint(a), __float_as_uint(b), false, false);
  return __uint_as_float(r[0]) + __uint_as_float(r[1]);
}
DEVI float wave_max(float v) {
  v = fmaxf(v, dpp<0xB1>(v)); v = fmaxf(v, dpp<0x4E>(v)); v = fmaxf(v, dpp<0x141>(v)); v = fmaxf(v, dpp<0x140>(v));
  auto r = __builtin_amdgcn_permlane16_swap(__float_as_uint(v), __float_as_uint(v), false, false);
  v = fmaxf(__uint_as_float(r[0]), __uint_as_float(r[1]));
  auto s = __builtin_amdgcn_permlane32_swap(__float_as_uint(v), __float_as_uint(v), false, false);
  return fmaxf(__uint_as_float(s[0]), __uint_as_float(s[1]));
}

DEVI f32x2 cmul(f32x2 a, f32x2 b) {
  const f32x2 t = a.yy * f32x2{-b.y, b.x};
  return __builtin_elementwise_fma(a.xx, b, t);
}
DEVI f32x2 cexpk(float zr, float zi, float k) {
  const float mag = __builtin_amdgcn_exp2f(zr * (k * 1.4426950408889634f));
  const float rev = __builtin_amdgcn_fractf(zi * (k * 0.15915494309189535f));
  return f32x2{mag * __builtin_amdgcn_cosf(rev), mag * __builtin_amdgcn_sinf(rev)};
}

DEVI u32x4 ld16c(rsrc_t r, u32 off) { return __builtin_amdgcn_raw_buffer_load_b128(r, off, 0, 16); }
DEVI u32x2 ld8c(rsrc_t r, u32 off) { return __builtin_amdgcn_raw_buffer_load_b64(r, off, 0, 16); }
DEVI u32 ld4c(rsrc_t r, u32 off) { return __builtin_amdgcn_raw_buffer_load_b32(r, off, 0, 16); }
DEVI void st16c(rsrc_t r, u32 off, u32x4 v) { __builtin_amdgcn_raw_buffer_store_b128(v, r, off, 0, 16); }
DEVI void st8c(rsrc_t r, u32 off, u32x2 v) { __builtin_amdgcn_raw_buffer_store_b64(v, r, off, 0, 16); }
DEVI void st4c(rsrc_t r, u32 off, u32 v) { __builtin_amdgcn_raw_buffer_store_b32(v, r, off, 0, 16); }
DEVI u32 only(bool c, u32 off) { return c ? off : 0x80000000u; }
#define MEMFENCE() asm volatile("" ::: "memory")
DEVI void drain_vm() {
  __builtin_amdgcn_s_waitcnt(0x0F70);
  asm volatile("s_waitcnt vmcnt(0)" ::: "memory");
}

template <int N>
DEVI float gather_sum(rsrc_t r, u32 off, u32 stride, u32 tag) {
  float v = 0.f;
#pragma unroll 1
  for (int spin = 0; spin < SPIN_CAP; ++spin) {
    u32x2 g[N];
#pragma unroll
    for (int i = 0; i < N; ++i) g[i] = ld8c(r, off + i * stride);
    u32 bad = 0;
    v = 0.f;
#pragma unroll
    for (int i = 0; i < N; ++i) {
      v += __uint_as_float(g[i][0]);
      bad |= g[i][1] ^ tag;
    }
    if (__all(bad == 0)) break;
    __builtin_amdgcn_s_sleep(2);
    MEMFENCE();
  }
  MEMFENCE();
  return v;
}
DEVI float sum_hi4(float v) {
  v += dpp<0x124>(v);
  v += dpp<0x128>(v);
  v = rs16(v, v);
  return rs32(v, v);
}
DEVI u32 silu2(u32 c) {
  const float a = bflo(c), b = bfhi(c);
  return pack_bf16(a * __builtin_amdgcn_rcpf(1.f + __expf(-a)), b * __builtin_amdgcn_rcpf(1.f + __expf(-b)));
}
DEVI float rdlane(float v, int l) { return __int_as_float(__builtin_amdgcn_readlane(__float_as_int(v), l)); }
DEVI void poll_flag(rsrc_t r, u32 off, u32 tag) {
#pragma unroll 1
  for (int spin = 0; spin < SPIN_CAP; ++spin) {
    const u32 f = ld4c(r, off);
    if (__all(f == tag)) break;
    __builtin_amdgcn_s_sleep(1);
    MEMFENCE();
  }
  MEMFENCE();
}

DEVI void lds_set(char* smem, int grp, int word) {
  MEMFENCE();
  *(__attribute__((address_space(3))) volatile u32*)(smem + L_SYNC + 16 * grp + 4 * word) = 1u;
  MEMFENCE();
}
DEVI u32 lds_peek(char* smem, int grp, int word) {
  return *(__attribute__((address_space(3))) volatile u32*)(smem + L_SYNC + 16 * grp + 4 * word);
}
DEVI u32 lds_arrive(char* smem, int grp) {
  u32 old;
  const u32 addr = (u32)(uintptr_t)(lds_char_p)(smem + L_SYNC + 16 * grp);
  asm volatile("s_mov_b64 exec, 1\n\tds_add_rtn_u32 %0, %1, %2\n\ts_mov_b64 exec, -1\n\ts_waitcnt lgkmcnt(0)"
               : "=&v"(old) : "v"(addr), "v"(1u) : "memory");
  return (u32)__builtin_amdgcn_readfirstlane((int)old);
}
template <int MASK>
DEVI void lds_wait(char* smem, int grp) {
  typedef __attribute__((address_space(3))) volatile u32x4* lds_u32x4_p;
#pragma unroll 1
  for (int spin = 0; spin < (1 << 22); ++spin) {
    MEMFENCE();
    const u32x4 v = *(lds_u32x4_p)(smem + L_SYNC + 16 * grp);
    const u32 a = ((MASK & 1) ? v[0] : 1u) & ((MASK & 2) ? v[1] : 1u) & ((MASK & 4) ? v[2] : 1u) & ((MASK & 8) ? v[3] : 1u);
    if (__builtin_amdgcn_readfirstlane((int)a)) break;
    __builtin_amdgcn_s_sleep(1);
  }
  MEMFENCE();
}

template <int MA, int MB>
DEVI void lds_wait2(char* smem, int ga, int gb) {
  typedef __attribute__((address_space(3))) volatile u32x4* lds_u32x4_p;
#pragma unroll 1
  for (int spin = 0; spin < (1 << 22); ++spin) {
    MEMFENCE();
    const u32x4 v = *(lds_u32x4_p)(smem + L_SYNC + 16 * ga), u = *(lds_u32x4_p)(smem + L_SYNC + 16 * gb);
    const u32 a = ((MA & 1) ? v[0] : 1u) & ((MA & 2) ? v[1] : 1u) & ((MA & 4) ? v[2] : 1u) & ((MA & 8) ? v[3] : 1u) &
                  ((MB & 1) ? u[0] : 1u) & ((MB & 2) ? u[1] : 1u) & ((MB & 4) ? u[2] : 1u) & ((MB & 8) ? u[3] : 1u);
    if (__builtin_amdgcn_readfirstlane((int)a)) break;
  }
  MEMFENCE();
}

DEVI void glds16(u32 voff, const void* sbase, u32 ldsaddr) {
  asm volatile("s_mov_b32 m0, %2\n\ts_nop 0\n\tglobal_load_lds_dwordx4 %0, %1 sc1" ::"v"(voff), "s"(sbase), "s"(ldsaddr) : "memory");
}
DEVI void glds16p2(u32 voff0, u32 voff1, const void* sbase, u32 ldsaddr) {
  asm volatile("s_mov_b32 m0, %3\n\ts_nop 0\n\tglobal_load_lds_dwordx4 %0, %2\n\tglobal_load_lds_dwordx4 %1, %2 offset:1024"
               ::"v"(voff0), "v"(voff1), "s"(sbase), "s"(ldsaddr) : "memory");
}
template <int N>
DEVI void wait_vm() { asm volatile("s_waitcnt vmcnt(%0)" ::"n"(N) : "memory"); }
template <int N, class T>
DEVI void wait_vm_reg(T& r) { asm volatile("s_waitcnt vmcnt(%1)" : "+v"(r) : "n"(N)); }
template <class T>
DEVI void pin(T& r) { asm volatile("" : "+v"(r)); }
DEVI void pin_s(const char*& r) { asm volatile("" : "+s"(r)); }
template <int OFF>
DEVI void gload16(u32x4& r, u32 voff, const void* sbase) {
  asm volatile("global_load_dwordx4 %0, %1, %2 offset:%3 sc1" : "=v"(r) : "v"(voff), "s"(sbase), "n"(OFF) : "memory");
}
template <int OFF>
DEVI void gload4(u32& r, u32 voff, const void* sbase) {
  asm volatile("global_load_dword %0, %1, %2 offset:%3 sc1" : "=v"(r) : "v"(voff), "s"(sbase), "n"(OFF) : "memory");
}
DEVI void gstore16(u32x4 v, u32 voff, const void* sbase) {
  asm volatile("global_store_dwordx4 %0, %1, %2 sc1\n\ts_nop 1" ::"v"(voff), "v"(v), "s"(sbase) : "memory");
}
DEVI void barrier() {
  MEMFENCE();
  __builtin_amdgcn_s_barrier();
  MEMFENCE();
}
DEVI void lds_barrier() {
  asm volatile("s_waitcnt lgkmcnt(0)" ::: "memory");
  __builtin_amdgcn_s_barrier();
  MEMFENCE();
}
DEVI bf16x4 tr_read(const char* p) { return __builtin_amdgcn_ds_read_tr16_b64_v4bf16((lds_bf16x4_p)(p)); }
DEVI bf16x8 tr_frag(const char* p0, const char* p1) {
  return __builtin_shufflevector(tr_read(p0), tr_read(p1), 0, 1, 2, 3, 4, 5, 6, 7);
}
DEVI bf16x8 tr_frag_at(u32 a0, u32 a1) {
  return __builtin_shufflevector(__builtin_amdgcn_ds_read_tr16_b64_v4bf16((lds_bf16x4_p)(uintptr_t)a0),
                                 __builtin_amdgcn_ds_read_tr16_b64_v4bf16((lds_bf16x4_p)(uintptr_t)a1), 0, 1, 2, 3, 4, 5, 6, 7);
}
DEVI bf16x8 as_frag(u32x4 v) { return __builtin_bit_cast(bf16x8, v); }
#define MFMA32(a, b, c) __builtin_amdgcn_mfma_f32_32x32x16_bf16(a, b, c, 0, 0, 0)
#define MFMA16(a, b, c) __builtin_amdgcn_mfma_f32_16x16x32_bf16(a, b, c, 0, 0, 0)

DEVI void transpose8(const u32x4 (&r)[8], u32x4 (&o)[8]) {
#pragma unroll
  for (int k = 0; k < 8; ++k)
#pragma unroll
    for (int i = 0; i < 4; ++i)
      o[k][i] = (k & 1) ? perm_hi(r[2 * i][k >> 1], r[2 * i + 1][k >> 1]) : perm_lo(r[2 * i][k >> 1], r[2 * i + 1][k >> 1]);
}
DEVI u32 ln_pack(float s, float q) {
  const float mean = s * (1.f / DM);
  const float var = fmaxf(q * (1.f / DM) - mean * mean, 0.f);
  return pack_bf16(mean, __builtin_amdgcn_sqrtf(rb(rb(var) + 1e-5f)));
}

template <int NP>
DEVI void mod_part(const u32x4 (&wv)[8], const u32 (&sl)[NP], rsrc_t r, u32 dst, u32 tag, int lane) {
  float acc[8] = {};
#pragma unroll
  for (int j = 0; j < NP; ++j)
#pragma unroll
    for (int k = 0; k < 4; ++k) {
      acc[2 * k] = dot2(perm_lo(wv[2 * j][k], wv[2 * j + 1][k]), sl[j], acc[2 * k]);
      acc[2 * k + 1] = dot2(perm_hi(wv[2 * j][k], wv[2 * j + 1][k]), sl[j], acc[2 * k + 1]);
    }
#pragma unroll
  for (int i = 0; i < 4; ++i) acc[i] = rs32(acc[i], acc[i + 4]);
#pragma unroll
  for (int i = 0; i < 2; ++i) {
    acc[i] = rs16(acc[i], acc[i + 2]);
    acc[i] += dpp<0x128>(acc[i]);
  }
  st16c(r, only((lane & 8) == 0, dst + 8 * (8 * (lane & 7) + 2 * (lane >> 4))), u32x4{__float_as_uint(acc[0]), tag, __float_as_uint(acc[1]), tag});
}

constexpr int ES0 = ESTART, EL = 21, ES1 = ES0 + EL > 66 ? ES0 + EL : 66, ES2 = ESTART2;
static_assert(ES2 + EL <= 125 && ES2 >= 99 && ES2 >= ES1 + EL && ES0 - 3 < 61 && ES0 + EL - 1 > 56, "activation streams");
constexpr int XL = XLATE;
constexpr int up_xpiece(int s) { return s < 10 && (s & 1) ? 5 + s / 2 : s >= XL && s < XL + 10 ? 10 + (s - XL) : -1; }
constexpr int up_stage(int s) { return s == 33 ? 0 : s == 35 ? 1 : s == 80 ? 2 : s == 82 ? 3 : s >= 108 && (s & 3) == 0 ? 9 + (s - 108) / 4 : -1; }
constexpr bool up_store(int s) { return s == ES0 + EL - 1 || s == ES1 + EL - 1 || s == ES2 + EL - 1; }
constexpr int up_after(int a, int b, int with) {
  int n = with ? (up_xpiece(a) >= 0) + 2 * (up_stage(a) >= 0) : 0;
  for (int s = a + 1; s < b; ++s) n += up_store(s) + (up_xpiece(s) >= 0) + 2 * (up_stage(s) >= 0);
  return n;
}

static_assert(AWIN >= 7 && AWIN <= 14 && LO2 >= 3 && 13 - AWIN < LO2 && 20 - AWIN < LO3 && LO3 - 3 > LO2, "down schedule");
constexpr int dn_iss(int n) {
  const int lo = n < 14 ? -1 : n < 21 ? LO2 : LO3;
  return n - AWIN > lo ? n - AWIN : lo;
}
constexpr int dn_issued(int j) {
  int n = 0;
  while (n < 28 && dn_iss(n) < j) ++n;
  return n;
}

struct Params {
  const u16 *x, *la, *ai, *cr, *ci, *ldt, *dsk, *wg, *wu, *wd, *c, *wm;
  u16* out;
  char* ws;
};

__global__ __launch_bounds__(512, 2) void fused_block(Params P) {
  extern __shared__ __attribute__((aligned(16))) char smem[];
  const int tid = threadIdx.x, lane = tid & 63;
  const int w = __builtin_amdgcn_readfirstlane(tid >> 6);
  const int b = blockIdx.x, xcd = b & 7, bi = b >> 3;
  char* const ws = P.ws;
  const rsrc_t R = __builtin_amdgcn_make_buffer_rsrc(ws, 0, WS_USED, 0x00020000);
  const int mt = 8 * (xcd & 1) + (bi & 7), fb = 8 * (xcd >> 2) + 2 * (bi >> 3) + ((xcd >> 1) & 1);
  const int par = fb & 1;
  const int m2 = 2 * xcd + (bi & 1), nt = bi >> 2, cp2 = (bi >> 1) & 1;
  const int ft = 7 * fb + w;
  const u32 l16 = lane * 16;
  const char* wsrc = ws + OFF_WP + (size_t)ft * 32768;
  u32x4 wf[32];
  u32 code_touch = 0;
  {
    const __attribute__((address_space(1))) char* pc;
    asm volatile("s_getpc_b64 %0" : "=s"(pc));
    if (w >= 6)
      code_touch = *(const __attribute__((address_space(1))) u32*)(pc + min((2 * (bi % 3) + (w & 1)) * 8192 + lane * 128, CODE_BYTES));
  }
  if (tid < 64) *(u32*)(smem + L_SYNC + 4 * tid) = 0;
  u32x4 pl[8];
  u32 ps[8];
  u32x2 cv;
  auto mod_load = [&]() {
    const int ra = 32 * (b & 15) + 4 * (lane >> 3);
    cv = *(const u32x2*)(P.c + ra);
#pragma unroll
    for (int i = 0; i < 4; ++i) pl[i] = *(const u32x4*)(P.wm + (size_t)(ra + i) * 3072 + 1024 * w + 64 * (b >> 4) + 8 * (lane & 7));
  };
  if (w < 4) {
    if (w == 0) mod_load();
    const int d = 64 * xcd + 2 * bi + (w >> 1), n = 64 * (w & 1) + lane;
    ps[0] = P.la[d * NS + n];
    ps[1] = P.ai[d * NS + n];
    ps[2] = P.cr[d * NS + n];
    ps[3] = P.ci[d * NS + n];
    ps[4] = P.cr[d * NS + (n ^ 64)];
    ps[5] = P.ci[d * NS + (n ^ 64)];
    ps[6] = P.ldt[d];
    ps[7] = P.dsk[(d & ~1) + ((lane >> 2) & 1)];
  } else if (w < 6) {
#pragma unroll
    for (int i = 0; i < 8; ++i) pl[i] = *(const u32x4*)(P.x + (size_t)(8 * b + 2 * (lane & 3) + (i & 1)) * DM + 8 * (lane >> 2) + 128 * (i >> 1));
  } else {
    const int un = min(b, 223);
    const u16* src = (((un >> 2) & 1) ? P.wu : P.wg) + (size_t)(64 * (2 * (un & 3) + (w & 1)) + 8 * (lane >> 3)) * DF + 64 * (un >> 3) + 8 * (lane & 7);
    __builtin_amdgcn_s_sleep(WDELAY);
#pragma unroll
    for (int i = 0; i < 8; ++i) pl[i] = *(const u32x4*)(src + i * DF);
  }
  u32 tag;
  asm volatile("s_load_dword %0, %1, 0x0\n\ts_waitcnt lgkmcnt(0)" : "=s"(tag) : "s"(ws + OFF_E) : "memory");
  tag += 0x9E3779B9u;
  barrier();
  auto load_w = [&](auto J) {
    constexpr int j = decltype(J)::value;
    gload16<(j & 3) * 1024>(wf[j], l16, wsrc + (j >> 2) * 4096);
  };

  const u32 lds0 = (u32)(uintptr_t)(lds_char_p)smem;
  const char *ag[2], *fag[2];
  auto down_addr = [&]() {
    int wv = w;
    asm volatile("" : "+s"(wv));
#pragma unroll
    for (int i = 0; i < 2; ++i) {
      ag[i] = ws + OFF_ACT + ((size_t)(4 * m2 + cp2) * NFT + 14 * wv + 7 * (cp2 ^ i)) * 1024;
      fag[i] = ws + OFF_FA + m2 * 4096 + (2 * wv + (cp2 ^ i)) * 64;
    }
  };
  const char* wdg[2] = {(const char*)(P.wd + (size_t)(224 * w + 112 * cp2) * DM + 64 * nt),
                        (const char*)(P.wd + (size_t)(224 * w + 112 * (cp2 ^ 1)) * DM + 64 * nt)};
  const u32 wdo = (lane >> 3) * 1024 + 16 * ((lane & 7) ^ ((lane >> 2) & 4));
  auto slot_lds = [&](int k, int wv) {
    const int sl = k < 9 ? k : k - 9;
    return sl == 4 ? L_SPARE + wv * 2048 : ((sl < 4 ? sl : sl - 1) >> 1) * 32768 + wv * 4096 + ((sl < 4 ? sl : sl - 1) & 1) * 2048;
  };
  auto stage_wd = [&](int k) { glds16p2(wdo, wdo + 7168, wdg[k / 7] + (k % 7) * 16384, lds0 + slot_lds(k, w)); };
  auto stage_all = [&](int k) {
    wait_vm<32>();
#pragma unroll 2
    for (int v = 0; v < 8; ++v) glds16p2(wdo, wdo + 7168, wdg[k / 7] + (k % 7) * 16384 + (v - 7) * (224 * 1024), lds0 + slot_lds(k, v));
  };
  const int q4 = (lane & 15) >> 2, p4 = lane & 3, hh = lane >> 5, blk = (lane >> 4) & 1;
  u32 wfo[2][3];
#pragma unroll
  for (int i = 0; i < 2; ++i) {
    const u32 o = lds0 + (8 * hh + q4) * 128 + 64 * (i ^ (q4 >> 1)) + 32 * blk + 8 * p4;
    wfo[i][0] = o + w * 4096;
    wfo[i][1] = o + w * 4096 + 65536;
    wfo[i][2] = o + w * 2048 + L_SPARE;
  }
  auto wd_frag = [&](int j) {
    const int so = slot_lds(j >> 1, 0), r = so >= L_SPARE ? 2 : so >> 16;
    const u32 a = wfo[j & 1][r] + (r == 2 ? 0 : so & 65535);
    return tr_frag_at(a, a + 512);
  };
  bf16x8 wr[10];
  u32x4 av[28];
  auto load_a = [&](auto J) {
    constexpr int j = decltype(J)::value, g = j / 7, i = j % 7;
    gload16<(i & 3) * 1024>(av[j], l16, ag[g & 1] + (size_t)(g >> 1) * 2 * NFT * 1024 + (i >> 2) * 4096);
  };
  u32 af[4];
  auto load_flag = [&](auto G) { gload4<decltype(G)::value * 1024>(af[decltype(G)::value], 0u, fag[decltype(G)::value & 1]); };
  auto check_flag = [&](auto G, auto YOUNGER) {
    constexpr int g = decltype(G)::value;
    wait_vm_reg<decltype(YOUNGER)::value>(af[g]);
    if (__builtin_expect((u32)__builtin_amdgcn_readfirstlane((int)af[g]) != tag, 0))
      poll_flag(R, OFF_FA + m2 * 4096 + g * 1024 + (2 * w + (cp2 ^ (g & 1))) * 64, tag);
    MEMFENCE();
  };

  auto down_phase = [&](auto) {
    constexpr int FD = 3;
    bf16x8 wd[28];
    auto put_partial = [&](int qd, const f32x16& a) {
#pragma unroll
      for (int g = 0; g < 4; ++g)
        *(f32x4*)(smem + qd * 32768 + w * 4096 + g * 1024 + l16) = f32x4{a[4 * g], a[4 * g + 1], a[4 * g + 2], a[4 * g + 3]};
    };
    const int cl = 8 * w + 4 * hh;
    auto finish = [&](int qj) {
      f32x4 s = {0.f, 0.f, 0.f, 0.f};
#pragma unroll
      for (int v = 0; v < 8; ++v) s += *(const f32x4*)(smem + (2 * (w >> 2) + qj) * 32768 + v * 4096 + (w & 3) * 1024 + l16);
      const f32x4 gb = *(const f32x4*)(smem + L_GB + 4 * cl);
      const u32x2 xr = __builtin_bit_cast(u32x2, tr_read(smem + L_RES + qj * 4096 + (cl + q4) * 64 + 32 * (blk ^ hh) + 8 * p4));
      u32x2 o;
#pragma unroll
      for (int e = 0; e < 2; ++e) o[e] = pack_bf16(bflo(xr[e]) + gb[2 * e] * s[2 * e], bfhi(xr[e]) + gb[2 * e + 1] * s[2 * e + 1]);
      return o;
    };
    f32x16 acc[4] = {};
    lds_barrier();
    st4c(R, only(w == 7 && lane == 0, OFF_FA + mt * 4096 + 2 * 1024 + fb * 64), tag);
    MEMFENCE();
    stage_wd(7);
    stage_wd(8);
    check_flag(std::integral_constant<int, 0>{}, std::integral_constant<int, 6>{});
    static_for<0, 2>(load_a);
    load_flag(std::integral_constant<int, 1>{});
    wait_vm<8>();
    barrier();
    st4c(R, only(w == 7 && lane == 0, OFF_FA + mt * 4096 + 3 * 1024 + fb * 64), tag);
    MEMFENCE();
    static_for<2, 7>(load_a);
    check_flag(std::integral_constant<int, 1>{}, std::integral_constant<int, 6>{});
    static_for<7, dn_issued(0)>(load_a);
#pragma unroll
    for (int j = 0; j < 14; ++j) wd[j] = j < 10 ? wr[j] : wd_frag(j);
    u16* op = P.out + (size_t)(64 * m2 + 32 * cp2 + (lane & 31)) * DM + 64 * nt + cl;
    u32x2 o0;
    static_for<0, 28>([&](auto J) {
      constexpr int j = decltype(J)::value, g = j / 7, k = j % 14;
      wait_vm_reg<dn_issued(j) - 1 - j>(av[j]);
      acc[g >> 1] = MFMA32(wd[2 * k], as_frag(av[j]), acc[g >> 1]);
      acc[2 + (g >> 1)] = MFMA32(wd[2 * k + 1], as_frag(av[j]), acc[2 + (g >> 1)]);
      pin(acc[g >> 1]);
      pin(acc[2 + (g >> 1)]);
      if constexpr (j == dn_iss(14)) check_flag(std::integral_constant<int, 2>{}, std::integral_constant<int, dn_issued(j) - dn_issued(j - FD)>{});
      if constexpr (j == dn_iss(21)) check_flag(std::integral_constant<int, 3>{}, std::integral_constant<int, dn_issued(j) - dn_issued(j - FD)>{});
      if constexpr (j + FD == dn_iss(14)) load_flag(std::integral_constant<int, 2>{});
      if constexpr (j + FD == dn_iss(21)) load_flag(std::integral_constant<int, 3>{});
      static_for<dn_issued(j), dn_issued(j + 1)>(load_a);
      if constexpr (j < 7) {
        wd[14 + 2 * j] = wd_frag(14 + 2 * j);
        wd[15 + 2 * j] = wd_frag(15 + 2 * j);
      }
      if constexpr (j == 15) {
        put_partial(0, acc[0]);
        put_partial(2, acc[2]);
      }
      if constexpr (j == 21) {
        lds_barrier();
        o0 = finish(0);
        pin(o0);
      }
    });
    put_partial(1, acc[1]);
    put_partial(3, acc[3]);
    *(u32x2*)op = o0;
    lds_barrier();
    *(u32x2*)(op + 1024 * DM) = finish(1);
  };

  auto x1_piece = [&](int chunk) {
    const int c0 = 64 * xcd + 2 * bi, c = (lane >> 2) & 1, tile = 8 * chunk + (lane >> 3);
    const float ga = *(const float*)(smem + L_EP + 4 * c), cb1 = *(const float*)(smem + L_EP + 8 + 4 * c), dk = *(const float*)(smem + L_EP + 16 + 4 * c);
    const int l0 = 32 * tile + 8 * ((lane & 3) ^ (2 * ((c0 >> 2) & 1)));
    const char* pp = smem + CV_P + c * (16384 + 16);
    const int po = (4 * l0) ^ (16 * (tile & 3));
    const f32x4 y0 = *(const f32x4*)(pp + po) + *(const f32x4*)(pp + 8192 + po);
    const f32x4 y1 = *(const f32x4*)(pp + (po ^ 16)) + *(const f32x4*)(pp + 8192 + (po ^ 16));
    const u32x4 hv = *(const u32x4*)(smem + CV_H + c * CV_HB + 480 + 2 * l0);
    const u32x4 xx = *(const u32x4*)(smem + CV_X + c * 4096 + 2 * l0);
    u32x4 o, os;
#pragma unroll
    for (int k = 0; k < 4; ++k) {
      const u32 yb = k < 2 ? pack_bf16(y0[2 * k], y0[2 * k + 1]) : pack_bf16(y1[2 * k - 4], y1[2 * k - 3]);
      const f32x2 zf = __builtin_elementwise_fma(unpack2(hv[k]), f32x2{dk, dk}, unpack2(yb));
      const f32x2 gz = unpack2(pack_bf16(zf.x, zf.y)) * ga;
      const f32x2 of = unpack2(xx[k]) + unpack2(pack_bf16(gz.x, gz.y));
      o[k] = pack_bf16(of.x, of.y);
      const f32x2 sf = unpack2(o[k]) * cb1;
      os[k] = pack_bf16(sf.x, sf.y);
    }
    const u32 go = (4 * ((tile & 31) >> 1) + 2 * (tile >> 5) + (tile & 1)) * 32768 + c0 * 64 + (lane & 7) * 16;
    st16c(R, OFF_XS + go, os);
    st16c(R, OFF_X1 + go, o);
    MEMFENCE();
  };
  auto x1_arrive = [&](int hf) {
    const bool last = lds_arrive(smem, hf ? 14 : 4) == 3;
    st4c(R, only(last && lane < 8, OFF_FX + hf * 32768 + 4096 * lane + 16 * b), tag);
    MEMFENCE();
  };

  if (w < 4) {
    const int ch = w >> 1, half = w & 1;
    const int c0 = 64 * xcd + 2 * bi;
    const int n = 64 * half + lane;
    auto mod_job = [&]() {
      pin(cv);
      const u32 sl[2] = {silu2(cv[0]), silu2(cv[1])};
#pragma unroll
      for (int i = 0; i < 4; ++i) pin(pl[i]);
      mod_part<2>(pl, sl, R, OFF_GM + (b & 15) * 24576 + 8192 * w + 512 * (b >> 4), tag, lane);
    };
    if (w == 0) {
      __builtin_amdgcn_s_waitcnt(0x0F70);
      mod_job();
    }
    int kidx[9];
#pragma unroll
    for (int e = 0; e < 5; ++e) {
      const int l0 = 1023 - 8 * n - 2 * e, l1 = l0 - 1;
      const u32 pb = pack_bf16((float)l0, (float)l1);
      kidx[2 * e] = L_KC + 4 * (w * 656 + ((l0 < 128) ? max(l0, 0) : (int)(pb & 0xffffu) - 133 * 128));
      if (e < 4) kidx[2 * e + 1] = L_KC + 4 * (w * 656 + ((l1 < 128) ? l1 : (int)(pb >> 16) - 133 * 128));
    }
#pragma unroll
    for (int e = 0; e < 9; ++e) pin(kidx[e]);
    if (lane < 2) *(u32x4*)(smem + CV_K + w * CV_KCOPY + 4096 + lane * 16) = u32x4{0, 0, 0, 0};
    if (w != 0) __builtin_amdgcn_s_waitcnt(0x0F70);
#pragma unroll
    for (int i = 0; i < 8; ++i) pin(ps[i]);
    MEMFENCE();
    if (w == 1 || w == 2) mod_load();
    const float v_cr = bflo(ps[2]), v_ci = bflo(ps[3]), o_cr = bflo(ps[4]), o_ci = bflo(ps[5]);
    const float dt = rb(expf(bflo(ps[6])));
    const float zr = -rb(expf(bflo(ps[0]))) * dt, zi = bflo(ps[1]) * dt;
    const float mx = wave_max(fmaxf(fmaxf(fabsf(v_cr), fabsf(v_ci)), fmaxf(fabsf(o_cr), fabsf(o_ci))));
    const int ex = min(max((int)(__float_as_uint(mx) >> 23), 1), 252);
    const float sdn = __uint_as_float((u32)(254 - ex) << 23);
    const float sup = __uint_as_float((u32)(ex + 1) << 23) * dt;
    {
      char* base = smem + w * KG_WAVEB + lane * 4;
      auto put = [&](int row, f32x2 v) { *(u32*)(base + row * KG_ROWB) = pack_f16(v); };
      const f32x2 e8 = cexpk(zr, zi, 8.f), e16 = cexpk(zr, zi, 16.f);
      const f32x2 e1 = cexpk(zr, -zi, 1.f), e2 = cexpk(zr, -zi, 2.f);
      const f32x2 e128 = cexpk(zr, -zi, 128.f), e256 = cexpk(zr, -zi, 256.f);
      f32x2 f0 = e16, f1 = e8;
      put(41, f32x2{1.f, 0.f});
      put(42, e8);
#pragma unroll
      for (int j = 2; j < 16; j += 2) {
        f1 = cmul(f1, e16);
        put(41 + j, f0);
        put(42 + j, f1);
        if (j < 14) f0 = cmul(f0, e16);
      }
      const f32x2 e4 = cmul(e2, e2);
      f32x2 g[2];
      g[0] = f32x2{v_cr * sdn, -v_ci * sdn};
      g[1] = cmul(g[0], e128);
#pragma unroll
      for (int i = 0; i < 17; ++i) {
        const f32x2 gi = g[i & 1];
        if (i < 2) {
          f32x2 a = gi, c = cmul(gi, e1);
#pragma unroll
          for (int e = 0; e < 8; e += 2) {
            put(8 * i + e, a);
            put(8 * i + e + 1, c);
            if (e < 6) { a = cmul(a, e2); c = cmul(c, e2); }
          }
        } else if (i < 4) {
          const f32x2 c = cmul(gi, e2);
          put(16 + 4 * (i - 2), gi);
          put(17 + 4 * (i - 2), c);
          put(18 + 4 * (i - 2), cmul(gi, e4));
          put(19 + 4 * (i - 2), cmul(c, e4));
        } else if (i < 8) {
          put(24 + 2 * (i - 4), gi);
          put(25 + 2 * (i - 4), cmul(gi, e4));
        } else {
          put(32 + (i - 8), gi);
        }
        if (i + 2 < 17) g[i & 1] = cmul(gi, e256);
      }
    }
    lds_set(smem, 0, w);
    if (w == 1 || w == 2) {
      __builtin_amdgcn_s_waitcnt(0x0F70);
      mod_job();
    }

    const int r = lane & 15, q = lane >> 4;
    {
      f32x4 acc[3] = {};
#pragma unroll
      for (int hf = 0; hf < 2; ++hf) {
        if (hf == 0) {
          if (ch) lds_wait<12>(smem, 0);
          else lds_wait<3>(smem, 0);
        }
        const char* rbase = smem + (2 * ch + hf) * KG_WAVEB + r * KG_ROWB + q * 16;
        f16x8 fr[4][4];
#pragma unroll
        for (int ks = 0; ks < 4; ++ks) {
          fr[ks][3] = *(const f16x8*)(rbase + ks * 64 + 41 * KG_ROWB);
#pragma unroll
          for (int t = 0; t < 3; ++t) fr[ks][t] = *(const f16x8*)(rbase + ks * 64 + 16 * t * KG_ROWB);
        }
        __builtin_amdgcn_sched_barrier(0);
#pragma unroll
        for (int ks = 0; ks < 4; ++ks)
#pragma unroll
          for (int t = 0; t < 3; ++t) acc[t] = __builtin_amdgcn_mfma_f32_16x16x32_f16(fr[ks][t], fr[ks][3], acc[t], 0, 0, 0);
      }
      pin(acc[0]); pin(acc[1]); pin(acc[2]);
      float* kc = (float*)(smem + L_KC) + w * 656;
      *(f32x4*)(kc + 128 * (q >> 1) + 4 * (q & 1) + 8 * r) = acc[0] * sup;
      if (q < 2) {
        *(f32x4*)(kc + 256 + 64 * q + 4 * r) = acc[1] * sup;
      } else {
        *(f32x2*)(kc + 384 + 64 * (q - 2) + 2 * r) = f32x2{acc[1][0], acc[1][1]} * sup;
        *(f32x2*)(kc + 416 + 64 * (q - 2) + 2 * r) = f32x2{acc[1][2], acc[1][3]} * sup;
      }
#pragma unroll
      for (int e = 0; e < 4; ++e)
        if (4 * q + e <= 8) kc[512 + 16 * (4 * q + e) + r] = acc[2][e] * sup;
      MEMFENCE();
      float kv[9];
#pragma unroll
      for (int e = 0; e < 9; ++e) kv[e] = *(const float*)(smem + kidx[e]);
      const float ka = kc[639 - n], kb = kc[640 - n];
      if (n == 127) kv[8] = 0.f;
      const float kt = (n & 1) ? ka : kb;
      const u32 aa = pack_bf16(ka, ka), bb = pack_bf16(kb, kb);
      *(u32x4*)(smem + CV_K + (2 * ch) * CV_KCOPY + 16 * n) = u32x4{bb, pack_bf16(kb, kt), aa, aa};
      *(u32x4*)(smem + CV_K + (2 * ch + 1) * CV_KCOPY + 16 * n) = u32x4{bb, pack_bf16(kt, ka), aa, aa};
      u32x4 o, s;
#pragma unroll
      for (int e = 0; e < 4; ++e) {
        o[e] = pack_bf16(kv[2 * e], kv[2 * e + 1]);
        s[e] = pack_bf16(kv[2 * e + 1], kv[2 * e + 2]);
      }
      *(u32x4*)(smem + CV_K + (2 * ch) * CV_KCOPY + 2048 + 16 * n) = o;
      *(u32x4*)(smem + CV_K + (2 * ch + 1) * CV_KCOPY + 2048 + 16 * n) = s;
    }
    lds_set(smem, 8, w);
    lds_wait2<15, 15>(smem, 8, 2);
    const char* abase = smem + CV_K + (2 * ch + ((r & 1) ^ 1)) * CV_KCOPY + 2 * (2030 - (r & ~1) + 8 * q) - 64 * (4 * half + 3 + 56);
    const char* bbase = smem + CV_H + ch * CV_HB + 2 * (224 + 16 * r + 8 * q) - 64 * (4 * half + 3);
    f32x4 acc[8] = {};
    bf16x8 af[2][8], bfr[2][8];
    u32 ab[4];
#pragma unroll
    for (int i = 0; i < 4; ++i) {
      ab[i] = (u32)(uintptr_t)(lds_char_p)abase + 1024 * i;
      pin(ab[i]);
    }
    auto ldfr = [&](int rho, int i) {
      const lds_u32_p p = (lds_u32_p)(uintptr_t)(ab[(7 - i) >> 1] + 64 * (3 - rho) + 512 * ((7 - i) & 1));
      af[rho & 1][i] = as_frag(u32x4{p[0], p[1], p[2], p[3]});
      bfr[rho & 1][i] = *(const bf16x8*)(bbase + 64 * (3 - rho) + 512 * i);
    };
    auto put_p = [&](int t0) {
#pragma unroll
      for (int t = t0; t < t0 + 4; ++t) *(f32x4*)(smem + CV_P + w * 8192 + ch * 16 + 1024 * t + 64 * r + 16 * (q ^ ((r >> 1) & 3))) = acc[t];
    };
    if (tid < 8 && (tid & 3) == 0) *(float*)(smem + L_EP + 16 + tid) = bflo(ps[7]);
#pragma unroll
    for (int i = 0; i < 4; ++i) ldfr(0, i);
    static_for<0, 48>([&](auto S) {
      constexpr int st = decltype(S)::value, nx = st + 4;
      constexpr bool pa = st < 16;
      constexpr int rho = pa ? st >> 2 : (st - 16) >> 3, m = pa ? st & 3 : (st - 16) & 7;
      if constexpr (nx < 16) ldfr(nx >> 2, nx & 3);
      else if constexpr (nx < 48) ldfr((nx - 16) >> 3, (nx - 16) & 7);
#pragma unroll
      for (int o = 0; o <= m; ++o) {
        if (pa ? m + o < 4 : (m + o >= 4 && m + o < 8)) {
          acc[m + o] = MFMA16(af[rho & 1][m], bfr[rho & 1][o], acc[m + o]);
          if (o != m) acc[m + o] = MFMA16(af[rho & 1][o], bfr[rho & 1][m], acc[m + o]);
        }
      }
#pragma unroll
      for (int i = 0; i < 3; ++i) {
        __builtin_amdgcn_sched_group_barrier(0x008, 1, 0);
        __builtin_amdgcn_sched_group_barrier(0x100, 1, 0);
      }
      __builtin_amdgcn_sched_barrier(0);
      if constexpr (st == 15) {
        put_p(0);
        lds_set(smem, 13, w);
      }
      if constexpr (st == EPA) {
#pragma unroll
        for (int t = 4; t < 8; ++t) pin(acc[t]);
        lds_wait2<15, 14>(smem, 13, 10);
        x1_piece(w);
      }
      if constexpr (st == ARR) {
#pragma unroll
        for (int t = 4; t < 8; ++t) pin(acc[t]);
        drain_vm();
        x1_arrive(0);
      }
    });
    put_p(4);
    lds_set(smem, 3, w);
  } else {
    __builtin_amdgcn_s_setprio(SPRIO);
    __builtin_amdgcn_s_waitcnt(0x0F70);
    const int un = min(b, 223), cg = un >> 3, type = (un >> 2) & 1, k8 = 2 * (un & 3) + (w & 1);
    const int kg = lane >> 3, cp = lane & 7;
    if (w < 6) {
      if (w == 4) {
#pragma unroll
        for (int a = 0; a < 4; ++a) {
          pin(pl[2 * a]);
          pin(pl[2 * a + 1]);
#pragma unroll
          for (int k = 0; k < 4; ++k)
            st16c(R, OFF_XG + b * 16384 + (a * 4 + k) * 1024 + l16, u32x4{pl[2 * a][k], tag, pl[2 * a + 1][k], tag});
        }
        drain_vm();
        lds_set(smem, 5, 1);
      } else {
#pragma unroll
        for (int i = 0; i < 8; ++i) pin(pl[i]);
        float s[2][2] = {}, qq[2][2] = {};
#pragma unroll
        for (int i = 0; i < 8; ++i)
#pragma unroll
          for (int k = 0; k < 4; ++k) {
            s[i & 1][k & 1] = dot2(pl[i][k], ONES2, s[i & 1][k & 1]);
            qq[i & 1][k & 1] = dot2(pl[i][k], pl[i][k], qq[i & 1][k & 1]);
          }
        float v = rs16(rs32(s[0][0] + s[0][1], qq[0][0] + qq[0][1]), rs32(s[1][0] + s[1][1], qq[1][0] + qq[1][1]));
        v += dpp<0x124>(v);
        v += dpp<0x128>(v);
        auto sw = __builtin_amdgcn_permlane32_swap(__float_as_uint(v), __float_as_uint(v), false, false);
        const u32 pk = ln_pack(__uint_as_float(sw[0]), __uint_as_float(sw[1]));
        st8c(R, only(lane < 32 && (lane & 15) < 4, OFF_GST + 64 * b + 16 * (lane & 3) + 8 * (lane >> 4)), u32x2{pk, tag});
      }
      const int t = tid - 256, c0 = 64 * xcd + 2 * bi, pr = c0 >> 1;
      const u32 xo = OFF_XG + (t >> 2) * 16384 + (((pr >> 6) * 4 + (pr & 3)) * 16 + ((pr >> 2) & 15)) * 64 + (t & 3) * 16;
      const u32 mo = OFF_GM + (lane >> 2) * 24576 + 8 * (((lane >> 1) & 1) * DM + c0 + (lane & 1));
      if (lane < 30) *(u32x4*)(smem + CV_H + (w - 4) * CV_HB + lane * 16) = u32x4{0, 0, 0, 0};
      u32x4 gx[8], gs[8];
      u32x2 gm;
      u32 bad = 1;
      lds_wait<2>(smem, 5);
      __builtin_amdgcn_s_sleep(GDELAY);
#pragma unroll 1
      for (int spin = 0; spin < SPIN_CAP; ++spin) {
        if (bad) {
          gm = ld8c(R, mo);
#pragma unroll
          for (int i = 0; i < 8; ++i) {
            gx[i] = ld16c(R, xo + i * (32 * 16384));
            gs[i] = ld16c(R, OFF_GST + 16 * t + 2048 * i);
          }
          bad = gm[1] ^ tag;
#pragma unroll
          for (int i = 0; i < 8; ++i) bad |= (gx[i][1] ^ tag) | (gx[i][3] ^ tag) | (gs[i][1] ^ tag) | (gs[i][3] ^ tag);
        }
        if (__all(bad == 0)) break;
        __builtin_amdgcn_s_sleep(8);
        MEMFENCE();
      }
      drain_vm();
      if (w == 5) lds_set(smem, 10, 0);
      const float md = rb(sum_hi4(__uint_as_float(gm[0])));
      const f32x2 sa = {rdlane(md, 0), rdlane(md, 1)}, ca1 = {rb(1.f + rdlane(md, 2)), rb(1.f + rdlane(md, 3))};
#pragma unroll
      for (int i = 0; i < 8; ++i) {
        f32x2 hv[2];
#pragma unroll
        for (int e = 0; e < 2; ++e) {
          const u32 st = gs[i][2 * e], xv = gx[i][2 * e];
          hv[e] = __builtin_elementwise_fma((f32x2{bflo(xv), bfhi(xv)} - bflo(st)) * __builtin_amdgcn_rcpf(bfhi(st)), ca1, sa);
        }
        *(u32*)(smem + CV_H + 480 + 512 * i + 4 * t) = pack_bf16(hv[0].x, hv[1].x);
        *(u32*)(smem + CV_H + CV_HB + 480 + 512 * i + 4 * t) = pack_bf16(hv[0].y, hv[1].y);
      }
      lds_set(smem, 2, w - 4);
#pragma unroll
      for (int i = 0; i < 8; ++i) {
        *(u32*)(smem + CV_X + 512 * i + 4 * t) = perm_lo(gx[i][0], gx[i][2]);
        *(u32*)(smem + CV_X + 4096 + 512 * i + 4 * t) = perm_hi(gx[i][0], gx[i][2]);
      }
      lds_set(smem, 10, w - 2);
    } else {
      u32x4 t[8];
#pragma unroll
      for (int i = 0; i < 8; ++i) pin(pl[i]);
      transpose8(pl, t);
      pin(code_touch);
      {
        char* tb = smem + L_TR + (w & 1) * 8192;
#pragma unroll
        for (int n = 0; n < 8; ++n)
          *(u32x4*)(tb + ((cp >> 1) * 8 + kg) * 256 + (((n & 3) + 8 * (n >> 2) + 4 * (cp & 1)) ^ (cp >> 1)) * 16) = t[n];
        MEMFENCE();
        if (b < 224) {
#pragma unroll
          for (int k = 0; k < 8; ++k) {
            const int row = 4 * k + (lane >> 4), fl = k >> 1;
            const u32x4 o = *(const u32x4*)(tb + row * 256 + ((lane & 15) ^ fl) * 16);
            st16c(R, OFF_WP + (((4 * cg + fl) * 32 + 4 * k8) * 64 + (row & 7) * 32 + 16 * type + (lane & 15)) * 16, o);
          }
        }
      }
      asm volatile("s_waitcnt lgkmcnt(0)" ::: "memory");
      lds_set(smem, 2, w - 4);
      drain_vm();
      st4c(R, only(lane == 0 && b < 224, OFF_FW + (cg * 16 + type * 8 + k8) * 64), tag);
      lds_wait<1>(smem, 10);
      if (w == 6) {
#pragma unroll 1
        for (int spin = 0; spin < SPIN_CAP; ++spin) {
          const u32 f = ld4c(R, OFF_FW + min(((7 * fb) >> 2) * 16 + lane, ((7 * fb + 6) >> 2) * 16 + 15) * 64);
          if (__all(f == tag)) break;
          __builtin_amdgcn_s_sleep(10);
          MEMFENCE();
        }
        drain_vm();
        lds_set(smem, 5, 0);
      } else {
        {
          const int c0 = 64 * xcd + 2 * bi;
          float v = gather_sum<2>(R, OFF_GM + (lane >> 3) * 49152 + 8 * ((2 + ((lane >> 1) & 3)) * DM + c0 + (lane & 1)), 24576, tag);
          v += dpp<0x128>(v);
          v = rs16(v, v);
          v = rb(rs32(v, v));
          if ((lane & 6) == 4) v = rb(1.f + v);
          if (lane < 8 && (lane & 2) == 0) *(float*)(smem + L_EP + 2 * (lane & 4) + 4 * (lane & 1)) = v;
          st8c(R, only(lane >= 2 && lane < 8, OFF_FM + ((lane >> 1) == 2 ? 0 : (lane >> 2) + 1) * 4096 + 8 * (c0 + (lane & 1))),
               u32x2{__float_as_uint(v), tag});
          lds_set(smem, 10, 1);
        }
#pragma unroll
        for (int i = 0; i < 7; ++i) t[i] = *(const u32x4*)(P.wd + (size_t)(7 * b + i) * DM + 8 * lane);
#pragma unroll
        for (int i = 0; i < 7; ++i) pin(t[i]);
        __builtin_amdgcn_s_sleep(VDELAY);
#pragma unroll 1
        for (int spin = 0; spin < SPIN_CAP; ++spin) {
          u32 bad = 0;
#pragma unroll
          for (int i = 0; i < 8; ++i) t[i] = ld16c(R, OFF_FM + i * 1024 + l16);
#pragma unroll
          for (int i = 0; i < 8; ++i) bad |= (t[i][1] ^ tag) | (t[i][3] ^ tag);
          if (__all(bad == 0)) break;
          __builtin_amdgcn_s_sleep(8);
          MEMFENCE();
        }
#pragma unroll
        for (int i = 0; i < 8; ++i) *(u32*)(smem + L_V + 256 * i + 32 * (i >> 2) + 4 * lane) = perm_hi(t[i][0], t[i][2]);
        *(u32x4*)(smem + L_V + 2112 + l16) = u32x4{0, 0, 0, 0};
        lds_set(smem, 5, 2);
      }
    }
    __builtin_amdgcn_s_setprio(0);
    if (w == 7) {
    const char* x1g = ws + OFF_X1 + (size_t)(4 * mt) * 32768;
    auto dma = [&](const char* src, int dst, int n) {
#pragma unroll 4
      for (int i = 0; i < n; ++i) glds16(l16, src + i * 1024, lds0 + dst + i * 1024);
    };
    const u32 fao = OFF_FA + mt * 4096;
    __builtin_amdgcn_s_setprio(3);
    auto x1_flags = [&](int hf) {
      u32 f[4];
#pragma unroll
      for (int i = 0; i < 4; ++i) f[i] = ld4c(R, OFF_FX + hf * 32768 + 4096 * xcd + (lane + 64 * i) * 16);
      return __all(((f[0] ^ tag) | (f[1] ^ tag) | (f[2] ^ tag) | (f[3] ^ tag)) == 0);
    };
    auto x1_poll = [&](int hf) {
#pragma unroll 1
      for (int spin = 0; spin < SPIN_CAP && !x1_flags(hf); ++spin) {
        __builtin_amdgcn_s_sleep(2);
        MEMFENCE();
      }
      drain_vm();
    };
    auto ln2_partial = [&](int hf, int raw) {
      dma(x1g + (2 * hf + (fb >> 3)) * 32768 + (fb & 7) * 4096, raw, 4);
      wait_vm<0>();
      const int g = lane >> 4;
      const char* sb = smem + raw + (8 * (g >> 1) + 4 * (g & 1) + q4) * 64 + 8 * p4;
      const bf16x8 ones = as_frag(u32x4{ONES2, ONES2, ONES2, ONES2});
      bf16x8 f[2][2];
#pragma unroll
      for (int k2 = 0; k2 < 2; ++k2)
#pragma unroll
        for (int c = 0; c < 2; ++c) {
          const char* p = sb + 32 * (c ^ (g & 1)) + k2 * 2048;
          f[c][k2] = tr_frag(p, p + 1024);
        }
#pragma unroll
      for (int c = 0; c < 2; ++c) {
        f32x4 aq = {}, as = {};
#pragma unroll
        for (int k2 = 0; k2 < 2; ++k2) {
          aq = MFMA16(f[c][k2], f[c][k2], aq);
          as = MFMA16(ones, f[c][k2], as);
        }
        const float sq = p4 == 0 ? aq[0] : p4 == 1 ? aq[1] : p4 == 2 ? aq[2] : aq[3];
        st16c(R, only(g == q4, OFF_GS2 + (((mt * 2 + hf) * 8 + (fb & 7)) * 64 + 32 * (fb >> 3) + 16 * c + (lane & 15)) * 16),
              u32x4{__float_as_uint(as[0]), tag, __float_as_uint(sq), tag});
      }
      MEMFENCE();
    };
    auto ln2_gather = [&](int hf) {
#pragma unroll 1
      for (int spin = 0; spin < SPIN_CAP; ++spin) {
        u32x4 g[8];
        u32 bad = 0;
        float s1 = 0.f, s2 = 0.f;
#pragma unroll
        for (int i = 0; i < 8; ++i) g[i] = ld16c(R, OFF_GS2 + (mt * 2 + hf) * 8192 + i * 1024 + l16);
#pragma unroll
        for (int i = 0; i < 8; ++i) {
          bad |= (g[i][1] ^ tag) | (g[i][3] ^ tag);
          s1 += __uint_as_float(g[i][0]);
          s2 += __uint_as_float(g[i][2]);
        }
        const u32 ms = ln_pack(s1, s2) ^ 0x8000u;
        *(u32x4*)(smem + L_SF + (64 * hf + (lane ^ (32 * par))) * 16) = u32x4{perm_lo(ms, ms), ms, perm_hi(ms, ms), 0};
        const float k0 = -2.f * 0.7978845608028654f * 1.4426950408889634f, k1 = k0 * 0.044715f;
        const float sd = bfhi(ms), rs = __builtin_amdgcn_rcpf(sd);
        *(f32x4*)(smem + L_RS + (64 * hf + (lane ^ (32 * par))) * 16) = f32x4{k0 * rs, k1 * rs * rs * rs, sd * sd, 0.f};
        if (__all(bad == 0)) break;
        __builtin_amdgcn_s_sleep(4);
        MEMFENCE();
      }
      lds_set(smem, 7, hf);
    };
    lds_wait<15>(smem, 3);
    __builtin_amdgcn_s_sleep(ADELAY);
    x1_poll(0);
    lds_wait<15>(smem, 1);
    dma(ws + OFF_XS + (size_t)(4 * mt + par) * 32768, 0, 14);
    lds_set(smem, 6, 1);
    ln2_partial(0, CV_P);
    lds_barrier();
    barrier();
    __builtin_amdgcn_s_sleep(SDELAY);
    ln2_gather(0);
    barrier();
    x1_poll(1);
    lds_set(smem, 6, 2);
    ln2_partial(1, L_SPARE);
    st4c(R, only(b == 0 && lane == 0, OFF_E), tag);
    lds_barrier();
    stage_all(4);
    barrier();
    ln2_gather(1);
#pragma unroll
    for (int k = 0; k < 4; ++k) stage_wd(k);
    wait_vm<8>();
    lds_barrier();
    st4c(R, only(lane == 0, fao + fb * 64), tag);
    stage_all(5);
    stage_all(6);
    dma(ws + OFF_X1 + (size_t)(4 * m2 + cp2) * 32768 + nt * 4096, L_RES, 4);
    dma(ws + OFF_X1 + (size_t)(4 * m2 + cp2 + 2) * 32768 + nt * 4096, L_RES + 4096, 4);
    wait_vm<40>();
#pragma unroll
    for (int j = 0; j < 10; ++j) wr[j] = wd_frag(j);
#pragma unroll
    for (int j = 0; j < 10; ++j) pin(wr[j]);
    lds_wait2<15, 7>(smem, 11, 12);
    st4c(R, only(lane == 0, fao + 1024 + fb * 64), tag);
    MEMFENCE();
#pragma unroll
    for (int k = 9; k < 14; ++k) stage_wd(k);
    down_addr();
    load_flag(std::integral_constant<int, 0>{});
    *(float*)(smem + L_GB + 4 * lane) = gather_sum<1>(R, OFF_FM + 2 * 4096 + 8 * (64 * nt + lane), 0, tag);
    asm volatile("s_waitcnt vmcnt(0) lgkmcnt(0)" ::: "memory");
    __builtin_amdgcn_s_setprio(0);
    down_phase(std::integral_constant<int, 1>{});
  }
  }

  if (w < 7) {
    lds_wait<1>(smem, 5);
    if (w >= 4) {
      lds_wait<3>(smem, 2);
      static_for<0, NPARK>(load_w);
    }
    static_for<NPARK, 32>(load_w);
    if (w >= 4) {
      lds_wait<15>(smem, 8);
#pragma unroll 4
      for (int i = 0; i < 4 * NPARK / 3; ++i) {
        const int f = 3 * i + (w - 4), tw = f & 3, k = f >> 2;
        glds16(l16, ws + OFF_WP + (size_t)(7 * fb + tw) * 32768 + k * 1024, lds0 + L_PARK + (tw * NPARK + k) * 1024);
      }
      drain_vm();
      lds_set(smem, 9, w - 4);
    } else {
      lds_wait<7>(smem, 9);
#pragma unroll
      for (int j = 0; j < NPARK; ++j) wf[j] = *(const u32x4*)(smem + L_PARK + (w * NPARK + j) * 1024 + l16);
      asm volatile("s_waitcnt lgkmcnt(0)" ::: "memory");
      lds_set(smem, 1, w);
    }
    if (w >= 3) {
      lds_wait2<15, 14>(smem, 3, 10);
      x1_piece(w == 3 ? 7 : w);
    }
    auto load_x = [&](int i) {
      const int pc = (i < 10 ? 0 : 64) + min(7 * (i % 10) + w, 63);
      glds16(l16, ws + OFF_XS + (size_t)(4 * mt) * 32768 + ((pc ^ (32 * par)) << 10), lds0 + pc * 1024);
    };
    auto first_x = [&]() {
#pragma unroll
      for (int i = 2; i < 5; ++i) load_x(i);
    };
    wait_vm<2>();
    if (w < 3) drain_vm();
#pragma unroll
    for (int j = 0; j < 32; ++j) pin(wf[j]);
    const bool early = __builtin_amdgcn_readfirstlane((int)lds_peek(smem, 6, 1));
    MEMFENCE();
    if (early) first_x();
    u32x4 aux;
    f32x4 d = {};
    {
      lds_wait<4>(smem, 5);
      const char* vb = smem + L_V + (((lane ^ (lane >> 4)) & 1) ? 2112 : (lane & 2) * 528) + 16 * hh;
      bf16x8 va[32];
#pragma unroll
      for (int j = 0; j < 12; ++j) va[j] = *(const bf16x8*)(vb + 32 * j);
      static_for<0, 32>([&](auto J) {
        constexpr int j = decltype(J)::value;
        if constexpr (j + 12 < 32) va[j + 12] = *(const bf16x8*)(vb + 32 * (j + 12));
        d = MFMA16(va[j], as_frag(wf[j]), d);
        pin(d);
        __builtin_amdgcn_sched_barrier(0);
      });
    }
    u32 xb[2][2];
#pragma unroll
    for (int i = 0; i < 2; ++i) {
      xb[i][0] = lds0 + 65536 * i + (8 * hh + q4) * 64 + 32 * blk + 8 * p4;
      xb[i][1] = lds0 + 65536 * i + (8 * hh + 4 + q4) * 64 + 32 * (blk ^ 1) + 8 * p4;
      pin(xb[i][0]);
      pin(xb[i][1]);
    }
    auto xfrag = [&](int s) { return tr_frag_at(xb[s >> 6][0] + (s & 63) * 1024, xb[s >> 6][1] + (s & 63) * 1024); };
    const char* actg[2];
    const char *ag0 = ws + OFF_ACT + ((size_t)(4 * mt + par) * NFT + ft) * 1024, *ag1 = ws + OFF_ACT + ((size_t)(4 * mt + (par ^ 1)) * NFT + ft) * 1024;
    u32 sfo = lds0 + L_SF + (lane & 31) * 16;
    u32 pubo = lds0 + L_SYNC + 16 * 11 + 4 * w;
    if (w >= 3) {
      if (early) wait_vm<3>();
      else wait_vm<0>();
      x1_arrive(1);
    }
    if (!early) {
      lds_wait<2>(smem, 6);
      first_x();
    }
    barrier();
    constexpr int XD = 3;
    bf16x8 xf[4];
#pragma unroll
    for (int s = 0; s < XD; ++s) xf[s] = xfrag(s);
    f32x16 acc[3];
    f32x4 gcs[2];
    float ea[4], em[4], prev = 0.f;
    u32x4 o;
    auto epi = [&](auto T, auto U) {
      constexpr int t = decltype(T)::value, u = decltype(U)::value, n = u >> 1;
      const f32x4& gc = gcs[t & 1];
      const f32x16& a = acc[t % 3];
      if constexpr ((u & 1) == 0) {
        if constexpr (n >= 3 && n < 11) {
          float v = em[(n - 3) & 3] * ea[(n - 3) & 3];
          pin(v);
          if constexpr ((n - 3) & 1) o[(n - 3) >> 1] = pack_bf16(prev, v);
          else prev = v;
          if constexpr (n == 10) gstore16(o, l16, actg[t & 1] + (size_t)(t >> 1) * 2 * NFT * 1024);
        }
        if constexpr (n >= 2 && n < 10) { ea[(n - 2) & 3] = __builtin_fmaf(ea[(n - 2) & 3], gc[2], gc[2]); pin(ea[(n - 2) & 3]); }
        if constexpr (n >= 1 && n < 9) { ea[(n - 1) & 3] *= a[n - 1]; pin(ea[(n - 1) & 3]); }
        if constexpr (n < 8) {
          ea[n & 3] = a[n] * a[n];
          em[n & 3] = a[n] * a[8 + n];
          pin(ea[n & 3]);
          pin(em[n & 3]);
        }
      } else {
        if constexpr (n >= 2 && n < 10) { ea[(n - 2) & 3] = __builtin_amdgcn_rcpf(ea[(n - 2) & 3]); pin(ea[(n - 2) & 3]); }
        if constexpr (n >= 1 && n < 9) { ea[(n - 1) & 3] = __builtin_amdgcn_exp2f(ea[(n - 1) & 3]); pin(ea[(n - 1) & 3]); }
        if constexpr (n < 8) { ea[n & 3] = __builtin_fmaf(ea[n & 3], gc[1], gc[0]); pin(ea[n & 3]); }
      }
    };
    bf16x8 sf;
    u32 peek, peek2;
    auto fold_load = [&](int t) {
      sf = *(const __attribute__((address_space(3))) bf16x8*)(uintptr_t)(sfo + t * 512);
      gcs[t & 1] = *(const __attribute__((address_space(3))) f32x4*)(uintptr_t)(sfo + (L_RS - L_SF) + t * 512);
    };
    auto fold = [&](int t) {
      acc[t % 3] = MFMA32(as_frag(aux), sf, acc[t % 3]);
      pin(acc[t % 3]);
    };
    constexpr int PUB = (ES1 + EL + 12 > 100 ? ES1 + EL + 12 : 100);
    static_for<0, 4>([&](auto T) {
      constexpr int t = decltype(T)::value;
      static_for<0, 32>([&](auto KS) {
        constexpr int ks = decltype(KS)::value, s = 32 * t + ks;
        if constexpr (s == 14 - XD) {
          wait_vm<up_after(-1, s, 0)>();
          barrier();
        }
        if constexpr (s == ES0 - 10) peek = lds_peek(smem, 7, 0);
        if constexpr (s == ES0 - 6) {
          if (__builtin_expect(!__builtin_amdgcn_readfirstlane((int)peek), 0)) lds_wait<1>(smem, 7);
          MEMFENCE();
          fold_load(0);
        }
        if constexpr (s == XL - 5) peek2 = lds_peek(smem, 6, 2);
        if constexpr (s == XL - 1) {
          if (__builtin_expect(!__builtin_amdgcn_readfirstlane((int)peek2), 0)) lds_wait<4>(smem, 6);
          MEMFENCE();
        }
        if constexpr (s == 64) {
#pragma unroll
          for (int i = 0; i < XD; ++i) xf[(s + i) & 3] = xfrag(s + i);
        }
        if constexpr (s == 78 - XD) {
          wait_vm<up_after(XL + 4, s, 0)>();
          barrier();
        }
        if constexpr (s == ES2 - 10) peek = lds_peek(smem, 7, 1);
        if constexpr (s == ES2 - 6) {
          if (__builtin_expect(!__builtin_amdgcn_readfirstlane((int)peek), 0)) lds_wait<2>(smem, 7);
          MEMFENCE();
          fold_load(2);
        }
        if constexpr ((t & 1) && ks == 29) fold_load(t);
        if constexpr (s == PUB) {
          wait_vm<up_after(ES1 + EL - 1, s, 1)>();
          *(__attribute__((address_space(3))) volatile u32*)(uintptr_t)pubo = 1u;
          MEMFENCE();
        }
        if constexpr (s + XD < 128 && !(s < 64 && s + XD >= 64)) xf[(s + XD) & 3] = xfrag(s + XD);
        if constexpr (ks == 0) acc[t % 3] = MFMA32(as_frag(wf[0]), xf[s & 3], f32x16{});
        else acc[t % 3] = MFMA32(as_frag(wf[ks]), xf[s & 3], acc[t % 3]);
        pin(acc[t % 3]);
        if constexpr (s == 20) {
          const float c = blk ? d[1] : d[0], sh = blk ? d[3] : d[2];
          const float c0 = rb(c), c1 = rb(c - c0), b0 = rb(sh), b1 = rb(sh - b0);
          aux = u32x4{pack_bf16(c0, c1), pack_bf16(c - c0 - c1, b0), pack_bf16(b1, sh - b0 - b1), 0};
          if (hh) aux = u32x4{0, 0, 0, 0};
          pin(aux);
        }
        if constexpr (s == 24) {
          pin_s(ag0); pin_s(ag1);
          actg[0] = ag0;
          actg[1] = ag1;
          pin(sfo);
          pin(pubo);
#pragma unroll
          for (int i = 0; i < 6; ++i) pin(wfo[i & 1][i >> 1]);
        }
        if constexpr (s == ES0 - 3) fold(0);
        if constexpr (s == ES2 - 3) fold(2);
        if constexpr ((t & 1) && ks == 31) fold(t);
        if constexpr (s >= ES0 && s < ES0 + EL) epi(std::integral_constant<int, 0>{}, std::integral_constant<int, s - ES0>{});
        if constexpr (s >= ES1 && s < ES1 + EL) epi(std::integral_constant<int, 1>{}, std::integral_constant<int, s - ES1>{});
        if constexpr (s >= ES2 && s < ES2 + EL) epi(std::integral_constant<int, 2>{}, std::integral_constant<int, s - ES2>{});
        if constexpr (s == 102) wait_vm<up_after(82, s, 0)>();
        if constexpr (t == 3 && ks < 10) wr[ks < 4 ? ks : ks < 6 ? ks + 4 : ks - 2] = wd_frag(ks < 4 ? ks : ks < 6 ? ks + 4 : ks - 2);
        if constexpr (t == 3 && ks == 11) {
#pragma unroll
          for (int j = 0; j < 10; ++j) pin(wr[j]);
        }
        if constexpr (up_xpiece(s) >= 0) load_x(up_xpiece(s));
        if constexpr (up_stage(s) >= 0) stage_wd(up_stage(s));
        __builtin_amdgcn_sched_barrier(0);
      });
      if constexpr (t == 0) wait_vm<up_after(9, 32, 0)>();
      if constexpr (t == 1) wait_vm<up_after(XL + 1, 64, 0)>();
      if constexpr (t == 2) wait_vm<up_after(ES0 + EL - 1, 96, 1)>();
      if constexpr (t < 3) lds_barrier();
    });
    down_addr();
    load_flag(std::integral_constant<int, 0>{});
#pragma unroll
    for (int i = 0; i < 8; i += 2) {
      float v[2];
#pragma unroll
      for (int e = 0; e < 2; ++e) {
        const f32x4& gc = gcs[1];
        const float a = acc[0][i + e];
        const float ex = __builtin_amdgcn_exp2f(a * __builtin_fmaf(a * a, gc[1], gc[0]));
        v[e] = a * acc[0][8 + i + e] * __builtin_amdgcn_rcpf(__builtin_fmaf(ex, gc[2], gc[2]));
      }
      o[i >> 1] = pack_bf16(v[0], v[1]);
    }
    gstore16(o, l16, actg[1] + (size_t)2 * NFT * 1024);
    wait_vm<up_after(ES2 + EL - 1, 128, 1) + 2>();
    down_phase(std::integral_constant<int, 0>{});
  }
}

extern "C" void kernel_launch(void* const* d_in, const int* in_sizes, int n_in, void* d_out, int out_size, void* d_ws,
                              size_t ws_size, hipStream_t stream) {
  Params p;
  p.x = (const u16*)d_in[0];
  p.la = (const u16*)d_in[1];
  p.ai = (const u16*)d_in[2];
  p.cr = (const u16*)d_in[3];
  p.ci = (const u16*)d_in[4];
  p.ldt = (const u16*)d_in[5];
  p.dsk = (const u16*)d_in[6];
  p.wg = (const u16*)d_in[8];
  p.wu = (const u16*)d_in[9];
  p.wd = (const u16*)d_in[10];
  p.c = (const u16*)d_in[11];
  p.wm = (const u16*)d_in[12];
  p.out = (u16*)d_out;
  p.ws = (char*)d_ws;
  (void)hipFuncSetAttribute((const void*)fused_block, hipFuncAttributeMaxDynamicSharedMemorySize, SMEM);
  fused_block<<<256, 512, SMEM, stream>>>(p);
}
```
